```python
import torch

def _softmax(x, axis=-1):
    return torch.softmax(x, dim=axis)
def _gelu(x):
    return torch.nn.functional.gelu(x, approximate="tanh")
def _silu(x):
    return torch.nn.functional.silu(x)
def _sigmoid(x):
    return torch.sigmoid(x)
def _layernorm(x, axis=-1, eps=1e-5):
    m = x.mean(dim=axis, keepdim=True)
    v = x.var(dim=axis, keepdim=True, unbiased=False)
    return (x - m) / torch.sqrt(v + eps)
def _scatter_add(tgt, idx, src, axis=0):
    return tgt.clone().index_add_(axis, idx, src)
def _segment_sum(src, seg, n):
    out = torch.zeros((n,) + src.shape[1:], dtype=src.dtype)
    return out.index_add_(0, seg, src)
def _pad(x, pw):
    flat = tuple(v for pair in reversed(pw) for v in pair)
    return torch.nn.functional.pad(x, flat)

kernel_name = 'standalone__modern_conv_block_slak__vision'

def setup_inputs():

    _w = (lambda a: (lambda t: t.to(torch.bfloat16) if t.is_floating_point() else t)(torch.as_tensor(a)))
    H = W = 224
    C = 96
    K = 7
    return {
        "x": _w((lambda *s: torch.randn(*s, dtype=torch.bfloat16) * 0.05)(1, H, W, C)),
        "dw": _w((lambda *s: torch.randn(*s, dtype=torch.bfloat16) * 0.05)(K, K, C)),
        "w1": _w((lambda *s: torch.randn(*s, dtype=torch.bfloat16) * 0.05)(C, 4 * C)),
        "w2": _w((lambda *s: torch.randn(*s, dtype=torch.bfloat16) * 0.05)(4 * C, C)),
        "grn_g": _w((lambda *s: torch.randn(*s, dtype=torch.bfloat16) * 0.05)(4 * C)),
        "grn_b": _w((lambda *s: torch.randn(*s, dtype=torch.bfloat16) * 0.05)(4 * C)),
    }

def reference(x, dw, w1, w2, grn_g, grn_b):
    B, H, W, C = x.shape
    K = dw.shape[0]
    pad = K // 2
    xp = _pad(x, ((0,0),(pad,pad),(pad,pad),(0,0)))
    out = torch.zeros_like(x)
    for i in range(K):
        for j in range(K):
            out = out + xp[:, i:i+H, j:j+W, :] * dw[i, j][None, None, None, :]
    h = _layernorm(out, axis=-1)
    h = _gelu(h @ w1)
    gx = torch.sqrt((h * h).sum(axis=(1, 2), keepdims=True) + 1e-6)
    nx = gx / (gx.mean(axis=-1, keepdims=True) + 1e-6)
    h = grn_g[None, None, None, :] * (h * nx) + grn_b[None, None, None, :] + h
    return x + h @ w2
```

```cpp
#include <hip/hip_runtime.h>
#include <hip/hip_bf16.h>
#include <stdint.h>

#define HH 224
#define WW 224
#define CC 96
#define HID 384
#define NPX (HH*WW)

__device__ __forceinline__ float bf2f(unsigned short u) { return __uint_as_float(((unsigned)u) << 16); }
__device__ __forceinline__ unsigned short f2bf(float f) { unsigned u = __float_as_uint(f); u += 0x7fffu + ((u >> 16) & 1u); return (unsigned short)(u >> 16); }

__global__ void k_zero(float* sums) { sums[threadIdx.x] = 0.f; }

__global__ void __launch_bounds__(384) k1(const unsigned short* x, const unsigned short* dw, const unsigned short* w1,
                                          unsigned short* h, float* sums) {
  __shared__ float v[4][CC];
  __shared__ float y[4][CC];
  int t = threadIdx.x; int pl = t / CC, c = t % CC;
  float ssq = 0.f;
  for (int it = 0; it < 49; it++) {
    int p0 = (blockIdx.x * 49 + it) * 4;
    int p = p0 + pl; int py = p / WW, px = p % WW;
    float acc = 0.f;
    for (int i = 0; i < 7; i++) for (int j = 0; j < 7; j++) {
      int yy = py + i - 3, xx = px + j - 3;
      if (yy >= 0 && yy < HH && xx >= 0 && xx < WW) acc += bf2f(x[(yy * WW + xx) * CC + c]) * bf2f(dw[(i * 7 + j) * CC + c]);
    }
    __syncthreads();
    v[pl][c] = bf2f(f2bf(acc));
    __syncthreads();
    float m = 0.f; for (int k = 0; k < CC; k++) m += v[pl][k]; m /= CC;
    float var = 0.f; for (int k = 0; k < CC; k++) { float d = v[pl][k] - m; var += d * d; } var /= CC;
    y[pl][c] = bf2f(f2bf((v[pl][c] - m) * rsqrtf(var + 1e-5f)));
    __syncthreads();
    for (int q = 0; q < 4; q++) {
      float a = 0.f;
      for (int k = 0; k < CC; k++) a += y[q][k] * bf2f(w1[k * HID + t]);
      float u = 0.7978845608f * (a + 0.044715f * a * a * a);
      float g = a / (1.f + __expf(-2.f * u));
      unsigned short gb = f2bf(g);
      h[(size_t)(p0 + q) * HID + t] = gb;
      ssq += g * g;
    }
  }
  atomicAdd(&sums[t], ssq);
}

__global__ void __launch_bounds__(384) k2(const unsigned short* x, const unsigned short* w2, const unsigned short* g, const unsigned short* b,
                                          const unsigned short* h, const float* sums, unsigned short* out) {
  __shared__ float s[HID];
  __shared__ float hh[4][HID];
  __shared__ float red[HID];
  int t = threadIdx.x;
  float gx = sqrtf(sums[t] + 1e-6f);
  red[t] = gx;
  __syncthreads();
  float m = 0.f; for (int k = 0; k < HID; k++) m += red[k]; m /= HID;
  float sc = 1.f + bf2f(g[t]) * gx / (m + 1e-6f);
  float bb = bf2f(b[t]);
  int pl = t / CC, c = t % CC;
  for (int it = 0; it < 49; it++) {
    int p0 = (blockIdx.x * 49 + it) * 4;
    __syncthreads();
    for (int q = 0; q < 4; q++) hh[q][t] = bf2f(h[(size_t)(p0 + q) * HID + t]) * sc + bb;
    __syncthreads();
    float a = 0.f;
    for (int k = 0; k < HID; k++) a += hh[pl][k] * bf2f(w2[k * CC + c]);
    out[(size_t)(p0 + pl) * CC + c] = f2bf(a + bf2f(x[(size_t)(p0 + pl) * CC + c]));
  }
}

extern "C" void kernel_launch(void* const* d_in, const int* in_sizes, int n_in,
                              void* d_out, int out_size, void* d_ws, size_t ws_size,
                              hipStream_t stream) {
  const unsigned short* x = (const unsigned short*)d_in[0];
  const unsigned short* dw = (const unsigned short*)d_in[1];
  const unsigned short* w1 = (const unsigned short*)d_in[2];
  const unsigned short* w2 = (const unsigned short*)d_in[3];
  const unsigned short* g = (const unsigned short*)d_in[4];
  const unsigned short* b = (const unsigned short*)d_in[5];
  float* sums = (float*)d_ws;
  unsigned short* h = (unsigned short*)((char*)d_ws + 4096);
  k_zero<<<1, 384, 0, stream>>>(sums);
  k1<<<256, 384, 0, stream>>>(x, dw, w1, h, sums);
  k2<<<256, 384, 0, stream>>>(x, w2, g, b, h, sums, (unsigned short*)d_out);
}
```

```cpp
#include <hip/hip_runtime.h>
#include <stdint.h>

typedef unsigned long long u64;
typedef unsigned u32;
typedef float f32x2 __attribute__((ext_vector_type(2)));
typedef float f32x4 __attribute__((ext_vector_type(4)));
typedef float f32x16 __attribute__((ext_vector_type(16)));
typedef short s16x4 __attribute__((ext_vector_type(4)));
typedef short s16x8 __attribute__((ext_vector_type(8)));
typedef __bf16 bf16x2 __attribute__((ext_vector_type(2)));
typedef u32 u32x2 __attribute__((ext_vector_type(2)));
typedef u32 u32x4 __attribute__((ext_vector_type(4)));
#define LDS_AS __attribute__((address_space(3)))
typedef __attribute__((address_space(1))) u64 gu64;
#define RLX_AGENT __ATOMIC_RELAXED, __HIP_MEMORY_SCOPE_AGENT
#define DI __device__ __forceinline__

constexpr int IMG = 224, CH = 96, HID = 384, NT = 768;
constexpr u32 X_BYTES = IMG * IMG * CH * 2;

constexpr float GA = 0.4686686930532063f, GK = 4.91222953926424f;
constexpr float GA_INV = 2.1337034344781243f, GA_INV2 = 4.552690346303743f;

constexpr u32 L_X = 0, X_REC = 832;
constexpr u32 L_Y = 0, L_YR = 36864;
constexpr u32 L_W1L = 37632;
constexpr u32 L_SCR = 74496, SCR_W = 2304;
constexpr u32 L_SS = 102144;
constexpr u32 L_V = 83200, V_ROW = 416;
constexpr u32 L_T = 83200, T_ROW = 288;
constexpr u32 L_W1E = 123136;
constexpr u32 L_HREM = 160000;
constexpr u32 L_W2 = 0;
constexpr u32 L_E = 74496;
constexpr u32 L_RP = 123648, L_BP = 126720, L_GG = 128256, L_PS = 129792, L_RED = 129856;
constexpr u32 LDS_BYTES = 163840;

constexpr int WS_EPOCH = 0, WS_TOT = 64, WS_PART = 64 + 8 * HID;
constexpr int SPIN_CAP = 1 << 20;

#define SB() __builtin_amdgcn_sched_barrier(0)
DI void bar_lds() { asm volatile("s_waitcnt lgkmcnt(0)\n\ts_barrier" ::: "memory"); }
DI void bar_all() { asm volatile("s_waitcnt vmcnt(0) lgkmcnt(0)\n\ts_barrier" ::: "memory"); }
DI u32 pack_bf16(float a, float b) { bf16x2 v = __builtin_convertvector(f32x2{a, b}, bf16x2); return __builtin_bit_cast(u32, v); }
DI float bf_lo(u32 v) { return __uint_as_float(v << 16); }
DI float bf_hi(u32 v) { return __uint_as_float(v & 0xffff0000u); }
DI u32 perm_lo(u32 a, u32 b) { return __builtin_amdgcn_perm(b, a, 0x05040100u); }
DI u32 perm_hi(u32 a, u32 b) { return __builtin_amdgcn_perm(b, a, 0x07060302u); }
DI s16x4 tr_read(LDS_AS char* p) { return __builtin_amdgcn_ds_read_tr16_b64_v4i16((LDS_AS s16x4*)p); }
DI s16x8 tr_frag(LDS_AS char* p, int step) {
  s16x4 a = tr_read(p), b = tr_read(p + step);
  return __builtin_shufflevector(a, b, 0, 1, 2, 3, 4, 5, 6, 7);
}
DI f32x16 mfma32(s16x8 a, s16x8 b, f32x16 c) { return __builtin_amdgcn_mfma_f32_32x32x16_bf16(a, b, c, 0, 0, 0); }
DI f32x4 mfma16(s16x8 a, s16x8 b, f32x4 c) { return __builtin_amdgcn_mfma_f32_16x16x32_bf16(a, b, c, 0, 0, 0); }
DI float sum_xor32(float v) { auto r = __builtin_amdgcn_permlane32_swap(__float_as_uint(v), __float_as_uint(v), false, false); return __uint_as_float(r[0]) + __uint_as_float(r[1]); }
DI float sum_xor16(float v) { auto r = __builtin_amdgcn_permlane16_swap(__float_as_uint(v), __float_as_uint(v), false, false); return __uint_as_float(r[0]) + __uint_as_float(r[1]); }
DI u64 granule(u64 tag, float v) { return (tag << 24) | (u64)(((__float_as_uint(v) + 0x40u) >> 7) & 0xffffffu); }
DI float granule_val(u64 g) { return __uint_as_float(((u32)g & 0xffffffu) << 7); }
DI int pos_channel(int p) { return 8 * ((p % 24) >> 1) + 2 * (p / 24) + (p & 1); }

DI f32x2 gelu2(f32x2 x) {
  f32x2 t = __builtin_elementwise_fma(x, x, f32x2{GK, GK});
  f32x2 z = t * x;
  f32x2 e = {__builtin_amdgcn_exp2f(-z.x), __builtin_amdgcn_exp2f(-z.y)};
  f32x2 d = e + 1.0f;
  f32x2 r = {__builtin_amdgcn_rcpf(d.x), __builtin_amdgcn_rcpf(d.y)};
  return x * r;
}

DI void layer_norm_unit(LDS_AS char* L, int lane, u32 vcol, u32 dst, u32 dst_step) {
  const int g = lane >> 4, q = (lane >> 2) & 3;
  LDS_AS char* p0 = L + L_V + (8 * g + 4 * (g & 1) + q) * V_ROW + vcol;
  LDS_AS char* p1 = L + L_V + (8 * g + 4 * ((g & 1) ^ 1) + q) * V_ROW + vcol;
  u32 v[12];
#pragma unroll
  for (int c = 0; c < 3; c++) {
    u32x2 a = __builtin_bit_cast(u32x2, tr_read(p0 + c * 32 * V_ROW));
    u32x2 b = __builtin_bit_cast(u32x2, tr_read(p1 + c * 32 * V_ROW));
    v[4 * c] = a.x; v[4 * c + 1] = a.y; v[4 * c + 2] = b.x; v[4 * c + 3] = b.y;
  }
  f32x2 f[12];
  f32x2 s = {0.f, 0.f}, ss = {0.f, 0.f};
#pragma unroll
  for (int i = 0; i < 12; i++) { f[i] = f32x2{bf_lo(v[i]), bf_hi(v[i])}; s += f[i]; ss = __builtin_elementwise_fma(f[i], f[i], ss); }
  const float sum = sum_xor32(sum_xor16(s.x + s.y)), sq = sum_xor32(sum_xor16(ss.x + ss.y));
  const float mu = sum * (1.0f / CH);
  const float var = fmaxf(__builtin_fmaf(-mu, mu, sq * (1.0f / CH)), 0.f);
  const float rs = GA * __builtin_amdgcn_rsqf(var + 1e-5f);
  const float off = -mu * rs;
#pragma unroll
  for (int c = 0; c < 3; c++) {
    u32x4 o;
#pragma unroll
    for (int i = 0; i < 4; i++) { f32x2 y = __builtin_elementwise_fma(f[4 * c + i], f32x2{rs, rs}, f32x2{off, off}); o[i] = pack_bf16(y.x, y.y); }
    *(LDS_AS u32x4*)(L + dst + c * dst_step) = o;
  }
}

#define STAMP(i)

__global__ void __launch_bounds__(NT, 3)
slak_block_kernel(const unsigned short* __restrict__ x, const unsigned short* __restrict__ dw, const unsigned short* __restrict__ w1,
                  const unsigned short* __restrict__ w2, const unsigned short* __restrict__ grn_g, const unsigned short* __restrict__ grn_b,
                  unsigned short* __restrict__ out, u64* ws) {
  extern __shared__ __attribute__((aligned(16))) char smem[];
  LDS_AS char* const L = (LDS_AS char*)smem;
  const int t = threadIdx.x, lane = t & 63, bid = blockIdx.x;
  const int w = __builtin_amdgcn_readfirstlane(t >> 6);
  STAMP(0);
  const int xcd = bid & 7, idx = bid >> 3;
  const int y0 = 14 * ((xcd >> 1) * 4 + (idx >> 3)), x0 = 14 * ((xcd & 1) * 8 + (idx & 7));
  const auto xrs = __builtin_amdgcn_make_buffer_rsrc((void*)x, 0, X_BYTES, 0x00020000);
  gu64* const gws = (gu64*)ws;

  u32 dwv[7];
  const u32 tb = t >= 432 ? t - 432 : 0, tbr = (tb * 1366) >> 16, tbc = tb - tbr * 48;
  if (w >= 6) {
#pragma unroll
    for (int j = 0; j < 7; j++) dwv[j] = *(const u32*)((const char*)dw + (tbr * (7 * CH * 2) + tbc * 4) + j * (CH * 2));
  }
  u32x4 xin[2][4];
  u32 xdst[2];
#pragma unroll
  for (int it = 0; it < 2; it++) {
    if (it == 0 || w < 7) {
      const u32 item = t + it * NT, P = (item * 5462) >> 16, o = item - P * 12, Y = (P * 6554) >> 16, X = P - Y * 10;
      const u32 gy = y0 - 3 + 2 * Y, gx = x0 - 3 + 2 * X;
      const u32 base = (u32)(__mul24((int)gy, IMG * CH * 2) + __mul24((int)gx, CH * 2)) + o * 16;
      const bool live = item < 1200;
      const bool yk[2] = {live && gy < (u32)IMG, live && gy + 1 < (u32)IMG}, xk[2] = {gx < (u32)IMG, gx + 1 < (u32)IMG};
      xdst[it] = L_X + P * X_REC + o * 16;
#pragma unroll
      for (int k = 0; k < 4; k++) {
        const u32 off = (yk[k >> 1] && xk[k & 1]) ? base + (k >> 1) * (IMG * CH * 2) + (k & 1) * (CH * 2) : 0x80000000u;
        xin[it][k] = __builtin_amdgcn_raw_buffer_load_b128(xrs, off, 0, 0);
      }
    }
  }
  const u64 epoch = __hip_atomic_load(gws + WS_EPOCH, RLX_AGENT);
  u32 warm = 0;
  if (w >= 10 && lane < 18) warm = *(const u32*)((w == 10 ? w1 : w2) + (idx * 18 + lane) * 64);

  if (t >= 432) {
    LDS_AS char* d = L + L_T + (24 * (tbc & 3) + 2 * (tbc >> 2)) * T_ROW + (tbr + 1) * 16;
    *(LDS_AS u32x4*)(d) = u32x4{perm_lo(dwv[0], dwv[1]), perm_lo(dwv[2], dwv[3]), perm_lo(dwv[4], dwv[5]), dwv[6] & 0xffffu};
    *(LDS_AS u32x4*)(d + 144) = u32x4{dwv[0] << 16, perm_lo(dwv[1], dwv[2]), perm_lo(dwv[3], dwv[4]), perm_lo(dwv[5], dwv[6])};
    *(LDS_AS u32x4*)(d + T_ROW) = u32x4{perm_hi(dwv[0], dwv[1]), perm_hi(dwv[2], dwv[3]), perm_hi(dwv[4], dwv[5]), dwv[6] >> 16};
    *(LDS_AS u32x4*)(d + T_ROW + 144) = u32x4{dwv[0] & 0xffff0000u, perm_hi(dwv[1], dwv[2]), perm_hi(dwv[3], dwv[4]), perm_hi(dwv[5], dwv[6])};
  } else if (t < 384) {
    *(LDS_AS u32x4*)(L + L_T + (t >> 2) * T_ROW + ((t >> 1) & 1) * 144 + (t & 1) * 128) = u32x4{0, 0, 0, 0};
  }
  bar_lds();
  STAMP(1);
  const int cg = w >> 1, xh = w & 1;
  u32x4 wr[8];
  {
    LDS_AS char* p = L + L_T + (16 * cg + (lane >> 2)) * T_ROW + (lane & 1) * 144 + (1 - ((lane >> 1) & 1)) * 16;
#pragma unroll
    for (int r = 0; r < 8; r++) wr[r] = *(LDS_AS u32x4*)(p + r * 16);
  }
  const unsigned short* w1src[3];
#pragma unroll
  for (int u = 0; u < 3; u++) {
    const int k = 16 * (3 * (w & 1) + u) + (lane >> 2), rho = k ^ (((k >> 3) & 1) << 2), ti = w >> 1;
    w1src[u] = w1 + pos_channel(rho) * HID + (6 * (ti / 3) + ti % 3) * 32 + (lane & 3) * 8;
    __builtin_amdgcn_global_load_lds((const __attribute__((address_space(1))) void*)w1src[u],
                                     (LDS_AS void*)(L + L_W1E + (w >> 1) * 6144 + (3 * (w & 1) + u) * 1024), 16, 0, 0);
  }
#pragma unroll
  for (int it = 0; it < 2; it++) {
    if (it == 0 || t < 432) {
#pragma unroll
      for (int j = 0; j < 4; j++)
        *(LDS_AS u32x4*)(L + xdst[it] + j * 192) = u32x4{perm_lo(xin[it][0][j], xin[it][1][j]), perm_lo(xin[it][2][j], xin[it][3][j]),
                                                         perm_hi(xin[it][0][j], xin[it][1][j]), perm_hi(xin[it][2][j], xin[it][3][j])};
    }
  }
  asm volatile("" :: "v"(warm));
  bar_all();
  STAMP(2);

  {
    LDS_AS char* xb = L + L_X + (4 * xh + (lane & 3)) * X_REC + (16 * cg + (lane >> 2)) * 8;
    LDS_AS char* xb2 = xb + 70 * X_REC;
    LDS_AS char* vb = L + L_V + (16 * cg + (lane >> 2)) * V_ROW + (4 * xh + (lane & 3)) * 8;
    const bool vok = 4 * xh + (lane & 3) < 7;
    f32x4 acc[7];
#pragma unroll
    for (int i = 0; i < 7; i++) acc[i] = f32x4{0.f, 0.f, 0.f, 0.f};
#pragma unroll
    for (int Y = 0; Y < 10; Y++) {
#pragma unroll
      for (int sx = 0; sx < 4; sx++) {
        const s16x4 d = Y < 7 ? *(LDS_AS s16x4*)(xb + (Y * 10 + sx) * X_REC) : *(LDS_AS s16x4*)(xb2 + ((Y - 7) * 10 + sx) * X_REC);
#pragma unroll
        for (int sy = 0; sy < 4; sy++) {
          const int Yo = Y - sy;
          if (Yo >= 0 && Yo < 7) {
            const s16x4 a = __builtin_bit_cast(s16x4, u32x2{wr[2 * sy][sx], wr[2 * sy + 1][sx]});
            acc[Yo] = __builtin_amdgcn_mfma_f32_4x4x4bf16_1k(a, d, acc[Yo], 0, 0, 0);
          }
        }
      }
      if (Y >= 3) {
        const f32x4 r = acc[Y - 3];
        if (vok) *(LDS_AS u32x2*)(vb + (Y - 3) * 56) = u32x2{pack_bf16(r[0], r[1]), pack_bf16(r[2], r[3])};
      }
    }
  }
  bar_lds();
  STAMP(3);
#pragma unroll
  for (int u = 0; u < 3; u++)
    __builtin_amdgcn_global_load_lds((const __attribute__((address_space(1))) void*)(w1src[u] + 96),
                                     (LDS_AS void*)(L + L_W1L + (w >> 1) * 6144 + (3 * (w & 1) + u) * 1024), 16, 0, 0);

  const int T = w >> 1, Hh = w & 1;
  layer_norm_unit(L, lane, (4 * w + (lane & 3)) * 8, L_Y + T * 6144 + ((lane >> 4) * 32 + 16 * Hh + (lane & 15)) * 16, 2048);
  if (w == 11) layer_norm_unit(L, lane, 48 * 8, L_YR + ((lane >> 4) * 4 + (lane & 3)) * 16, 256);
  bar_all();
  STAMP(4);

  const int g4 = lane >> 4, q4 = (lane >> 2) & 3, p4 = lane & 3;
  s16x8 hf[12];
  {
    s16x8 yf[6];
#pragma unroll
    for (int k = 0; k < 6; k++) yf[k] = *(LDS_AS s16x8*)(L + L_Y + T * 6144 + k * 1024 + lane * 16);
    const u32 wl = (8 * (g4 >> 1) + q4) * 64 + (g4 & 1) * 32 + p4 * 8 + Hh * 3 * 6144;
    LDS_AS char* wE = L + L_W1E + wl;
    LDS_AS char* wL = L + L_W1L + wl;
    LDS_AS char* sw = L + L_SCR + w * SCR_W + (lane & 31) * 72 + (lane >> 5) * 8;
    LDS_AS char* sr = L + L_SCR + w * SCR_W + (8 * (g4 >> 1) + q4) * 72 + (4 * ((g4 & 1) ^ Hh) + p4) * 8;
    LDS_AS char* ssw = L + L_SS + ((2 * T + (lane >> 5)) * HID + Hh * 192 + (lane & 31)) * 4;
    const f32x16 zero16 = {0.f, 0.f, 0.f, 0.f, 0.f, 0.f, 0.f, 0.f, 0.f, 0.f, 0.f, 0.f, 0.f, 0.f, 0.f, 0.f};
    f32x16 acc = zero16;
#pragma unroll
    for (int k = 0; k < 6; k++) acc = mfma32(yf[k], tr_frag(wE + k * 1024, 256), acc);
    SB();
#pragma unroll
    for (int s = 0; s < 6; s++) {
      LDS_AS char* wp = s < 5 ? (s + 1 < 3 ? wE : wL) + ((s + 1) % 3) * 6144 : (T < 3 ? wE : wL) + (T % 3) * 6144;
      s16x8 wf[6];
#pragma unroll
      for (int k = 0; k < 3; k++) wf[k] = tr_frag(wp + k * 1024, 256);
      if (s == 5) {
#pragma unroll
        for (int k = 0; k < 6; k++) yf[k] = *(LDS_AS s16x8*)(L + L_YR + ((2 * k + (lane >> 5)) * 4 + (lane & 3)) * 16);
      }
      f32x2 xv[8], z[8];
#pragma unroll
      for (int i = 0; i < 8; i++) { xv[i] = f32x2{acc[2 * i], acc[2 * i + 1]}; z[i] = __builtin_elementwise_fma(xv[i], xv[i], f32x2{GK, GK}) * xv[i]; }
      SB();
      f32x16 nxt = zero16;
#pragma unroll
      for (int i = 0; i < 16; i++) {
        if (i % 3 == 0 && i < 9) { nxt = mfma32(yf[i / 3], wf[i / 3], nxt); SB(); }
        z[i >> 1][i & 1] = __builtin_amdgcn_exp2f(-z[i >> 1][i & 1]);
        if (i % 3 == 2 && i < 9) SB();
      }
#pragma unroll
      for (int k = 3; k < 6; k++) wf[k] = tr_frag(wp + k * 1024, 256);
      SB();
#pragma unroll
      for (int i = 0; i < 8; i++) z[i] += 1.0f;
      SB();
#pragma unroll
      for (int i = 0; i < 16; i++) {
        if (i % 3 == 0 && i < 9) { nxt = mfma32(yf[3 + i / 3], wf[3 + i / 3], nxt); SB(); }
        z[i >> 1][i & 1] = __builtin_amdgcn_rcpf(z[i >> 1][i & 1]);
        if (i % 3 == 2 && i < 9) SB();
      }
      SB();
      f32x2 ssa = {0.f, 0.f};
#pragma unroll
      for (int Q = 0; Q < 4; Q++) {
        const f32x2 a = xv[2 * Q] * z[2 * Q], b = xv[2 * Q + 1] * z[2 * Q + 1];
        ssa = __builtin_elementwise_fma(a, a, ssa);
        ssa = __builtin_elementwise_fma(b, b, ssa);
        *(LDS_AS u32x2*)(sw + Q * 16) = u32x2{pack_bf16(a.x, a.y), pack_bf16(b.x, b.y)};
      }
      hf[2 * s] = tr_frag(sr, 4 * 72);
      hf[2 * s + 1] = tr_frag(sr + 16 * 72, 4 * 72);
      *(LDS_AS float*)(ssw + s * 128) = ssa.x + ssa.y;
      acc = nxt;
      SB();
    }
    {
      const f32x2 a = gelu2(f32x2{acc[0], acc[1]}), b = gelu2(f32x2{acc[2], acc[3]});
      const f32x2 sq = __builtin_elementwise_fma(a, a, b * b);
      if (lane < 32) {
        const int hc = Hh * 192 + T * 32 + lane;
        *(LDS_AS u32x2*)(L + L_HREM + hc * 8) = u32x2{pack_bf16(a.x, a.y), pack_bf16(b.x, b.y)};
        *(LDS_AS float*)(L + L_SS + (12 * HID + hc) * 4) = sq.x + sq.y;
      }
    }
  }
  bar_lds();
  STAMP(5);

  const u64 tag = (epoch + 1) & ((1ull << 40) - 1);
  if (t < HID) {
    float s = 0.f;
#pragma unroll
    for (int i = 0; i < 13; i++) s += *(LDS_AS float*)(L + L_SS + (i * HID + t) * 4);
    __hip_atomic_store(gws + WS_PART + ((t >> 4) * 256 + bid) * 16 + (t & 15), granule(tag, s), RLX_AGENT);
  }
  u32x4 w2r[6];
#pragma unroll
  for (int i = 0; i < 6; i++) w2r[i] = *(const u32x4*)(w2 + ((t >> 2) + 192 * (i & 1)) * CH + 32 * (i >> 1) + 8 * (t & 3));
  const int pidx = 4 * w + ((lane >> 2) & 3);
  const u32 ooff = (u32)(((y0 + 2 * (pidx / 7) + ((lane >> 1) & 1)) * IMG + x0 + 2 * (pidx % 7) + (lane & 1)) * (CH * 2) + g4 * 16);
  u32x4 xr[3];
#pragma unroll
  for (int i = 0; i < 3; i++) xr[i] = *(const u32x4*)((const char*)x + ooff + i * 64);
  s16x8 bfr[6];
#pragma unroll
  for (int i = 0; i < 6; i++) bfr[i] = *(const s16x8*)(grn_b + 16 * (6 * (w / 3) + i) + 8 * (lane >> 5));
  const float gam = t < HID ? bf_lo(grn_g[t]) : 0.f;

  if (bid < 24) {
    u64 gr[6];
#pragma unroll
    for (int i = 0; i < 6; i++) gr[i] = (t + i * NT < 4096) ? ~(tag << 24) : (tag << 24);
    for (int spin = 0; spin < SPIN_CAP; spin++) {
      bool ok = true;
#pragma unroll
      for (int i = 0; i < 6; i++) if ((gr[i] >> 24) != tag) gr[i] = __hip_atomic_load(gws + WS_PART + bid * 4096 + t + i * NT, RLX_AGENT);
#pragma unroll
      for (int i = 0; i < 6; i++) ok &= (gr[i] >> 24) == tag;
      if (__all(ok)) break;
    }
    float s = 0.f;
#pragma unroll
    for (int i = 0; i < 6; i++) s += granule_val(gr[i]);
    s = sum_xor32(sum_xor16(s));
    if (lane < 16) *(LDS_AS float*)(L + L_RED + (w * 16 + lane) * 4) = s;
    bar_lds();
    if (t < 128) {
      float tot = 0.f;
#pragma unroll
      for (int i = 0; i < 12; i++) tot += *(LDS_AS float*)(L + L_RED + (i * 16 + (t & 15)) * 4);
      const float gx = __builtin_amdgcn_sqrtf(__builtin_fmaf(tot, GA_INV2, 1e-6f));
      __hip_atomic_store(gws + WS_TOT + (t >> 4) * HID + bid * 16 + (t & 15), granule(tag, gx), RLX_AGENT);
    }
  }

#pragma unroll
  for (int i = 0; i < 6; i++) *(LDS_AS u32x4*)(L + L_W2 + (t + i * NT) * 16) = w2r[i];
  bar_lds();
  const int pq = 4 * (g4 & 1) + p4;
  const u32 w2l = (8 * (g4 >> 1) + q4) * 64 + (4 * (pq & 1) + 2 * (pq >> 2) + ((pq >> 1) & 1)) * 8;
  {
    LDS_AS char* ap = L + L_W2 + (w % 3) * 24576 + (w / 3) * 6144 + w2l;
    f32x16 acc = {0.f, 0.f, 0.f, 0.f, 0.f, 0.f, 0.f, 0.f, 0.f, 0.f, 0.f, 0.f, 0.f, 0.f, 0.f, 0.f};
#pragma unroll
    for (int k = 0; k < 6; k++) acc = mfma32(tr_frag(ap + k * 1024, 256), bfr[k], acc);
    if ((lane & 31) == 0) {
      LDS_AS char* d = L + L_BP + ((w / 3) * CH + (w % 3) * 32 + (lane >> 5) * 16) * 4;
#pragma unroll
      for (int i = 0; i < 4; i++) *(LDS_AS f32x4*)(d + i * 16) = f32x4{acc[4 * i], acc[4 * i + 1], acc[4 * i + 2], acc[4 * i + 3]};
    }
  }
  bar_lds();
  f32x4 xb[6];
#pragma unroll
  for (int i = 0; i < 6; i++) {
    f32x4 a = f32x4{bf_lo(xr[i >> 1][2 * (i & 1)]), bf_hi(xr[i >> 1][2 * (i & 1)]), bf_lo(xr[i >> 1][2 * (i & 1) + 1]), bf_hi(xr[i >> 1][2 * (i & 1) + 1])};
#pragma unroll
    for (int k = 0; k < 4; k++) a += *(LDS_AS f32x4*)(L + L_BP + (k * CH + (i >> 1) * 32 + g4 * 8 + (i & 1) * 4) * 4);
    asm volatile("" : "+v"(a));
    xb[i] = a;
    SB();
  }
  f32x2 w2f[24];
#pragma unroll
  for (int i = 0; i < 24; i++) w2f[i] = f32x2{bf_lo(w2r[i >> 2][i & 3]), bf_hi(w2r[i >> 2][i & 3])};
#pragma unroll
  for (int i = 0; i < 24; i++) asm volatile("" : "+v"(w2f[i]));
  STAMP(6);

  if (t < HID) {
    u64 gr = ~(tag << 24);
    for (int spin = 0; spin < SPIN_CAP; spin++) {
      if ((gr >> 24) != tag) gr = __hip_atomic_load(gws + WS_TOT + (bid & 7) * HID + t, RLX_AGENT);
      if (__all((gr >> 24) == tag)) break;
    }
    const float gx = granule_val(gr);
    *(LDS_AS float*)(L + L_GG + t * 4) = gam * gx;
    float s = sum_xor32(sum_xor16(gx));
    s += __builtin_bit_cast(float, __builtin_amdgcn_update_dpp(0, __builtin_bit_cast(int, s), 0x128, 0xf, 0xf, false));
    s += __builtin_bit_cast(float, __builtin_amdgcn_update_dpp(0, __builtin_bit_cast(int, s), 0x124, 0xf, 0xf, false));
    s += __builtin_bit_cast(float, __builtin_amdgcn_update_dpp(0, __builtin_bit_cast(int, s), 0x4e, 0xf, 0xf, false));
    s += __builtin_bit_cast(float, __builtin_amdgcn_update_dpp(0, __builtin_bit_cast(int, s), 0xb1, 0xf, 0xf, false));
    if (lane == 0) *(LDS_AS float*)(L + L_PS + w * 4) = s;
  }
  bar_lds();
  STAMP(7);
  if (bid == 0 && t == 0) __hip_atomic_store(gws + WS_EPOCH, epoch + 1, RLX_AGENT);
  {
    const f32x4 pa = *(LDS_AS f32x4*)(L + L_PS);
    const f32x2 pb = *(LDS_AS f32x2*)(L + L_PS + 16);
    const float mean = (((pa[0] + pa[1]) + (pa[2] + pa[3])) + (pb[0] + pb[1])) * (1.0f / HID);
    const float inv = GA_INV * __builtin_amdgcn_rcpf(mean + 1e-6f);
    const float s0 = __builtin_fmaf(*(LDS_AS float*)(L + L_GG + (t >> 2) * 4), inv, GA_INV);
    const float s1 = __builtin_fmaf(*(LDS_AS float*)(L + L_GG + (192 + (t >> 2)) * 4), inv, GA_INV);
#pragma unroll
    for (int i = 0; i < 6; i++) {
      const float s = (i & 1) ? s1 : s0;
      u32x4 o;
#pragma unroll
      for (int j = 0; j < 4; j++) { const f32x2 v = w2f[4 * i + j] * s; o[j] = pack_bf16(v.x, v.y); }
      *(LDS_AS u32x4*)(L + L_W2 + (t + i * NT) * 16) = o;
    }
  }
  bar_lds();
  STAMP(8);

  {
    LDS_AS char* ap = L + L_W2 + Hh * 12 * 1024 + w2l;
    LDS_AS char* ew = L + L_E + w * 4096 + lane * 16;
    LDS_AS char* er = L + L_E + (w ^ 1) * 4096 + lane * 16;
    f32x4 keep[2];
#pragma unroll
    for (int ot = 0; ot < 4; ot++) {
      f32x16 acc = {0.f, 0.f, 0.f, 0.f, 0.f, 0.f, 0.f, 0.f, 0.f, 0.f, 0.f, 0.f, 0.f, 0.f, 0.f, 0.f};
      if (ot < 3) {
#pragma unroll
        for (int k = 0; k < 12; k++) acc = mfma32(tr_frag(ap + ot * 24576 + k * 1024, 256), hf[k], acc);
      }
      if (ot == 0) {
        const int o16 = w % 6, kh = w / 6;
        LDS_AS char* rp = L + L_W2 + (o16 >> 1) * 24576 + (kh * 192 + 8 * g4 + q4) * 64 + (4 * (o16 & 1) + p4) * 8;
        LDS_AS char* hp = L + L_HREM + (kh * 192 + 8 * g4 + q4) * 8;
        f32x4 ra = {0.f, 0.f, 0.f, 0.f};
#pragma unroll
        for (int k = 0; k < 6; k++) ra = mfma16(tr_frag(rp + k * 2048, 256), tr_frag(hp + k * 256, 32), ra);
        if ((lane & 15) < 4) *(LDS_AS f32x4*)(L + L_RP + ((kh * 4 + (lane & 3)) * CH + 16 * o16 + 4 * g4) * 4) = ra;
      } else {
        bar_lds();
        const int pt = ot - 1;
        const f32x4 a = keep[0] + *(LDS_AS f32x4*)(er + (pt & 1) * 2048) + xb[2 * pt];
        const f32x4 b = keep[1] + *(LDS_AS f32x4*)(er + (pt & 1) * 2048 + 1024) + xb[2 * pt + 1];
        *(u32x4*)((char*)out + ooff + pt * 64) = u32x4{pack_bf16(a[0], a[1]), pack_bf16(a[2], a[3]), pack_bf16(b[0], b[1]), pack_bf16(b[2], b[3])};
        if (ot == 1 && t < 48) {
          const int px = t / 12, oc = t % 12;
          const u32 ro = (u32)(((y0 + 12 + (px >> 1)) * IMG + x0 + 12 + (px & 1)) * (CH * 2) + oc * 16);
          const u32x4 xv = *(const u32x4*)((const char*)x + ro);
          f32x4 r[2];
#pragma unroll
          for (int i = 0; i < 2; i++) {
            f32x4 a2 = f32x4{bf_lo(xv[2 * i]), bf_hi(xv[2 * i]), bf_lo(xv[2 * i + 1]), bf_hi(xv[2 * i + 1])};
#pragma unroll
            for (int k = 0; k < 4; k++) a2 += *(LDS_AS f32x4*)(L + L_BP + (k * CH + oc * 8 + i * 4) * 4);
            a2 += *(LDS_AS f32x4*)(L + L_RP + (px * CH + oc * 8 + i * 4) * 4);
            a2 += *(LDS_AS f32x4*)(L + L_RP + ((4 + px) * CH + oc * 8 + i * 4) * 4);
            r[i] = a2;
          }
          *(u32x4*)((char*)out + ro) = u32x4{pack_bf16(r[0][0], r[0][1]), pack_bf16(r[0][2], r[0][3]), pack_bf16(r[1][0], r[1][1]), pack_bf16(r[1][2], r[1][3])};
        }
      }
      if (ot < 3) {
        f32x4 send[2];
#pragma unroll
        for (int r = 0; r < 8; r++) {
          auto sw2 = __builtin_amdgcn_permlane16_swap(__float_as_uint(acc[r]), __float_as_uint(acc[r + 8]), false, false);
          keep[r >> 2][r & 3] = __uint_as_float(sw2[0]);
          send[r >> 2][r & 3] = __uint_as_float(sw2[1]);
        }
        *(LDS_AS f32x4*)(ew + (ot & 1) * 2048) = send[0];
        *(LDS_AS f32x4*)(ew + (ot & 1) * 2048 + 1024) = send[1];
      }
    }
  }
  STAMP(9);
}

extern "C" void kernel_launch(void* const* d_in, const int* in_sizes, int n_in,
                              void* d_out, int out_size, void* d_ws, size_t ws_size,
                              hipStream_t stream) {
  (void)in_sizes; (void)n_in; (void)out_size; (void)ws_size;
  (void)hipFuncSetAttribute((const void*)slak_block_kernel, hipFuncAttributeMaxDynamicSharedMemorySize, LDS_BYTES);
  slak_block_kernel<<<256, NT, LDS_BYTES, stream>>>(
      (const unsigned short*)d_in[0], (const unsigned short*)d_in[1], (const unsigned short*)d_in[2],
      (const unsigned short*)d_in[3], (const unsigned short*)d_in[4], (const unsigned short*)d_in[5],
      (unsigned short*)d_out, (u64*)d_ws);
}
```

```cpp
#include <hip/hip_runtime.h>
#include <stdint.h>

typedef unsigned long long u64;
typedef unsigned u32;
typedef float f32x2 __attribute__((ext_vector_type(2)));
typedef float f32x4 __attribute__((ext_vector_type(4)));
typedef float f32x16 __attribute__((ext_vector_type(16)));
typedef short s16x4 __attribute__((ext_vector_type(4)));
typedef short s16x8 __attribute__((ext_vector_type(8)));
typedef __bf16 bf16x2 __attribute__((ext_vector_type(2)));
typedef u32 u32x2 __attribute__((ext_vector_type(2)));
typedef u32 u32x4 __attribute__((ext_vector_type(4)));
#define LDS_AS __attribute__((address_space(3)))
typedef __attribute__((address_space(1))) u64 gu64;
#define RLX_AGENT __ATOMIC_RELAXED, __HIP_MEMORY_SCOPE_AGENT
#define DI __device__ __forceinline__

constexpr int IMG = 224, CH = 96, HID = 384, NT = 768;
constexpr u32 X_BYTES = IMG * IMG * CH * 2;

constexpr float GA = 0.4686686930532063f, GK = 4.91222953926424f;
constexpr float GA_INV = 2.1337034344781243f, GA_INV2 = 4.552690346303743f;

constexpr u32 L_X = 0, X_REC = 832;
constexpr u32 L_Y = 0, L_YR = 36864;
constexpr u32 L_W1L = 37632;
constexpr u32 L_SCR = 74496, SCR_W = 2304;
constexpr u32 L_SS = 102144;
constexpr u32 L_V = 83200, V_ROW = 416;
constexpr u32 L_T = 83200, T_ROW = 288;
constexpr u32 L_W1E = 123136;
constexpr u32 L_HREM = 160000;
constexpr u32 L_W2 = 0;
constexpr u32 L_E = 74496;
constexpr u32 L_B = 163072;
constexpr u32 L_RP = 123648, L_BP = 126720, L_GG = 128256, L_PS = 129792, L_RED = 129856;
constexpr u32 LDS_BYTES = 163840;

constexpr int WS_EPOCH = 0, WS_TOT = 64, WS_PART = 64 + 8 * HID;
#define CODE_LINES 0
constexpr int SPIN_CAP = 1 << 20;

#define SB() __builtin_amdgcn_sched_barrier(0)
DI void bar_lds() { asm volatile("s_waitcnt lgkmcnt(0)\n\ts_barrier" ::: "memory"); }
DI void bar_all() { asm volatile("s_waitcnt vmcnt(0) lgkmcnt(0)\n\ts_barrier" ::: "memory"); }
#define LD_AUX 0
#define ST_AUX 16
typedef __amdgpu_buffer_rsrc_t rsrc_t;
DI rsrc_t make_rsrc(const void* p, u32 bytes) { return __builtin_amdgcn_make_buffer_rsrc((void*)p, 0, bytes, 0x00020000); }
DI u32x4 ld128(rsrc_t r, u32 off) { return __builtin_amdgcn_raw_buffer_load_b128(r, off, 0, LD_AUX); }
DI u32 pack_bf16(float a, float b) { bf16x2 v = __builtin_convertvector(f32x2{a, b}, bf16x2); return __builtin_bit_cast(u32, v); }
DI float bf_lo(u32 v) { return __uint_as_float(v << 16); }
DI float bf_hi(u32 v) { return __uint_as_float(v & 0xffff0000u); }
DI u32 perm_lo(u32 a, u32 b) { return __builtin_amdgcn_perm(b, a, 0x05040100u); }
DI u32 perm_hi(u32 a, u32 b) { return __builtin_amdgcn_perm(b, a, 0x07060302u); }
DI s16x4 tr_read(LDS_AS char* p) { return __builtin_amdgcn_ds_read_tr16_b64_v4i16((LDS_AS s16x4*)p); }
DI s16x8 tr_frag(LDS_AS char* p, int step) {
  s16x4 a = tr_read(p), b = tr_read(p + step);
  return __builtin_shufflevector(a, b, 0, 1, 2, 3, 4, 5, 6, 7);
}
DI f32x16 mfma32(s16x8 a, s16x8 b, f32x16 c) { return __builtin_amdgcn_mfma_f32_32x32x16_bf16(a, b, c, 0, 0, 0); }
DI f32x4 mfma16(s16x8 a, s16x8 b, f32x4 c) { return __builtin_amdgcn_mfma_f32_16x16x32_bf16(a, b, c, 0, 0, 0); }
DI float sum_xor32(float v) { auto r = __builtin_amdgcn_permlane32_swap(__float_as_uint(v), __float_as_uint(v), false, false); return __uint_as_float(r[0]) + __uint_as_float(r[1]); }
DI float sum_xor16(float v) { auto r = __builtin_amdgcn_permlane16_swap(__float_as_uint(v), __float_as_uint(v), false, false); return __uint_as_float(r[0]) + __uint_as_float(r[1]); }
DI u64 granule(u64 tag, float v) { return (tag << 24) | (u64)(((__float_as_uint(v) + 0x40u) >> 7) & 0xffffffu); }
DI float granule_val(u64 g) { return __uint_as_float(((u32)g & 0xffffffu) << 7); }
DI int pos_channel(int p) { return 8 * ((p % 24) >> 1) + 2 * (p / 24) + (p & 1); }

DI f32x2 gelu2(f32x2 x) {
  f32x2 t = __builtin_elementwise_fma(x, x, f32x2{GK, GK});
  f32x2 z = t * x;
  f32x2 e = {__builtin_amdgcn_exp2f(-z.x), __builtin_amdgcn_exp2f(-z.y)};
  f32x2 d = e + 1.0f;
  f32x2 r = {__builtin_amdgcn_rcpf(d.x), __builtin_amdgcn_rcpf(d.y)};
  return x * r;
}

DI void layer_norm_unit(LDS_AS char* L, int lane, u32 vcol, u32 dst, u32 dst_step) {
  const int g = lane >> 4, q = (lane >> 2) & 3;
  LDS_AS char* p0 = L + L_V + (8 * g + 4 * (g & 1) + q) * V_ROW + vcol;
  LDS_AS char* p1 = L + L_V + (8 * g + 4 * ((g & 1) ^ 1) + q) * V_ROW + vcol;
  u32 v[12];
#pragma unroll
  for (int c = 0; c < 3; c++) {
    u32x2 a = __builtin_bit_cast(u32x2, tr_read(p0 + c * 32 * V_ROW));
    u32x2 b = __builtin_bit_cast(u32x2, tr_read(p1 + c * 32 * V_ROW));
    v[4 * c] = a.x; v[4 * c + 1] = a.y; v[4 * c + 2] = b.x; v[4 * c + 3] = b.y;
  }
  f32x2 f[12];
  f32x2 s = {0.f, 0.f}, ss = {0.f, 0.f};
#pragma unroll
  for (int i = 0; i < 12; i++) { f[i] = f32x2{bf_lo(v[i]), bf_hi(v[i])}; s += f[i]; ss = __builtin_elementwise_fma(f[i], f[i], ss); }
  const float sum = sum_xor32(sum_xor16(s.x + s.y)), sq = sum_xor32(sum_xor16(ss.x + ss.y));
  const float mu = sum * (1.0f / CH);
  const float var = fmaxf(__builtin_fmaf(-mu, mu, sq * (1.0f / CH)), 0.f);
  const float rs = GA * __builtin_amdgcn_rsqf(var + 1e-5f);
  const float off = -mu * rs;
#pragma unroll
  for (int c = 0; c < 3; c++) {
    u32x4 o;
#pragma unroll
    for (int i = 0; i < 4; i++) { f32x2 y = __builtin_elementwise_fma(f[4 * c + i], f32x2{rs, rs}, f32x2{off, off}); o[i] = pack_bf16(y.x, y.y); }
    *(LDS_AS u32x4*)(L + dst + c * dst_step) = o;
  }
}

DI float add1(float a, float b) { float r; asm("v_add_f32 %0, %1, %2" : "=v"(r) : "v"(a), "v"(b)); return r; }

DI void finish_tile(LDS_AS char* er, const float* keep, f32x4 xa, f32x4 xc, rsrc_t ors, u32 off) {
  bar_lds();
  const f32x4 pa = *(LDS_AS f32x4*)(er), pc = *(LDS_AS f32x4*)(er + 1024);
  float v[8];
#pragma unroll
  for (int i = 0; i < 4; i++) { v[i] = add1(add1(keep[i], xa[i]), pa[i]); v[4 + i] = add1(add1(keep[4 + i], xc[i]), pc[i]); }
  __builtin_amdgcn_raw_buffer_store_b128(u32x4{pack_bf16(v[0], v[1]), pack_bf16(v[2], v[3]), pack_bf16(v[4], v[5]), pack_bf16(v[6], v[7])}, ors, off, 0, ST_AUX);
  SB();
}

#define STAMP(i)

__global__ void __launch_bounds__(NT, 3)
slak_block_kernel(const unsigned short* __restrict__ x, const unsigned short* __restrict__ dw, const unsigned short* __restrict__ w1,
                  const unsigned short* __restrict__ w2, const unsigned short* __restrict__ grn_g, const unsigned short* __restrict__ grn_b,
                  unsigned short* __restrict__ out, u64* ws) {
  extern __shared__ __attribute__((aligned(16))) char smem[];
  LDS_AS char* const L = (LDS_AS char*)smem;
  const int t = threadIdx.x, lane = t & 63, bid = blockIdx.x;
  const int w = __builtin_amdgcn_readfirstlane(t >> 6);
  STAMP(0);
  u32 code_pf = 0;
  if (t < CODE_LINES / 4) {
    const char* pc = (const char*)(__builtin_amdgcn_s_getpc() & ~255ull);
    code_pf = *(const u32*)(pc + (((bid >> 3) & 3) + 4 * t) * 128);
  }
  const int xcd = bid & 7, idx = bid >> 3;
  const int y0 = 14 * ((xcd >> 1) * 4 + (idx >> 3)), x0 = 14 * ((xcd & 1) * 8 + (idx & 7));
  const rsrc_t xrs = make_rsrc(x, X_BYTES);
  gu64* const gws = (gu64*)ws;

  u32x4 xin[2][4];
  u32 xdst[2];
  auto load_item = [&](int it) {
    const u32 item = t + it * NT, P = (item * 5462) >> 16, o = item - P * 12, Y = (P * 6554) >> 16, X = P - Y * 10;
    const u32 gy = y0 - 3 + 2 * Y, gx = x0 - 3 + 2 * X;
    u32 base = (u32)(__mul24((int)gy, IMG * CH * 2) + __mul24((int)gx, CH * 2)) + o * 16;
    asm volatile("" : "+v"(base));
    const bool live = item < 1200;
    const bool yk[2] = {live && gy < (u32)IMG, live && gy + 1 < (u32)IMG}, xk[2] = {gx < (u32)IMG, gx + 1 < (u32)IMG};
    xdst[it] = L_X + P * X_REC + o * 16;
#pragma unroll
    for (int k = 0; k < 4; k++) {
      const u32 off = (yk[k >> 1] && xk[k & 1]) ? base + (k >> 1) * (IMG * CH * 2) : 0x80000000u;
      xin[it][k] = ld128(xrs, off + (k & 1) * (CH * 2));
    }
  };
  auto store_item = [&](int it) {
#pragma unroll
    for (int j = 0; j < 4; j++)
      *(LDS_AS u32x4*)(L + xdst[it] + j * 192) = u32x4{perm_lo(xin[it][0][j], xin[it][1][j]), perm_lo(xin[it][2][j], xin[it][3][j]),
                                                       perm_hi(xin[it][0][j], xin[it][1][j]), perm_hi(xin[it][2][j], xin[it][3][j])};
  };
  const int cg = w >> 1, xh = w & 1;
  if (w >= 9) {
    const u32 tb = t - 576, r = (tb * 2731) >> 16, c4 = tb - r * 24;
    const rsrc_t drs = make_rsrc(dw, 49 * CH * 2);
    u32x2 dv[7];
#pragma unroll
    for (int j = 0; j < 7; j++) dv[j] = __builtin_amdgcn_raw_buffer_load_b64(drs, r * (7 * CH * 2) + c4 * 8 + j * (CH * 2), 0, LD_AUX);
    load_item(0);
    if (tb < 168) {
#pragma unroll
      for (int h = 0; h < 2; h++) {
        LDS_AS char* d = L + L_T + (24 * (2 * (c4 & 1) + h) + 2 * (c4 >> 1)) * T_ROW + (r + 1) * 16;
        *(LDS_AS u32x4*)(d) = u32x4{perm_lo(dv[0][h], dv[1][h]), perm_lo(dv[2][h], dv[3][h]), perm_lo(dv[4][h], dv[5][h]), dv[6][h] & 0xffffu};
        *(LDS_AS u32x4*)(d + 144) = u32x4{dv[0][h] << 16, perm_lo(dv[1][h], dv[2][h]), perm_lo(dv[3][h], dv[4][h]), perm_lo(dv[5][h], dv[6][h])};
        *(LDS_AS u32x4*)(d + T_ROW) = u32x4{perm_hi(dv[0][h], dv[1][h]), perm_hi(dv[2][h], dv[3][h]), perm_hi(dv[4][h], dv[5][h]), dv[6][h] >> 16};
        *(LDS_AS u32x4*)(d + T_ROW + 144) = u32x4{dv[0][h] & 0xffff0000u, perm_hi(dv[1][h], dv[2][h]), perm_hi(dv[3][h], dv[4][h]), perm_hi(dv[5][h], dv[6][h])};
      }
    }
  } else {
    load_item(0);
    if (t < 384) *(LDS_AS u32x4*)(L + L_T + (t >> 2) * T_ROW + ((t >> 1) & 1) * 144 + (t & 1) * 128) = u32x4{0, 0, 0, 0};
  }
  STAMP(1);
  bar_lds();
  STAMP(2);
  s16x4 wa[4][4];
  u32x4 w1r[6];
  u64 epoch;
  u32x4 aux16;
  {
    LDS_AS char* p = L + L_T + (16 * cg + (lane >> 2)) * T_ROW + (lane & 1) * 144 + (1 - ((lane >> 1) & 1)) * 16;
    u32x4 wr[8];
#pragma unroll
    for (int r = 0; r < 8; r++) wr[r] = *(LDS_AS u32x4*)(p + r * 16);
    if (w < 7) load_item(1);
    const int k = 16 * (w % 6) + (lane >> 2), rho = k ^ (((k >> 3) & 1) << 2);
    const u32 w1o = pos_channel(rho) * (HID * 2) + (w / 6) * 384 + (lane & 3) * 16;
    const rsrc_t w1rs = make_rsrc(w1, CH * HID * 2);
#pragma unroll
    for (int u = 0; u < 6; u++) w1r[u] = ld128(w1rs, w1o + u * 64);
    epoch = __hip_atomic_load(gws + WS_EPOCH, RLX_AGENT);
    aux16 = ld128(w == 7 ? make_rsrc(grn_b, HID * 2) : make_rsrc(w2, w == 8 ? HID * CH * 2 : 0),
                  w == 7 ? lane * 16 : (lane < 18 ? (idx * 18 + lane) * 128 : 0x80000000u));
#pragma unroll
    for (int i = 0; i < 16; i++) {
      u32x2 v = {wr[2 * (i >> 2)][i & 3], wr[2 * (i >> 2) + 1][i & 3]};
      asm volatile("" : "+v"(v));
      wa[i >> 2][i & 3] = __builtin_bit_cast(s16x4, v);
    }
  }
  STAMP(3);
  store_item(0);
  if (t < 432) store_item(1);
  bar_lds();
  STAMP(4);

  {
    LDS_AS char* xb = L + L_X + (4 * xh + (lane & 3)) * X_REC + (16 * cg + (lane >> 2)) * 8;
    LDS_AS char* xb2 = xb + 70 * X_REC;
    LDS_AS char* vb = L + L_V + (16 * cg + (lane >> 2)) * V_ROW + (4 * xh + (lane & 3)) * 8;
    const bool vok = 4 * xh + (lane & 3) < 7;
    f32x4 acc[7];
#pragma unroll
    for (int i = 0; i < 7; i++) acc[i] = f32x4{0.f, 0.f, 0.f, 0.f};
    s16x4 d[10][4];
    auto load_row = [&](int Y) {
#pragma unroll
      for (int sx = 0; sx < 4; sx++)
        d[Y][sx] = Y < 7 ? *(volatile LDS_AS s16x4*)(xb + (Y * 10 + sx) * X_REC) : *(volatile LDS_AS s16x4*)(xb2 + ((Y - 7) * 10 + sx) * X_REC);
    };
    load_row(0); load_row(1); load_row(2);
    SB();
#pragma unroll
    for (int Y = 0; Y < 10; Y++) {
      if (Y + 3 < 10) load_row(Y + 3);
#pragma unroll
      for (int sx = 0; sx < 4; sx++) {
#pragma unroll
        for (int sy = 3; sy >= 0; sy--) {
          const int Yo = Y - sy;
          if (Yo >= 0 && Yo < 7) acc[Yo] = __builtin_amdgcn_mfma_f32_4x4x4bf16_1k(wa[sy][sx], d[Y][sx], acc[Yo], 0, 0, 0);
        }
      }
      if (Y >= 4) {
        const f32x4 r = acc[Y - 4];
        if (vok) *(LDS_AS u32x2*)(vb + (Y - 4) * 56) = u32x2{pack_bf16(r[0], r[1]), pack_bf16(r[2], r[3])};
      }
      SB();
    }
    if (vok) *(LDS_AS u32x2*)(vb + 6 * 56) = u32x2{pack_bf16(acc[6][0], acc[6][1]), pack_bf16(acc[6][2], acc[6][3])};
  }
  const u32 w1d = (w / 6) * 3 * 6144 + (w % 6) * 1024 + lane * 16;
#pragma unroll
  for (int u = 0; u < 3; u++) *(LDS_AS u32x4*)(L + L_W1E + w1d + u * 6144) = w1r[u];
  if (w == 7 && lane < 48) *(LDS_AS u32x4*)(L + L_B + lane * 16) = aux16;
  asm volatile("" :: "v"(aux16), "v"(code_pf));
  epoch = ((u64)(u32)__builtin_amdgcn_readfirstlane((int)(epoch >> 32)) << 32) | (u32)__builtin_amdgcn_readfirstlane((int)epoch);
  bar_lds();
  STAMP(5);
#pragma unroll
  for (int u = 0; u < 3; u++) *(LDS_AS u32x4*)(L + L_W1L + w1d + u * 6144) = w1r[3 + u];

  const int T = w >> 1, Hh = w & 1;
  layer_norm_unit(L, lane, (4 * w + (lane & 3)) * 8, L_Y + T * 6144 + ((lane >> 4) * 32 + 16 * Hh + (lane & 15)) * 16, 2048);
  if (w == 11) layer_norm_unit(L, lane, 48 * 8, L_YR + ((lane >> 4) * 4 + (lane & 3)) * 16, 256);
  bar_lds();
  STAMP(6);

  const int g4 = lane >> 4, q4 = (lane >> 2) & 3, p4 = lane & 3;
  s16x8 hf[12];
  u32x4 w2r[6], xr[3], xrem;
  float gam;
  const rsrc_t w2rs = make_rsrc(w2, HID * CH * 2);
  const u32 w2o = (t >> 2) * (CH * 2) + (t & 3) * 16;
  const u32 remo = t < 48 ? (u32)(((y0 + 12 + t / 24) * IMG + x0 + 12) * (CH * 2) + (t % 24) * 16) : 0x80000000u;
  const int pidx = 4 * w + ((lane >> 2) & 3);
  const u32 ooff = (u32)(((y0 + 2 * (pidx / 7) + ((lane >> 1) & 1)) * IMG + x0 + 2 * (pidx % 7) + (lane & 1)) * (CH * 2) + g4 * 16);
  const f32x16 zero16 = {0.f, 0.f, 0.f, 0.f, 0.f, 0.f, 0.f, 0.f, 0.f, 0.f, 0.f, 0.f, 0.f, 0.f, 0.f, 0.f};
  {
    const u32 wl = (8 * (g4 >> 1) + q4) * 64 + (g4 & 1) * 32 + p4 * 8 + Hh * 3 * 6144;
    LDS_AS char* wE = L + L_W1E + wl;
    LDS_AS char* wL = L + L_W1L + wl;
    LDS_AS char* sw = L + L_SCR + w * SCR_W + (lane & 31) * 72 + (lane >> 5) * 8;
    LDS_AS char* sr = L + L_SCR + w * SCR_W + (8 * (g4 >> 1) + q4) * 72 + (4 * ((g4 & 1) ^ Hh) + p4) * 8;
    LDS_AS char* ssw = L + L_SS + ((2 * T + (lane >> 5)) * HID + Hh * 192 + (lane & 31)) * 4;
    s16x8 yf[6], wf[6];
    f32x16 acc = zero16;
    {
      LDS_AS char* wp = (T < 3 ? wE : wL) + (T % 3) * 6144;
      s16x8 yr[6], wq[6];
#pragma unroll
      for (int k = 0; k < 6; k++) { yr[k] = *(LDS_AS s16x8*)(L + L_YR + ((2 * k + (lane >> 5)) * 4 + (lane & 3)) * 16); wq[k] = tr_frag(wp + k * 1024, 256); }
#pragma unroll
      for (int k = 0; k < 6; k++) { yf[k] = *(LDS_AS s16x8*)(L + L_Y + T * 6144 + k * 1024 + lane * 16); wf[k] = tr_frag(wE + k * 1024, 256); }
      SB();
      f32x16 ra = zero16;
#pragma unroll
      for (int k = 0; k < 6; k++) ra = mfma32(yr[k], wq[k], ra);
#pragma unroll
      for (int k = 0; k < 6; k++) acc = mfma32(yf[k], wf[k], acc);
#pragma unroll
      for (int k = 0; k < 3; k++) wf[k] = tr_frag(wE + 6144 + k * 1024, 256);
      SB();
      const f32x2 a = gelu2(f32x2{ra[0], ra[1]}), b = gelu2(f32x2{ra[2], ra[3]});
      const f32x2 sq = __builtin_elementwise_fma(a, a, b * b);
      if (lane < 32) {
        const int hc = Hh * 192 + T * 32 + lane;
        *(LDS_AS u32x2*)(L + L_HREM + hc * 8) = u32x2{pack_bf16(a.x, a.y), pack_bf16(b.x, b.y)};
        *(LDS_AS float*)(L + L_SS + (12 * HID + hc) * 4) = sq.x + sq.y;
      }
      SB();
    }
#pragma unroll
    for (int s = 0; s < 6; s++) {
      LDS_AS char* wp = (s + 1 < 3 ? wE : wL) + ((s + 1) % 3) * 6144;
      LDS_AS char* wp2 = (s + 2 < 3 ? wE : wL) + ((s + 2) % 3) * 6144;
      if (s < 5) {
#pragma unroll
        for (int k = 3; k < 6; k++) wf[k] = tr_frag(wp + k * 1024, 256);
      }
      f32x2 xv[8], z[8];
#pragma unroll
      for (int i = 0; i < 8; i++) { xv[i] = f32x2{acc[2 * i], acc[2 * i + 1]}; z[i] = __builtin_elementwise_fma(xv[i], xv[i], f32x2{GK, GK}); }
      SB();
#pragma unroll
      for (int i = 0; i < 8; i++) z[i] *= xv[i];
      SB();
      f32x16 nxt = zero16;
#pragma unroll
      for (int i = 0; i < 16; i++) {
        if (s < 5 && i % 3 == 0 && i < 9) { nxt = mfma32(yf[i / 3], wf[i / 3], nxt); SB(); }
        if (s == 5 && i % 3 == 0) { w2r[i / 3] = ld128(w2rs, w2o + ((i / 3) & 1) * (192 * CH * 2) + (i / 6) * 64); SB(); }
        z[i >> 1][i & 1] = __builtin_amdgcn_exp2f(-z[i >> 1][i & 1]);
        if (i % 3 == 2 && (i < 9 || s == 5)) SB();
      }
      if (s < 4) {
#pragma unroll
        for (int k = 0; k < 3; k++) wf[k] = tr_frag(wp2 + k * 1024, 256);
      }
      SB();
#pragma unroll
      for (int i = 0; i < 8; i++) z[i] += 1.0f;
      SB();
#pragma unroll
      for (int i = 0; i < 16; i++) {
        if (s < 5 && i % 3 == 0 && i < 9) { nxt = mfma32(yf[3 + i / 3], wf[3 + i / 3], nxt); SB(); }
        if (s == 5 && i % 3 == 0 && i < 9) { xr[i / 3] = ld128(xrs, ooff + (i / 3) * 64); SB(); }
        if (s == 5 && i == 9) { xrem = ld128(xrs, remo); gam = bf_lo(grn_g[t < HID ? t : 0]); SB(); }
        z[i >> 1][i & 1] = __builtin_amdgcn_rcpf(z[i >> 1][i & 1]);
        if (i % 3 == 2 && i < 9) SB();
      }
      SB();
#pragma unroll
      for (int i = 0; i < 8; i++) z[i] *= xv[i];
      SB();
      f32x2 ssa = {0.f, 0.f}, ssb = {0.f, 0.f};
#pragma unroll
      for (int Q = 0; Q < 4; Q++) {
        ssa = __builtin_elementwise_fma(z[2 * Q], z[2 * Q], ssa);
        ssb = __builtin_elementwise_fma(z[2 * Q + 1], z[2 * Q + 1], ssb);
        *(volatile LDS_AS u32x2*)(sw + Q * 16) = u32x2{pack_bf16(z[2 * Q].x, z[2 * Q].y), pack_bf16(z[2 * Q + 1].x, z[2 * Q + 1].y)};
      }
      hf[2 * s] = tr_frag(sr, 4 * 72);
      hf[2 * s + 1] = tr_frag(sr + 16 * 72, 4 * 72);
      ssa += ssb;
      *(LDS_AS float*)(ssw + s * 128) = ssa.x + ssa.y;
      acc = nxt;
      SB();
    }
  }
  bar_lds();
  STAMP(7);

  const u64 tag = (epoch + 1) & ((1ull << 40) - 1);
  if (t < HID) {
    float s = 0.f;
#pragma unroll
    for (int i = 0; i < 13; i++) s += *(LDS_AS float*)(L + L_SS + (i * HID + t) * 4);
    __hip_atomic_store(gws + WS_PART + ((t >> 4) * 256 + bid) * 16 + (t & 15), granule(tag, s), RLX_AGENT);
  }

  if (bid < 24) {
    u64 gr[6];
#pragma unroll
    for (int i = 0; i < 6; i++) gr[i] = (t + i * NT < 4096) ? ~(tag << 24) : (tag << 24);
    for (int spin = 0; spin < SPIN_CAP; spin++) {
      bool ok = true;
#pragma unroll
      for (int i = 0; i < 6; i++) if ((gr[i] >> 24) != tag) gr[i] = __hip_atomic_load(gws + WS_PART + bid * 4096 + t + i * NT, RLX_AGENT);
#pragma unroll
      for (int i = 0; i < 6; i++) ok &= (gr[i] >> 24) == tag;
      if (__all(ok)) break;
    }
    float s = 0.f;
#pragma unroll
    for (int i = 0; i < 6; i++) s += granule_val(gr[i]);
    s = sum_xor32(sum_xor16(s));
    if (lane < 16) *(LDS_AS float*)(L + L_RED + (w * 16 + lane) * 4) = s;
    bar_lds();
    if (t < 128) {
      float tot = 0.f;
#pragma unroll
      for (int i = 0; i < 12; i++) tot += *(LDS_AS float*)(L + L_RED + (i * 16 + (t & 15)) * 4);
      const float gx = __builtin_amdgcn_sqrtf(__builtin_fmaf(tot, GA_INV2, 1e-6f));
      __hip_atomic_store(gws + WS_TOT + (t >> 4) * HID + bid * 16 + (t & 15), granule(tag, gx), RLX_AGENT);
    }
  }

#pragma unroll
  for (int i = 0; i < 6; i++) *(LDS_AS u32x4*)(L + L_W2 + (t + i * NT) * 16) = w2r[i];
  bar_lds();
  const int pq = 4 * (g4 & 1) + p4;
  const u32 w2l = (8 * (g4 >> 1) + q4) * 64 + (4 * (pq & 1) + 2 * (pq >> 2) + ((pq >> 1) & 1)) * 8;
  {
    LDS_AS char* ap = L + L_W2 + (w % 3) * 24576 + (w / 3) * 6144 + w2l;
    f32x16 acc = zero16;
    s16x8 af[6], bfr[6];
#pragma unroll
    for (int k = 0; k < 6; k++) { af[k] = tr_frag(ap + k * 1024, 256); bfr[k] = *(LDS_AS s16x8*)(L + L_B + (6 * (w / 3) + k) * 32 + (lane >> 5) * 16); }
    SB();
#pragma unroll
    for (int k = 0; k < 6; k++) acc = mfma32(af[k], bfr[k], acc);
    if ((lane & 31) == 0) {
      LDS_AS char* d = L + L_BP + ((w / 3) * CH + (w % 3) * 32 + (lane >> 5) * 16) * 4;
#pragma unroll
      for (int i = 0; i < 4; i++) *(LDS_AS f32x4*)(d + i * 16) = f32x4{acc[4 * i], acc[4 * i + 1], acc[4 * i + 2], acc[4 * i + 3]};
    }
  }
  bar_lds();
  f32x4 xb[6];
#pragma unroll
  for (int i = 0; i < 6; i++) {
    f32x4 a = f32x4{bf_lo(xr[i >> 1][2 * (i & 1)]), bf_hi(xr[i >> 1][2 * (i & 1)]), bf_lo(xr[i >> 1][2 * (i & 1) + 1]), bf_hi(xr[i >> 1][2 * (i & 1) + 1])};
#pragma unroll
    for (int k = 0; k < 4; k++) a += *(LDS_AS f32x4*)(L + L_BP + (k * CH + (i >> 1) * 32 + g4 * 8 + (i & 1) * 4) * 4);
    asm volatile("" : "+v"(a));
    xb[i] = a;
    SB();
  }
  f32x2 w2f[24];
#pragma unroll
  for (int i = 0; i < 24; i++) w2f[i] = f32x2{bf_lo(w2r[i >> 2][i & 3]), bf_hi(w2r[i >> 2][i & 3])};
#pragma unroll
  for (int i = 0; i < 24; i++) asm volatile("" : "+v"(w2f[i]));
  STAMP(8);

  if (t < HID) {
    u64 gr = ~(tag << 24);
    for (int spin = 0; spin < SPIN_CAP; spin++) {
      if ((gr >> 24) != tag) gr = __hip_atomic_load(gws + WS_TOT + (bid & 7) * HID + t, RLX_AGENT);
      if (__all((gr >> 24) == tag)) break;
    }
    const float gx = granule_val(gr);
    *(LDS_AS float*)(L + L_GG + t * 4) = gam * gx;
    float s = sum_xor32(sum_xor16(gx));
    s += __builtin_bit_cast(float, __builtin_amdgcn_update_dpp(0, __builtin_bit_cast(int, s), 0x128, 0xf, 0xf, false));
    s += __builtin_bit_cast(float, __builtin_amdgcn_update_dpp(0, __builtin_bit_cast(int, s), 0x124, 0xf, 0xf, false));
    s += __builtin_bit_cast(float, __builtin_amdgcn_update_dpp(0, __builtin_bit_cast(int, s), 0x4e, 0xf, 0xf, false));
    s += __builtin_bit_cast(float, __builtin_amdgcn_update_dpp(0, __builtin_bit_cast(int, s), 0xb1, 0xf, 0xf, false));
    if (lane == 0) *(LDS_AS float*)(L + L_PS + w * 4) = s;
  }
  bar_lds();
  STAMP(9);
  if (bid == 0 && t == 0) __hip_atomic_store(gws + WS_EPOCH, epoch + 1, RLX_AGENT);
  {
    const f32x4 pa = *(LDS_AS f32x4*)(L + L_PS);
    const f32x2 pb = *(LDS_AS f32x2*)(L + L_PS + 16);
    const float mean = (((pa[0] + pa[1]) + (pa[2] + pa[3])) + (pb[0] + pb[1])) * (1.0f / HID);
    const float inv = GA_INV * __builtin_amdgcn_rcpf(mean + 1e-6f);
    const float s0 = __builtin_fmaf(*(LDS_AS float*)(L + L_GG + (t >> 2) * 4), inv, GA_INV);
    const float s1 = __builtin_fmaf(*(LDS_AS float*)(L + L_GG + (192 + (t >> 2)) * 4), inv, GA_INV);
#pragma unroll
    for (int i = 0; i < 6; i++) {
      const float s = (i & 1) ? s1 : s0;
      u32x4 o;
#pragma unroll
      for (int j = 0; j < 4; j++) { const f32x2 v = w2f[4 * i + j] * s; o[j] = pack_bf16(v.x, v.y); }
      *(LDS_AS u32x4*)(L + L_W2 + (t + i * NT) * 16) = o;
    }
  }
  bar_lds();
  STAMP(10);

  {
    LDS_AS char* ap = L + L_W2 + Hh * 12 * 1024 + w2l;
    LDS_AS char* ew = L + L_E + w * 4096 + lane * 16;
    LDS_AS char* er = L + L_E + (w ^ 1) * 4096 + lane * 16;
    constexpr int PF = 4;
    const rsrc_t ors = make_rsrc(out, X_BYTES);
    s16x8 af[36];
#pragma unroll
    for (int i = 0; i < PF; i++) af[i] = tr_frag(ap + i * 1024, 256);
    {
      const int o16 = w % 6, kh = w / 6;
      LDS_AS char* rp = L + L_W2 + (o16 >> 1) * 24576 + (kh * 192 + 8 * g4 + q4) * 64 + (4 * (o16 & 1) + p4) * 8;
      LDS_AS char* hp = L + L_HREM + (kh * 192 + 8 * g4 + q4) * 8;
      s16x8 ra_a[6], ra_b[6];
#pragma unroll
      for (int k = 0; k < 6; k++) { ra_a[k] = tr_frag(rp + k * 2048, 256); ra_b[k] = tr_frag(hp + k * 256, 32); }
      SB();
      f32x16 acc = zero16;
      f32x4 ra = {0.f, 0.f, 0.f, 0.f};
      float keep[8];
#pragma unroll
      for (int i = 0; i < 36; i++) {
        const int ot = i / 12, k = i % 12;
        if (k == 0) acc = zero16;
        acc = mfma32(af[i], hf[k], acc);
        if (i + PF < 36) af[i + PF] = tr_frag(ap + ((i + PF) / 12) * 24576 + ((i + PF) % 12) * 1024, 256);
        if (i >= 4 && i < 10) ra = mfma16(ra_a[i - 4], ra_b[i - 4], ra);
        if (i == 11 && (lane & 15) < 4) *(LDS_AS f32x4*)(L + L_RP + ((kh * 4 + (lane & 3)) * CH + 16 * o16 + 4 * g4) * 4) = ra;
        SB();
        if (k == 6 && ot > 0) finish_tile(er + ((ot - 1) & 1) * 2048, keep, xb[2 * ot - 2], xb[2 * ot - 1], ors, ooff + (ot - 1) * 64);
        if (i == 19 && t < 48) {
          const int px = t / 12, oc = t % 12;
          const u32x4 xv = xrem;
          f32x4 r[2];
#pragma unroll
          for (int j = 0; j < 2; j++) {
            f32x4 a2 = f32x4{bf_lo(xv[2 * j]), bf_hi(xv[2 * j]), bf_lo(xv[2 * j + 1]), bf_hi(xv[2 * j + 1])};
#pragma unroll
            for (int kq = 0; kq < 4; kq++) a2 += *(LDS_AS f32x4*)(L + L_BP + (kq * CH + oc * 8 + j * 4) * 4);
            a2 += *(LDS_AS f32x4*)(L + L_RP + (px * CH + oc * 8 + j * 4) * 4);
            a2 += *(LDS_AS f32x4*)(L + L_RP + ((4 + px) * CH + oc * 8 + j * 4) * 4);
            r[j] = a2;
          }
          __builtin_amdgcn_raw_buffer_store_b128(u32x4{pack_bf16(r[0][0], r[0][1]), pack_bf16(r[0][2], r[0][3]), pack_bf16(r[1][0], r[1][1]), pack_bf16(r[1][2], r[1][3])}, ors, remo, 0, ST_AUX);
        }
        if (k == 11) {
          f32x4 send[2];
#pragma unroll
          for (int r = 0; r < 8; r++) {
            auto sw2 = __builtin_amdgcn_permlane16_swap(__float_as_uint(acc[r]), __float_as_uint(acc[r + 8]), false, false);
            keep[r] = __uint_as_float(sw2[0]);
            send[r >> 2][r & 3] = __uint_as_float(sw2[1]);
          }
          *(LDS_AS f32x4*)(ew + (ot & 1) * 2048) = send[0];
          *(LDS_AS f32x4*)(ew + (ot & 1) * 2048 + 1024) = send[1];
          SB();
        }
      }
      finish_tile(er, keep, xb[4], xb[5], ors, ooff + 128);
    }
  }
  STAMP(11);
}

extern "C" void kernel_launch(void* const* d_in, const int* in_sizes, int n_in,
                              void* d_out, int out_size, void* d_ws, size_t ws_size,
                              hipStream_t stream) {
  (void)in_sizes; (void)n_in; (void)out_size; (void)ws_size;
  (void)hipFuncSetAttribute((const void*)slak_block_kernel, hipFuncAttributeMaxDynamicSharedMemorySize, LDS_BYTES);
  slak_block_kernel<<<256, NT, LDS_BYTES, stream>>>(
      (const unsigned short*)d_in[0], (const unsigned short*)d_in[1], (const unsigned short*)d_in[2],
      (const unsigned short*)d_in[3], (const unsigned short*)d_in[4], (const unsigned short*)d_in[5],
      (unsigned short*)d_out, (u64*)d_ws);
}
```

```cpp
#include <hip/hip_runtime.h>
#include <stdint.h>
#define DIAG
#define NSTAMP 12

typedef unsigned long long u64;
typedef unsigned u32;
typedef float f32x2 __attribute__((ext_vector_type(2)));
typedef float f32x4 __attribute__((ext_vector_type(4)));
typedef float f32x16 __attribute__((ext_vector_type(16)));
typedef short s16x4 __attribute__((ext_vector_type(4)));
typedef short s16x8 __attribute__((ext_vector_type(8)));
typedef __bf16 bf16x2 __attribute__((ext_vector_type(2)));
typedef u32 u32x2 __attribute__((ext_vector_type(2)));
typedef u32 u32x4 __attribute__((ext_vector_type(4)));
#define LDS_AS __attribute__((address_space(3)))
typedef __attribute__((address_space(1))) u64 gu64;
#define RLX_AGENT __ATOMIC_RELAXED, __HIP_MEMORY_SCOPE_AGENT
#define DI __device__ __forceinline__

constexpr int IMG = 224, CH = 96, HID = 384, NT = 768;
constexpr u32 X_BYTES = IMG * IMG * CH * 2;

constexpr float GA = 0.4686686930532063f, GK = 4.91222953926424f;
constexpr float GA_INV = 2.1337034344781243f, GA_INV2 = 4.552690346303743f;

constexpr u32 L_X = 0, X_REC = 832;
constexpr u32 L_Y = 0, L_YR = 36864;
constexpr u32 L_W1L = 37632;
constexpr u32 L_SCR = 74496, SCR_W = 2304;
constexpr u32 L_SS = 102144;
constexpr u32 L_V = 83200, V_ROW = 416;
constexpr u32 L_T = 83200, T_ROW = 288;
constexpr u32 L_W1E = 123136;
constexpr u32 L_HREM = 160000;
constexpr u32 L_W2 = 0;
constexpr u32 L_E = 74496;
constexpr u32 L_B = 163072;
constexpr u32 L_RP = 123648, L_BP = 126720, L_GG = 128256, L_PS = 129792, L_RED = 129856;
constexpr u32 LDS_BYTES = 163840;

constexpr int WS_EPOCH = 0, WS_TOT = 64, WS_PART = 64 + 8 * HID;
#define CODE_LINES 160
constexpr int SPIN_CAP = 1 << 20;

#define SB() __builtin_amdgcn_sched_barrier(0)
DI void bar_lds() { asm volatile("s_waitcnt lgkmcnt(0)\n\ts_barrier" ::: "memory"); }
DI void bar_all() { asm volatile("s_waitcnt vmcnt(0) lgkmcnt(0)\n\ts_barrier" ::: "memory"); }
#define LD_AUX 0
#define ST_AUX 16
typedef __amdgpu_buffer_rsrc_t rsrc_t;
DI rsrc_t make_rsrc(const void* p, u32 bytes) { return __builtin_amdgcn_make_buffer_rsrc((void*)p, 0, bytes, 0x00020000); }
DI u32x4 ld128(rsrc_t r, u32 off) { return __builtin_amdgcn_raw_buffer_load_b128(r, off, 0, LD_AUX); }
DI u32 pack_bf16(float a, float b) { bf16x2 v = __builtin_convertvector(f32x2{a, b}, bf16x2); return __builtin_bit_cast(u32, v); }
DI float bf_lo(u32 v) { return __uint_as_float(v << 16); }
DI float bf_hi(u32 v) { return __uint_as_float(v & 0xffff0000u); }
DI u32 perm_lo(u32 a, u32 b) { return __builtin_amdgcn_perm(b, a, 0x05040100u); }
DI u32 perm_hi(u32 a, u32 b) { return __builtin_amdgcn_perm(b, a, 0x07060302u); }
DI s16x4 tr_read(LDS_AS char* p) { return __builtin_amdgcn_ds_read_tr16_b64_v4i16((LDS_AS s16x4*)p); }
DI s16x8 tr_frag(LDS_AS char* p, int step) {
  s16x4 a = tr_read(p), b = tr_read(p + step);
  return __builtin_shufflevector(a, b, 0, 1, 2, 3, 4, 5, 6, 7);
}
DI f32x16 mfma32(s16x8 a, s16x8 b, f32x16 c) { return __builtin_amdgcn_mfma_f32_32x32x16_bf16(a, b, c, 0, 0, 0); }
DI f32x4 mfma16(s16x8 a, s16x8 b, f32x4 c) { return __builtin_amdgcn_mfma_f32_16x16x32_bf16(a, b, c, 0, 0, 0); }
DI float sum_xor32(float v) { auto r = __builtin_amdgcn_permlane32_swap(__float_as_uint(v), __float_as_uint(v), false, false); return __uint_as_float(r[0]) + __uint_as_float(r[1]); }
DI float sum_xor16(float v) { auto r = __builtin_amdgcn_permlane16_swap(__float_as_uint(v), __float_as_uint(v), false, false); return __uint_as_float(r[0]) + __uint_as_float(r[1]); }
DI u64 granule(u64 tag, float v) { return (tag << 24) | (u64)(((__float_as_uint(v) + 0x40u) >> 7) & 0xffffffu); }
DI float granule_val(u64 g) { return __uint_as_float(((u32)g & 0xffffffu) << 7); }
DI int pos_channel(int p) { return 8 * ((p % 24) >> 1) + 2 * (p / 24) + (p & 1); }

DI f32x2 gelu2(f32x2 x) {
  f32x2 t = __builtin_elementwise_fma(x, x, f32x2{GK, GK});
  f32x2 z = t * x;
  f32x2 e = {__builtin_amdgcn_exp2f(-z.x), __builtin_amdgcn_exp2f(-z.y)};
  f32x2 d = e + 1.0f;
  f32x2 r = {__builtin_amdgcn_rcpf(d.x), __builtin_amdgcn_rcpf(d.y)};
  return x * r;
}

DI void layer_norm_unit(LDS_AS char* L, int lane, u32 vcol, u32 dst, u32 dst_step) {
  const int g = lane >> 4, q = (lane >> 2) & 3;
  LDS_AS char* p0 = L + L_V + (8 * g + 4 * (g & 1) + q) * V_ROW + vcol;
  LDS_AS char* p1 = L + L_V + (8 * g + 4 * ((g & 1) ^ 1) + q) * V_ROW + vcol;
  u32 v[12];
#pragma unroll
  for (int c = 0; c < 3; c++) {
    u32x2 a = __builtin_bit_cast(u32x2, tr_read(p0 + c * 32 * V_ROW));
    u32x2 b = __builtin_bit_cast(u32x2, tr_read(p1 + c * 32 * V_ROW));
    v[4 * c] = a.x; v[4 * c + 1] = a.y; v[4 * c + 2] = b.x; v[4 * c + 3] = b.y;
  }
  f32x2 f[12];
  f32x2 s = {0.f, 0.f}, ss = {0.f, 0.f};
#pragma unroll
  for (int i = 0; i < 12; i++) { f[i] = f32x2{bf_lo(v[i]), bf_hi(v[i])}; s += f[i]; ss = __builtin_elementwise_fma(f[i], f[i], ss); }
  const float sum = sum_xor32(sum_xor16(s.x + s.y)), sq = sum_xor32(sum_xor16(ss.x + ss.y));
  const float mu = sum * (1.0f / CH);
  const float var = fmaxf(__builtin_fmaf(-mu, mu, sq * (1.0f / CH)), 0.f);
  const float rs = GA * __builtin_amdgcn_rsqf(var + 1e-5f);
  const float off = -mu * rs;
#pragma unroll
  for (int c = 0; c < 3; c++) {
    u32x4 o;
#pragma unroll
    for (int i = 0; i < 4; i++) { f32x2 y = __builtin_elementwise_fma(f[4 * c + i], f32x2{rs, rs}, f32x2{off, off}); o[i] = pack_bf16(y.x, y.y); }
    *(LDS_AS u32x4*)(L + dst + c * dst_step) = o;
  }
}

DI float add1(float a, float b) { float r; asm("v_add_f32 %0, %1, %2" : "=v"(r) : "v"(a), "v"(b)); return r; }

DI void finish_tile(LDS_AS char* er, const float* keep, f32x4 xa, f32x4 xc, rsrc_t ors, u32 off) {
  bar_lds();
  const f32x4 pa = *(LDS_AS f32x4*)(er), pc = *(LDS_AS f32x4*)(er + 1024);
  float v[8];
#pragma unroll
  for (int i = 0; i < 4; i++) { v[i] = add1(add1(keep[i], xa[i]), pa[i]); v[4 + i] = add1(add1(keep[4 + i], xc[i]), pc[i]); }
  __builtin_amdgcn_raw_buffer_store_b128(u32x4{pack_bf16(v[0], v[1]), pack_bf16(v[2], v[3]), pack_bf16(v[4], v[5]), pack_bf16(v[6], v[7])}, ors, off, 0, ST_AUX);
  SB();
}

#define STAMP(i) do { SB(); st_r[i] = __builtin_amdgcn_s_memrealtime(); st_c[i] = __builtin_amdgcn_s_memtime(); SB(); } while (0)

__global__ void __launch_bounds__(NT, 3)
slak_block_kernel(const unsigned short* __restrict__ x, const unsigned short* __restrict__ dw, const unsigned short* __restrict__ w1,
                  const unsigned short* __restrict__ w2, const unsigned short* __restrict__ grn_g, const unsigned short* __restrict__ grn_b,
                  unsigned short* __restrict__ out, u64* ws) {
  extern __shared__ __attribute__((aligned(16))) char smem[];
  LDS_AS char* const L = (LDS_AS char*)smem;
  const int t = threadIdx.x, lane = t & 63, bid = blockIdx.x;
  const int w = __builtin_amdgcn_readfirstlane(t >> 6);
  u64* stamps = ws + (1 << 20);
  u64 st_r[NSTAMP], st_c[NSTAMP];
  STAMP(0);
  u32 code_pf = 0;
  if (t < CODE_LINES / 4) {
    const char* pc = (const char*)(__builtin_amdgcn_s_getpc() & ~255ull);
    code_pf = *(const u32*)(pc + (((bid >> 3) & 3) + 4 * t) * 128);
  }
  const int xcd = bid & 7, idx = bid >> 3;
  const int y0 = 14 * ((xcd >> 1) * 4 + (idx >> 3)), x0 = 14 * ((xcd & 1) * 8 + (idx & 7));
  const rsrc_t xrs = make_rsrc(x, X_BYTES);
  gu64* const gws = (gu64*)ws;

  u32x4 xin[2][4];
  u32 xdst[2];
  auto load_item = [&](int it) {
    const u32 item = t + it * NT, P = (item * 5462) >> 16, o = item - P * 12, Y = (P * 6554) >> 16, X = P - Y * 10;
    const u32 gy = y0 - 3 + 2 * Y, gx = x0 - 3 + 2 * X;
    u32 base = (u32)(__mul24((int)gy, IMG * CH * 2) + __mul24((int)gx, CH * 2)) + o * 16;
    asm volatile("" : "+v"(base));
    const bool live = item < 1200;
    const bool yk[2] = {live && gy < (u32)IMG, live && gy + 1 < (u32)IMG}, xk[2] = {gx < (u32)IMG, gx + 1 < (u32)IMG};
    xdst[it] = L_X + P * X_REC + o * 16;
#pragma unroll
    for (int k = 0; k < 4; k++) {
      const u32 off = (yk[k >> 1] && xk[k & 1]) ? base + (k >> 1) * (IMG * CH * 2) : 0x80000000u;
      xin[it][k] = ld128(xrs, off + (k & 1) * (CH * 2));
    }
  };
  auto store_item = [&](int it) {
#pragma unroll
    for (int j = 0; j < 4; j++)
      *(LDS_AS u32x4*)(L + xdst[it] + j * 192) = u32x4{perm_lo(xin[it][0][j], xin[it][1][j]), perm_lo(xin[it][2][j], xin[it][3][j]),
                                                       perm_hi(xin[it][0][j], xin[it][1][j]), perm_hi(xin[it][2][j], xin[it][3][j])};
  };
  const int cg = w >> 1, xh = w & 1;
  if (w >= 9) {
    const u32 tb = t - 576, r = (tb * 2731) >> 16, c4 = tb - r * 24;
    const rsrc_t drs = make_rsrc(dw, 49 * CH * 2);
    u32x2 dv[7];
#pragma unroll
    for (int j = 0; j < 7; j++) dv[j] = __builtin_amdgcn_raw_buffer_load_b64(drs, r * (7 * CH * 2) + c4 * 8 + j * (CH * 2), 0, LD_AUX);
    load_item(0);
    if (tb < 168) {
#pragma unroll
      for (int h = 0; h < 2; h++) {
        LDS_AS char* d = L + L_T + (24 * (2 * (c4 & 1) + h) + 2 * (c4 >> 1)) * T_ROW + (r + 1) * 16;
        *(LDS_AS u32x4*)(d) = u32x4{perm_lo(dv[0][h], dv[1][h]), perm_lo(dv[2][h], dv[3][h]), perm_lo(dv[4][h], dv[5][h]), dv[6][h] & 0xffffu};
        *(LDS_AS u32x4*)(d + 144) = u32x4{dv[0][h] << 16, perm_lo(dv[1][h], dv[2][h]), perm_lo(dv[3][h], dv[4][h]), perm_lo(dv[5][h], dv[6][h])};
        *(LDS_AS u32x4*)(d + T_ROW) = u32x4{perm_hi(dv[0][h], dv[1][h]), perm_hi(dv[2][h], dv[3][h]), perm_hi(dv[4][h], dv[5][h]), dv[6][h] >> 16};
        *(LDS_AS u32x4*)(d + T_ROW + 144) = u32x4{dv[0][h] & 0xffff0000u, perm_hi(dv[1][h], dv[2][h]), perm_hi(dv[3][h], dv[4][h]), perm_hi(dv[5][h], dv[6][h])};
      }
    }
  } else {
    load_item(0);
    if (t < 384) *(LDS_AS u32x4*)(L + L_T + (t >> 2) * T_ROW + ((t >> 1) & 1) * 144 + (t & 1) * 128) = u32x4{0, 0, 0, 0};
  }
  STAMP(1);
  bar_lds();
  STAMP(2);
  s16x4 wa[4][4];
  u32x4 w1r[6];
  u64 epoch;
  u32x4 aux16;
  {
    LDS_AS char* p = L + L_T + (16 * cg + (lane >> 2)) * T_ROW + (lane & 1) * 144 + (1 - ((lane >> 1) & 1)) * 16;
    u32x4 wr[8];
#pragma unroll
    for (int r = 0; r < 8; r++) wr[r] = *(LDS_AS u32x4*)(p + r * 16);
    if (w < 7) load_item(1);
    const int k = 16 * (w % 6) + (lane >> 2), rho = k ^ (((k >> 3) & 1) << 2);
    const u32 w1o = pos_channel(rho) * (HID * 2) + (w / 6) * 384 + (lane & 3) * 16;
    const rsrc_t w1rs = make_rsrc(w1, CH * HID * 2);
#pragma unroll
    for (int u = 0; u < 6; u++) w1r[u] = ld128(w1rs, w1o + u * 64);
    epoch = __hip_atomic_load(gws + WS_EPOCH, RLX_AGENT);
    aux16 = ld128(w == 7 ? make_rsrc(grn_b, HID * 2) : make_rsrc(w2, w == 8 ? HID * CH * 2 : 0),
                  w == 7 ? lane * 16 : (lane < 18 ? (idx * 18 + lane) * 128 : 0x80000000u));
#pragma unroll
    for (int i = 0; i < 16; i++) {
      u32x2 v = {wr[2 * (i >> 2)][i & 3], wr[2 * (i >> 2) + 1][i & 3]};
      asm volatile("" : "+v"(v));
      wa[i >> 2][i & 3] = __builtin_bit_cast(s16x4, v);
    }
  }
  STAMP(3);
  store_item(0);
  if (t < 432) store_item(1);
  bar_lds();
  STAMP(4);

  {
    LDS_AS char* xb = L + L_X + (4 * xh + (lane & 3)) * X_REC + (16 * cg + (lane >> 2)) * 8;
    LDS_AS char* xb2 = xb + 70 * X_REC;
    LDS_AS char* vb = L + L_V + (16 * cg + (lane >> 2)) * V_ROW + (4 * xh + (lane & 3)) * 8;
    const bool vok = 4 * xh + (lane & 3) < 7;
    f32x4 acc[7];
#pragma unroll
    for (int i = 0; i < 7; i++) acc[i] = f32x4{0.f, 0.f, 0.f, 0.f};
    s16x4 d[10][4];
    auto load_row = [&](int Y) {
#pragma unroll
      for (int sx = 0; sx < 4; sx++)
        d[Y][sx] = Y < 7 ? *(volatile LDS_AS s16x4*)(xb + (Y * 10 + sx) * X_REC) : *(volatile LDS_AS s16x4*)(xb2 + ((Y - 7) * 10 + sx) * X_REC);
    };
    load_row(0); load_row(1); load_row(2);
    SB();
#pragma unroll
    for (int Y = 0; Y < 10; Y++) {
      if (Y + 3 < 10) load_row(Y + 3);
#pragma unroll
      for (int sx = 0; sx < 4; sx++) {
#pragma unroll
        for (int sy = 3; sy >= 0; sy--) {
          const int Yo = Y - sy;
          if (Yo >= 0 && Yo < 7) acc[Yo] = __builtin_amdgcn_mfma_f32_4x4x4bf16_1k(wa[sy][sx], d[Y][sx], acc[Yo], 0, 0, 0);
        }
      }
      if (Y >= 4) {
        const f32x4 r = acc[Y - 4];
        if (vok) *(LDS_AS u32x2*)(vb + (Y - 4) * 56) = u32x2{pack_bf16(r[0], r[1]), pack_bf16(r[2], r[3])};
      }
      SB();
    }
    if (vok) *(LDS_AS u32x2*)(vb + 6 * 56) = u32x2{pack_bf16(acc[6][0], acc[6][1]), pack_bf16(acc[6][2], acc[6][3])};
  }
  const u32 w1d = (w / 6) * 3 * 6144 + (w % 6) * 1024 + lane * 16;
#pragma unroll
  for (int u = 0; u < 3; u++) *(LDS_AS u32x4*)(L + L_W1E + w1d + u * 6144) = w1r[u];
  if (w == 7 && lane < 48) *(LDS_AS u32x4*)(L + L_B + lane * 16) = aux16;
  asm volatile("" :: "v"(aux16), "v"(code_pf));
  epoch = ((u64)(u32)__builtin_amdgcn_readfirstlane((int)(epoch >> 32)) << 32) | (u32)__builtin_amdgcn_readfirstlane((int)epoch);
  bar_lds();
  STAMP(5);
#pragma unroll
  for (int u = 0; u < 3; u++) *(LDS_AS u32x4*)(L + L_W1L + w1d + u * 6144) = w1r[3 + u];

  const int T = w >> 1, Hh = w & 1;
  layer_norm_unit(L, lane, (4 * w + (lane & 3)) * 8, L_Y + T * 6144 + ((lane >> 4) * 32 + 16 * Hh + (lane & 15)) * 16, 2048);
  if (w == 11) layer_norm_unit(L, lane, 48 * 8, L_YR + ((lane >> 4) * 4 + (lane & 3)) * 16, 256);
  bar_lds();
  STAMP(6);

  const int g4 = lane >> 4, q4 = (lane >> 2) & 3, p4 = lane & 3;
  s16x8 hf[12];
  u32x4 w2r[6], xr[3], xrem;
  float gam;
  const rsrc_t w2rs = make_rsrc(w2, HID * CH * 2);
  const u32 w2o = (t >> 2) * (CH * 2) + (t & 3) * 16;
  const u32 remo = t < 48 ? (u32)(((y0 + 12 + t / 24) * IMG + x0 + 12) * (CH * 2) + (t % 24) * 16) : 0x80000000u;
  const int pidx = 4 * w + ((lane >> 2) & 3);
  const u32 ooff = (u32)(((y0 + 2 * (pidx / 7) + ((lane >> 1) & 1)) * IMG + x0 + 2 * (pidx % 7) + (lane & 1)) * (CH * 2) + g4 * 16);
  const f32x16 zero16 = {0.f, 0.f, 0.f, 0.f, 0.f, 0.f, 0.f, 0.f, 0.f, 0.f, 0.f, 0.f, 0.f, 0.f, 0.f, 0.f};
  {
    const u32 wl = (8 * (g4 >> 1) + q4) * 64 + (g4 & 1) * 32 + p4 * 8 + Hh * 3 * 6144;
    LDS_AS char* wE = L + L_W1E + wl;
    LDS_AS char* wL = L + L_W1L + wl;
    LDS_AS char* sw = L + L_SCR + w * SCR_W + (lane & 31) * 72 + (lane >> 5) * 8;
    LDS_AS char* sr = L + L_SCR + w * SCR_W + (8 * (g4 >> 1) + q4) * 72 + (4 * ((g4 & 1) ^ Hh) + p4) * 8;
    LDS_AS char* ssw = L + L_SS + ((2 * T + (lane >> 5)) * HID + Hh * 192 + (lane & 31)) * 4;
    s16x8 yf[6], wf[6];
    f32x16 acc = zero16;
    {
      LDS_AS char* wp = (T < 3 ? wE : wL) + (T % 3) * 6144;
      s16x8 yr[6], wq[6];
#pragma unroll
      for (int k = 0; k < 6; k++) { yr[k] = *(LDS_AS s16x8*)(L + L_YR + ((2 * k + (lane >> 5)) * 4 + (lane & 3)) * 16); wq[k] = tr_frag(wp + k * 1024, 256); }
#pragma unroll
      for (int k = 0; k < 6; k++) { yf[k] = *(LDS_AS s16x8*)(L + L_Y + T * 6144 + k * 1024 + lane * 16); wf[k] = tr_frag(wE + k * 1024, 256); }
      SB();
      f32x16 ra = zero16;
#pragma unroll
      for (int k = 0; k < 6; k++) ra = mfma32(yr[k], wq[k], ra);
#pragma unroll
      for (int k = 0; k < 6; k++) acc = mfma32(yf[k], wf[k], acc);
#pragma unroll
      for (int k = 0; k < 3; k++) wf[k] = tr_frag(wE + 6144 + k * 1024, 256);
      SB();
      const f32x2 a = gelu2(f32x2{ra[0], ra[1]}), b = gelu2(f32x2{ra[2], ra[3]});
      const f32x2 sq = __builtin_elementwise_fma(a, a, b * b);
      if (lane < 32) {
        const int hc = Hh * 192 + T * 32 + lane;
        *(LDS_AS u32x2*)(L + L_HREM + hc * 8) = u32x2{pack_bf16(a.x, a.y), pack_bf16(b.x, b.y)};
        *(LDS_AS float*)(L + L_SS + (12 * HID + hc) * 4) = sq.x + sq.y;
      }
      SB();
    }
#pragma unroll
    for (int s = 0; s < 6; s++) {
      LDS_AS char* wp = (s + 1 < 3 ? wE : wL) + ((s + 1) % 3) * 6144;
      LDS_AS char* wp2 = (s + 2 < 3 ? wE : wL) + ((s + 2) % 3) * 6144;
      if (s < 5) {
#pragma unroll
        for (int k = 3; k < 6; k++) wf[k] = tr_frag(wp + k * 1024, 256);
      }
      f32x2 xv[8], z[8];
#pragma unroll
      for (int i = 0; i < 8; i++) { xv[i] = f32x2{acc[2 * i], acc[2 * i + 1]}; z[i] = __builtin_elementwise_fma(xv[i], xv[i], f32x2{GK, GK}); }
      SB();
#pragma unroll
      for (int i = 0; i < 8; i++) z[i] *= xv[i];
      SB();
      f32x16 nxt = zero16;
#pragma unroll
      for (int i = 0; i < 16; i++) {
        if (s < 5 && i % 3 == 0 && i < 9) { nxt = mfma32(yf[i / 3], wf[i / 3], nxt); SB(); }
        if (s == 5 && i % 3 == 0) { w2r[i / 3] = ld128(w2rs, w2o + ((i / 3) & 1) * (192 * CH * 2) + (i / 6) * 64); SB(); }
        z[i >> 1][i & 1] = __builtin_amdgcn_exp2f(-z[i >> 1][i & 1]);
        if (i % 3 == 2 && (i < 9 || s == 5)) SB();
      }
      if (s < 4) {
#pragma unroll
        for (int k = 0; k < 3; k++) wf[k] = tr_frag(wp2 + k * 1024, 256);
      }
      SB();
#pragma unroll
      for (int i = 0; i < 8; i++) z[i] += 1.0f;
      SB();
#pragma unroll
      for (int i = 0; i < 16; i++) {
        if (s < 5 && i % 3 == 0 && i < 9) { nxt = mfma32(yf[3 + i / 3], wf[3 + i / 3], nxt); SB(); }
        if (s == 5 && i % 3 == 0 && i < 9) { xr[i / 3] = ld128(xrs, ooff + (i / 3) * 64); SB(); }
        if (s == 5 && i == 9) { xrem = ld128(xrs, remo); gam = bf_lo(grn_g[t < HID ? t : 0]); SB(); }
        z[i >> 1][i & 1] = __builtin_amdgcn_rcpf(z[i >> 1][i & 1]);
        if (i % 3 == 2 && i < 9) SB();
      }
      SB();
#pragma unroll
      for (int i = 0; i < 8; i++) z[i] *= xv[i];
      SB();
      f32x2 ssa = {0.f, 0.f}, ssb = {0.f, 0.f};
#pragma unroll
      for (int Q = 0; Q < 4; Q++) {
        ssa = __builtin_elementwise_fma(z[2 * Q], z[2 * Q], ssa);
        ssb = __builtin_elementwise_fma(z[2 * Q + 1], z[2 * Q + 1], ssb);
        *(volatile LDS_AS u32x2*)(sw + Q * 16) = u32x2{pack_bf16(z[2 * Q].x, z[2 * Q].y), pack_bf16(z[2 * Q + 1].x, z[2 * Q + 1].y)};
      }
      hf[2 * s] = tr_frag(sr, 4 * 72);
      hf[2 * s + 1] = tr_frag(sr + 16 * 72, 4 * 72);
      ssa += ssb;
      *(LDS_AS float*)(ssw + s * 128) = ssa.x + ssa.y;
      acc = nxt;
      SB();
    }
  }
  bar_lds();
  STAMP(7);

  const u64 tag = (epoch + 1) & ((1ull << 40) - 1);
  if (t < HID) {
    float s = 0.f;
#pragma unroll
    for (int i = 0; i < 13; i++) s += *(LDS_AS float*)(L + L_SS + (i * HID + t) * 4);
    __hip_atomic_store(gws + WS_PART + ((t >> 4) * 256 + bid) * 16 + (t & 15), granule(tag, s), RLX_AGENT);
  }

  if (bid < 24) {
    u64 gr[6];
#pragma unroll
    for (int i = 0; i < 6; i++) gr[i] = (t + i * NT < 4096) ? ~(tag << 24) : (tag << 24);
    for (int spin = 0; spin < SPIN_CAP; spin++) {
      bool ok = true;
#pragma unroll
      for (int i = 0; i < 6; i++) if ((gr[i] >> 24) != tag) gr[i] = __hip_atomic_load(gws + WS_PART + bid * 4096 + t + i * NT, RLX_AGENT);
#pragma unroll
      for (int i = 0; i < 6; i++) ok &= (gr[i] >> 24) == tag;
      if (__all(ok)) break;
    }
    float s = 0.f;
#pragma unroll
    for (int i = 0; i < 6; i++) s += granule_val(gr[i]);
    s = sum_xor32(sum_xor16(s));
    if (lane < 16) *(LDS_AS float*)(L + L_RED + (w * 16 + lane) * 4) = s;
    bar_lds();
    if (t < 128) {
      float tot = 0.f;
#pragma unroll
      for (int i = 0; i < 12; i++) tot += *(LDS_AS float*)(L + L_RED + (i * 16 + (t & 15)) * 4);
      const float gx = __builtin_amdgcn_sqrtf(__builtin_fmaf(tot, GA_INV2, 1e-6f));
      __hip_atomic_store(gws + WS_TOT + (t >> 4) * HID + bid * 16 + (t & 15), granule(tag, gx), RLX_AGENT);
    }
  }

#pragma unroll
  for (int i = 0; i < 6; i++) *(LDS_AS u32x4*)(L + L_W2 + (t + i * NT) * 16) = w2r[i];
  bar_lds();
  const int pq = 4 * (g4 & 1) + p4;
  const u32 w2l = (8 * (g4 >> 1) + q4) * 64 + (4 * (pq & 1) + 2 * (pq >> 2) + ((pq >> 1) & 1)) * 8;
  {
    LDS_AS char* ap = L + L_W2 + (w % 3) * 24576 + (w / 3) * 6144 + w2l;
    f32x16 acc = zero16;
    s16x8 af[6], bfr[6];
#pragma unroll
    for (int k = 0; k < 6; k++) { af[k] = tr_frag(ap + k * 1024, 256); bfr[k] = *(LDS_AS s16x8*)(L + L_B + (6 * (w / 3) + k) * 32 + (lane >> 5) * 16); }
    SB();
#pragma unroll
    for (int k = 0; k < 6; k++) acc = mfma32(af[k], bfr[k], acc);
    if ((lane & 31) == 0) {
      LDS_AS char* d = L + L_BP + ((w / 3) * CH + (w % 3) * 32 + (lane >> 5) * 16) * 4;
#pragma unroll
      for (int i = 0; i < 4; i++) *(LDS_AS f32x4*)(d + i * 16) = f32x4{acc[4 * i], acc[4 * i + 1], acc[4 * i + 2], acc[4 * i + 3]};
    }
  }
  bar_lds();
  f32x4 xb[6];
#pragma unroll
  for (int i = 0; i < 6; i++) {
    f32x4 a = f32x4{bf_lo(xr[i >> 1][2 * (i & 1)]), bf_hi(xr[i >> 1][2 * (i & 1)]), bf_lo(xr[i >> 1][2 * (i & 1) + 1]), bf_hi(xr[i >> 1][2 * (i & 1) + 1])};
#pragma unroll
    for (int k = 0; k < 4; k++) a += *(LDS_AS f32x4*)(L + L_BP + (k * CH + (i >> 1) * 32 + g4 * 8 + (i & 1) * 4) * 4);
    asm volatile("" : "+v"(a));
    xb[i] = a;
    SB();
  }
  f32x2 w2f[24];
#pragma unroll
  for (int i = 0; i < 24; i++) w2f[i] = f32x2{bf_lo(w2r[i >> 2][i & 3]), bf_hi(w2r[i >> 2][i & 3])};
#pragma unroll
  for (int i = 0; i < 24; i++) asm volatile("" : "+v"(w2f[i]));
  STAMP(8);

  if (t < HID) {
    u64 gr = ~(tag << 24);
    for (int spin = 0; spin < SPIN_CAP; spin++) {
      if ((gr >> 24) != tag) gr = __hip_atomic_load(gws + WS_TOT + (bid & 7) * HID + t, RLX_AGENT);
      if (__all((gr >> 24) == tag)) break;
    }
    const float gx = granule_val(gr);
    *(LDS_AS float*)(L + L_GG + t * 4) = gam * gx;
    float s = sum_xor32(sum_xor16(gx));
    s += __builtin_bit_cast(float, __builtin_amdgcn_update_dpp(0, __builtin_bit_cast(int, s), 0x128, 0xf, 0xf, false));
    s += __builtin_bit_cast(float, __builtin_amdgcn_update_dpp(0, __builtin_bit_cast(int, s), 0x124, 0xf, 0xf, false));
    s += __builtin_bit_cast(float, __builtin_amdgcn_update_dpp(0, __builtin_bit_cast(int, s), 0x4e, 0xf, 0xf, false));
    s += __builtin_bit_cast(float, __builtin_amdgcn_update_dpp(0, __builtin_bit_cast(int, s), 0xb1, 0xf, 0xf, false));
    if (lane == 0) *(LDS_AS float*)(L + L_PS + w * 4) = s;
  }
  bar_lds();
  STAMP(9);
  if (bid == 0 && t == 0) __hip_atomic_store(gws + WS_EPOCH, epoch + 1, RLX_AGENT);
  {
    const f32x4 pa = *(LDS_AS f32x4*)(L + L_PS);
    const f32x2 pb = *(LDS_AS f32x2*)(L + L_PS + 16);
    const float mean = (((pa[0] + pa[1]) + (pa[2] + pa[3])) + (pb[0] + pb[1])) * (1.0f / HID);
    const float inv = GA_INV * __builtin_amdgcn_rcpf(mean + 1e-6f);
    const float s0 = __builtin_fmaf(*(LDS_AS float*)(L + L_GG + (t >> 2) * 4), inv, GA_INV);
    const float s1 = __builtin_fmaf(*(LDS_AS float*)(L + L_GG + (192 + (t >> 2)) * 4), inv, GA_INV);
#pragma unroll
    for (int i = 0; i < 6; i++) {
      const float s = (i & 1) ? s1 : s0;
      u32x4 o;
#pragma unroll
      for (int j = 0; j < 4; j++) { const f32x2 v = w2f[4 * i + j] * s; o[j] = pack_bf16(v.x, v.y); }
      *(LDS_AS u32x4*)(L + L_W2 + (t + i * NT) * 16) = o;
    }
  }
  bar_lds();
  STAMP(10);

  {
    LDS_AS char* ap = L + L_W2 + Hh * 12 * 1024 + w2l;
    LDS_AS char* ew = L + L_E + w * 4096 + lane * 16;
    LDS_AS char* er = L + L_E + (w ^ 1) * 4096 + lane * 16;
    constexpr int PF = 4;
    const rsrc_t ors = make_rsrc(out, X_BYTES);
    s16x8 af[36];
#pragma unroll
    for (int i = 0; i < PF; i++) af[i] = tr_frag(ap + i * 1024, 256);
    {
      const int o16 = w % 6, kh = w / 6;
      LDS_AS char* rp = L + L_W2 + (o16 >> 1) * 24576 + (kh * 192 + 8 * g4 + q4) * 64 + (4 * (o16 & 1) + p4) * 8;
      LDS_AS char* hp = L + L_HREM + (kh * 192 + 8 * g4 + q4) * 8;
      s16x8 ra_a[6], ra_b[6];
#pragma unroll
      for (int k = 0; k < 6; k++) { ra_a[k] = tr_frag(rp + k * 2048, 256); ra_b[k] = tr_frag(hp + k * 256, 32); }
      SB();
      f32x16 acc = zero16;
      f32x4 ra = {0.f, 0.f, 0.f, 0.f};
      float keep[8];
#pragma unroll
      for (int i = 0; i < 36; i++) {
        const int ot = i / 12, k = i % 12;
        if (k == 0) acc = zero16;
        acc = mfma32(af[i], hf[k], acc);
        if (i + PF < 36) af[i + PF] = tr_frag(ap + ((i + PF) / 12) * 24576 + ((i + PF) % 12) * 1024, 256);
        if (i >= 4 && i < 10) ra = mfma16(ra_a[i - 4], ra_b[i - 4], ra);
        if (i == 11 && (lane & 15) < 4) *(LDS_AS f32x4*)(L + L_RP + ((kh * 4 + (lane & 3)) * CH + 16 * o16 + 4 * g4) * 4) = ra;
        SB();
        if (k == 6 && ot > 0) finish_tile(er + ((ot - 1) & 1) * 2048, keep, xb[2 * ot - 2], xb[2 * ot - 1], ors, ooff + (ot - 1) * 64);
        if (i == 19 && t < 48) {
          const int px = t / 12, oc = t % 12;
          const u32x4 xv = xrem;
          f32x4 r[2];
#pragma unroll
          for (int j = 0; j < 2; j++) {
            f32x4 a2 = f32x4{bf_lo(xv[2 * j]), bf_hi(xv[2 * j]), bf_lo(xv[2 * j + 1]), bf_hi(xv[2 * j + 1])};
#pragma unroll
            for (int kq = 0; kq < 4; kq++) a2 += *(LDS_AS f32x4*)(L + L_BP + (kq * CH + oc * 8 + j * 4) * 4);
            a2 += *(LDS_AS f32x4*)(L + L_RP + (px * CH + oc * 8 + j * 4) * 4);
            a2 += *(LDS_AS f32x4*)(L + L_RP + ((4 + px) * CH + oc * 8 + j * 4) * 4);
            r[j] = a2;
          }
          __builtin_amdgcn_raw_buffer_store_b128(u32x4{pack_bf16(r[0][0], r[0][1]), pack_bf16(r[0][2], r[0][3]), pack_bf16(r[1][0], r[1][1]), pack_bf16(r[1][2], r[1][3])}, ors, remo, 0, ST_AUX);
        }
        if (k == 11) {
          f32x4 send[2];
#pragma unroll
          for (int r = 0; r < 8; r++) {
            auto sw2 = __builtin_amdgcn_permlane16_swap(__float_as_uint(acc[r]), __float_as_uint(acc[r + 8]), false, false);
            keep[r] = __uint_as_float(sw2[0]);
            send[r >> 2][r & 3] = __uint_as_float(sw2[1]);
          }
          *(LDS_AS f32x4*)(ew + (ot & 1) * 2048) = send[0];
          *(LDS_AS f32x4*)(ew + (ot & 1) * 2048 + 1024) = send[1];
          SB();
        }
      }
      finish_tile(er, keep, xb[4], xb[5], ors, ooff + 128);
    }
  }
  STAMP(11);
  { const u64 d = st_r[0] - st_r[0]; const u64 e = __builtin_amdgcn_s_memrealtime() + d;
    if (w == 0) while (__builtin_amdgcn_s_memrealtime() < e) __builtin_amdgcn_s_sleep(1); (void)stamps; }
}

extern "C" void kernel_launch(void* const* d_in, const int* in_sizes, int n_in,
                              void* d_out, int out_size, void* d_ws, size_t ws_size,
                              hipStream_t stream) {
  (void)in_sizes; (void)n_in; (void)out_size; (void)ws_size;
  (void)hipFuncSetAttribute((const void*)slak_block_kernel, hipFuncAttributeMaxDynamicSharedMemorySize, LDS_BYTES);
  slak_block_kernel<<<256, NT, LDS_BYTES, stream>>>(
      (const unsigned short*)d_in[0], (const unsigned short*)d_in[1], (const unsigned short*)d_in[2],
      (const unsigned short*)d_in[3], (const unsigned short*)d_in[4], (const unsigned short*)d_in[5],
      (unsigned short*)d_out, (u64*)d_ws);
}
```

```cpp
#include <hip/hip_runtime.h>
#include <stdint.h>

typedef unsigned long long u64;
typedef unsigned u32;
typedef float f32x2 __attribute__((ext_vector_type(2)));
typedef float f32x4 __attribute__((ext_vector_type(4)));
typedef float f32x16 __attribute__((ext_vector_type(16)));
typedef short s16x4 __attribute__((ext_vector_type(4)));
typedef short s16x8 __attribute__((ext_vector_type(8)));
typedef __bf16 bf16x2 __attribute__((ext_vector_type(2)));
typedef u32 u32x2 __attribute__((ext_vector_type(2)));
typedef u32 u32x4 __attribute__((ext_vector_type(4)));
#define LDS_AS __attribute__((address_space(3)))
typedef __attribute__((address_space(1))) u64 gu64;
#define RLX_AGENT __ATOMIC_RELAXED, __HIP_MEMORY_SCOPE_AGENT
#define DI __device__ __forceinline__

constexpr int IMG = 224, CH = 96, HID = 384, NT = 768;
constexpr u32 X_BYTES = IMG * IMG * CH * 2;

constexpr float GA = 0.4686686930532063f, GK = 4.91222953926424f;
constexpr float GA_INV = 2.1337034344781243f, GA_INV2 = 4.552690346303743f;

constexpr u32 L_X = 0, X_REC = 832;
constexpr u32 L_Y = 0, L_YR = 36864;
constexpr u32 L_W1L = 37632;
constexpr u32 L_SCR = 74496, SCR_W = 2304;
constexpr u32 L_SS = 102144;
constexpr u32 L_V = 83200, V_ROW = 416;
constexpr u32 L_T = 83200, T_ROW = 288;
constexpr u32 L_W1E = 123136;
constexpr u32 L_HREM = 160000;
constexpr u32 L_W2 = 0;
constexpr u32 L_E = 74496;
constexpr u32 L_B = 163072;
constexpr u32 L_RP = 123648, L_BP = 126720, L_GG = 128256, L_PS = 129792, L_RED = 129856;
constexpr u32 LDS_BYTES = 163840;

constexpr int WS_EPOCH = 0, WS_TOT = 64, WS_PART = 64 + 8 * HID;
#define CODE_LINES 160
constexpr int SPIN_CAP = 1 << 20;

#define SB() __builtin_amdgcn_sched_barrier(0)
DI void bar_lds() { asm volatile("s_waitcnt lgkmcnt(0)\n\ts_barrier" ::: "memory"); }
DI void bar_all() { asm volatile("s_waitcnt vmcnt(0) lgkmcnt(0)\n\ts_barrier" ::: "memory"); }
#define LD_AUX 0
#define ST_AUX 16
typedef __amdgpu_buffer_rsrc_t rsrc_t;
DI rsrc_t make_rsrc(const void* p, u32 bytes) { return __builtin_amdgcn_make_buffer_rsrc((void*)p, 0, bytes, 0x00020000); }
DI u32x4 ld128(rsrc_t r, u32 off) { return __builtin_amdgcn_raw_buffer_load_b128(r, off, 0, LD_AUX); }
DI u32 pack_bf16(float a, float b) { bf16x2 v = __builtin_convertvector(f32x2{a, b}, bf16x2); return __builtin_bit_cast(u32, v); }
DI float bf_lo(u32 v) { return __uint_as_float(v << 16); }
DI float bf_hi(u32 v) { return __uint_as_float(v & 0xffff0000u); }
DI u32 perm_lo(u32 a, u32 b) { return __builtin_amdgcn_perm(b, a, 0x05040100u); }
DI u32 perm_hi(u32 a, u32 b) { return __builtin_amdgcn_perm(b, a, 0x07060302u); }
DI s16x4 tr_read(LDS_AS char* p) { return __builtin_amdgcn_ds_read_tr16_b64_v4i16((LDS_AS s16x4*)p); }
DI s16x8 tr_frag(LDS_AS char* p, int step) {
  s16x4 a = tr_read(p), b = tr_read(p + step);
  return __builtin_shufflevector(a, b, 0, 1, 2, 3, 4, 5, 6, 7);
}
DI f32x16 mfma32(s16x8 a, s16x8 b, f32x16 c) { return __builtin_amdgcn_mfma_f32_32x32x16_bf16(a, b, c, 0, 0, 0); }
DI f32x4 mfma16(s16x8 a, s16x8 b, f32x4 c) { return __builtin_amdgcn_mfma_f32_16x16x32_bf16(a, b, c, 0, 0, 0); }
DI float sum_xor32(float v) { auto r = __builtin_amdgcn_permlane32_swap(__float_as_uint(v), __float_as_uint(v), false, false); return __uint_as_float(r[0]) + __uint_as_float(r[1]); }
DI float sum_xor16(float v) { auto r = __builtin_amdgcn_permlane16_swap(__float_as_uint(v), __float_as_uint(v), false, false); return __uint_as_float(r[0]) + __uint_as_float(r[1]); }
DI u64 granule(u64 tag, float v) { return (tag << 24) | (u64)(((__float_as_uint(v) + 0x40u) >> 7) & 0xffffffu); }
DI float granule_val(u64 g) { return __uint_as_float(((u32)g & 0xffffffu) << 7); }
DI int pos_channel(int p) { return 8 * ((p % 24) >> 1) + 2 * (p / 24) + (p & 1); }

DI f32x2 gelu2(f32x2 x) {
  f32x2 t = __builtin_elementwise_fma(x, x, f32x2{GK, GK});
  f32x2 z = t * x;
  f32x2 e = {__builtin_amdgcn_exp2f(-z.x), __builtin_amdgcn_exp2f(-z.y)};
  f32x2 d = e + 1.0f;
  f32x2 r = {__builtin_amdgcn_rcpf(d.x), __builtin_amdgcn_rcpf(d.y)};
  return x * r;
}

DI void layer_norm_unit(LDS_AS char* L, int lane, u32 vcol, u32 dst, u32 dst_step) {
  const int g = lane >> 4, q = (lane >> 2) & 3;
  LDS_AS char* p0 = L + L_V + (8 * g + 4 * (g & 1) + q) * V_ROW + vcol;
  LDS_AS char* p1 = L + L_V + (8 * g + 4 * ((g & 1) ^ 1) + q) * V_ROW + vcol;
  u32 v[12];
#pragma unroll
  for (int c = 0; c < 3; c++) {
    u32x2 a = __builtin_bit_cast(u32x2, tr_read(p0 + c * 32 * V_ROW));
    u32x2 b = __builtin_bit_cast(u32x2, tr_read(p1 + c * 32 * V_ROW));
    v[4 * c] = a.x; v[4 * c + 1] = a.y; v[4 * c + 2] = b.x; v[4 * c + 3] = b.y;
  }
  f32x2 f[12];
  f32x2 s = {0.f, 0.f}, ss = {0.f, 0.f};
#pragma unroll
  for (int i = 0; i < 12; i++) { f[i] = f32x2{bf_lo(v[i]), bf_hi(v[i])}; s += f[i]; ss = __builtin_elementwise_fma(f[i], f[i], ss); }
  const float sum = sum_xor32(sum_xor16(s.x + s.y)), sq = sum_xor32(sum_xor16(ss.x + ss.y));
  const float mu = sum * (1.0f / CH);
  const float var = fmaxf(__builtin_fmaf(-mu, mu, sq * (1.0f / CH)), 0.f);
  const float rs = GA * __builtin_amdgcn_rsqf(var + 1e-5f);
  const float off = -mu * rs;
#pragma unroll
  for (int c = 0; c < 3; c++) {
    u32x4 o;
#pragma unroll
    for (int i = 0; i < 4; i++) { f32x2 y = __builtin_elementwise_fma(f[4 * c + i], f32x2{rs, rs}, f32x2{off, off}); o[i] = pack_bf16(y.x, y.y); }
    *(LDS_AS u32x4*)(L + dst + c * dst_step) = o;
  }
}

DI float add1(float a, float b) { float r; asm("v_add_f32 %0, %1, %2" : "=v"(r) : "v"(a), "v"(b)); return r; }

DI void finish_tile(LDS_AS char* er, const float* keep, f32x4 xa, f32x4 xc, rsrc_t ors, u32 off) {
  bar_lds();
  const f32x4 pa = *(LDS_AS f32x4*)(er), pc = *(LDS_AS f32x4*)(er + 1024);
  float v[8];
#pragma unroll
  for (int i = 0; i < 4; i++) { v[i] = add1(add1(keep[i], xa[i]), pa[i]); v[4 + i] = add1(add1(keep[4 + i], xc[i]), pc[i]); }
  __builtin_amdgcn_raw_buffer_store_b128(u32x4{pack_bf16(v[0], v[1]), pack_bf16(v[2], v[3]), pack_bf16(v[4], v[5]), pack_bf16(v[6], v[7])}, ors, off, 0, ST_AUX);
  SB();
}

#define STAMP(i)

__global__ void __launch_bounds__(NT, 3)
slak_block_kernel(const unsigned short* __restrict__ x, const unsigned short* __restrict__ dw, const unsigned short* __restrict__ w1,
                  const unsigned short* __restrict__ w2, const unsigned short* __restrict__ grn_g, const unsigned short* __restrict__ grn_b,
                  unsigned short* __restrict__ out, u64* ws) {
  extern __shared__ __attribute__((aligned(16))) char smem[];
  LDS_AS char* const L = (LDS_AS char*)smem;
  const int t = threadIdx.x, lane = t & 63, bid = blockIdx.x;
  const int w = __builtin_amdgcn_readfirstlane(t >> 6);
  STAMP(0);
  u32 code_pf = 0;
  if (t < CODE_LINES / 4) {
    const char* pc = (const char*)(__builtin_amdgcn_s_getpc() & ~255ull);
    code_pf = *(const u32*)(pc + (((bid >> 3) & 3) + 4 * t) * 128);
  }
  const int xcd = bid & 7, idx = bid >> 3;
  const int y0 = 14 * ((xcd >> 1) * 4 + (idx >> 3)), x0 = 14 * ((xcd & 1) * 8 + (idx & 7));
  const rsrc_t xrs = make_rsrc(x, X_BYTES);
  gu64* const gws = (gu64*)ws;

  u32x4 xin[2][4];
  u32 xdst[2];
  auto load_item = [&](int it) {
    const u32 item = t + it * NT, P = (item * 5462) >> 16, o = item - P * 12, Y = (P * 6554) >> 16, X = P - Y * 10;
    const u32 gy = y0 - 3 + 2 * Y, gx = x0 - 3 + 2 * X;
    u32 base = (u32)(__mul24((int)gy, IMG * CH * 2) + __mul24((int)gx, CH * 2)) + o * 16;
    asm volatile("" : "+v"(base));
    const bool live = item < 1200;
    const bool yk[2] = {live && gy < (u32)IMG, live && gy + 1 < (u32)IMG}, xk[2] = {gx < (u32)IMG, gx + 1 < (u32)IMG};
    xdst[it] = L_X + P * X_REC + o * 16;
#pragma unroll
    for (int k = 0; k < 4; k++) {
      const u32 off = (yk[k >> 1] && xk[k & 1]) ? base + (k >> 1) * (IMG * CH * 2) : 0x80000000u;
      xin[it][k] = ld128(xrs, off + (k & 1) * (CH * 2));
    }
  };
  auto store_item = [&](int it) {
#pragma unroll
    for (int j = 0; j < 4; j++)
      *(LDS_AS u32x4*)(L + xdst[it] + j * 192) = u32x4{perm_lo(xin[it][0][j], xin[it][1][j]), perm_lo(xin[it][2][j], xin[it][3][j]),
                                                       perm_hi(xin[it][0][j], xin[it][1][j]), perm_hi(xin[it][2][j], xin[it][3][j])};
  };
  const int cg = w >> 1, xh = w & 1;
  if (w >= 9) {
    const u32 tb = t - 576, r = (tb * 2731) >> 16, c4 = tb - r * 24;
    const rsrc_t drs = make_rsrc(dw, 49 * CH * 2);
    u32x2 dv[7];
#pragma unroll
    for (int j = 0; j < 7; j++) dv[j] = __builtin_amdgcn_raw_buffer_load_b64(drs, r * (7 * CH * 2) + c4 * 8 + j * (CH * 2), 0, LD_AUX);
    load_item(0);
    if (tb < 168) {
#pragma unroll
      for (int h = 0; h < 2; h++) {
        LDS_AS char* d = L + L_T + (24 * (2 * (c4 & 1) + h) + 2 * (c4 >> 1)) * T_ROW + (r + 1) * 16;
        *(LDS_AS u32x4*)(d) = u32x4{perm_lo(dv[0][h], dv[1][h]), perm_lo(dv[2][h], dv[3][h]), perm_lo(dv[4][h], dv[5][h]), dv[6][h] & 0xffffu};
        *(LDS_AS u32x4*)(d + 144) = u32x4{dv[0][h] << 16, perm_lo(dv[1][h], dv[2][h]), perm_lo(dv[3][h], dv[4][h]), perm_lo(dv[5][h], dv[6][h])};
        *(LDS_AS u32x4*)(d + T_ROW) = u32x4{perm_hi(dv[0][h], dv[1][h]), perm_hi(dv[2][h], dv[3][h]), perm_hi(dv[4][h], dv[5][h]), dv[6][h] >> 16};
        *(LDS_AS u32x4*)(d + T_ROW + 144) = u32x4{dv[0][h] & 0xffff0000u, perm_hi(dv[1][h], dv[2][h]), perm_hi(dv[3][h], dv[4][h]), perm_hi(dv[5][h], dv[6][h])};
      }
    }
  } else {
    load_item(0);
    if (t < 384) *(LDS_AS u32x4*)(L + L_T + (t >> 2) * T_ROW + ((t >> 1) & 1) * 144 + (t & 1) * 128) = u32x4{0, 0, 0, 0};
  }
  STAMP(1);
  bar_lds();
  STAMP(2);
  s16x4 wa[4][4];
  u32x4 w1r[6];
  u64 epoch;
  u32x4 aux16;
  {
    LDS_AS char* p = L + L_T + (16 * cg + (lane >> 2)) * T_ROW + (lane & 1) * 144 + (1 - ((lane >> 1) & 1)) * 16;
    u32x4 wr[8];
#pragma unroll
    for (int r = 0; r < 8; r++) wr[r] = *(LDS_AS u32x4*)(p + r * 16);
    if (w < 7) load_item(1);
    const int k = 16 * (w % 6) + (lane >> 2), rho = k ^ (((k >> 3) & 1) << 2);
    const u32 w1o = pos_channel(rho) * (HID * 2) + (w / 6) * 384 + (lane & 3) * 16;
    const rsrc_t w1rs = make_rsrc(w1, CH * HID * 2);
#pragma unroll
    for (int u = 0; u < 6; u++) w1r[u] = ld128(w1rs, w1o + u * 64);
    epoch = __hip_atomic_load(gws + WS_EPOCH, RLX_AGENT);
    aux16 = ld128(w == 7 ? make_rsrc(grn_b, HID * 2) : make_rsrc(w2, w == 8 ? HID * CH * 2 : 0),
                  w == 7 ? lane * 16 : (lane < 18 ? (idx * 18 + lane) * 128 : 0x80000000u));
#pragma unroll
    for (int i = 0; i < 16; i++) {
      u32x2 v = {wr[2 * (i >> 2)][i & 3], wr[2 * (i >> 2) + 1][i & 3]};
      asm volatile("" : "+v"(v));
      wa[i >> 2][i & 3] = __builtin_bit_cast(s16x4, v);
    }
  }
  STAMP(3);
  store_item(0);
  if (t < 432) store_item(1);
  bar_lds();
  STAMP(4);
  if (w == 11 && idx == 0) asm volatile("buffer_wbl2 sc0 sc1" ::: "memory");

  {
    LDS_AS char* xb = L + L_X + (4 * xh + (lane & 3)) * X_REC + (16 * cg + (lane >> 2)) * 8;
    LDS_AS char* xb2 = xb + 70 * X_REC;
    LDS_AS char* vb = L + L_V + (16 * cg + (lane >> 2)) * V_ROW + (4 * xh + (lane & 3)) * 8;
    const bool vok = 4 * xh + (lane & 3) < 7;
    f32x4 acc[7];
#pragma unroll
    for (int i = 0; i < 7; i++) acc[i] = f32x4{0.f, 0.f, 0.f, 0.f};
    s16x4 d[10][4];
    auto load_row = [&](int Y) {
#pragma unroll
      for (int sx = 0; sx < 4; sx++)
        d[Y][sx] = Y < 7 ? *(volatile LDS_AS s16x4*)(xb + (Y * 10 + sx) * X_REC) : *(volatile LDS_AS s16x4*)(xb2 + ((Y - 7) * 10 + sx) * X_REC);
    };
    load_row(0); load_row(1); load_row(2);
    SB();
#pragma unroll
    for (int Y = 0; Y < 10; Y++) {
      if (Y + 3 < 10) load_row(Y + 3);
#pragma unroll
      for (int sx = 0; sx < 4; sx++) {
#pragma unroll
        for (int sy = 3; sy >= 0; sy--) {
          const int Yo = Y - sy;
          if (Yo >= 0 && Yo < 7) acc[Yo] = __builtin_amdgcn_mfma_f32_4x4x4bf16_1k(wa[sy][sx], d[Y][sx], acc[Yo], 0, 0, 0);
        }
      }
      if (Y >= 4) {
        const f32x4 r = acc[Y - 4];
        if (vok) *(LDS_AS u32x2*)(vb + (Y - 4) * 56) = u32x2{pack_bf16(r[0], r[1]), pack_bf16(r[2], r[3])};
      }
      SB();
    }
    if (vok) *(LDS_AS u32x2*)(vb + 6 * 56) = u32x2{pack_bf16(acc[6][0], acc[6][1]), pack_bf16(acc[6][2], acc[6][3])};
  }
  const u32 w1d = (w / 6) * 3 * 6144 + (w % 6) * 1024 + lane * 16;
#pragma unroll
  for (int u = 0; u < 3; u++) *(LDS_AS u32x4*)(L + L_W1E + w1d + u * 6144) = w1r[u];
  if (w == 7 && lane < 48) *(LDS_AS u32x4*)(L + L_B + lane * 16) = aux16;
  asm volatile("" :: "v"(aux16), "v"(code_pf));
  epoch = ((u64)(u32)__builtin_amdgcn_readfirstlane((int)(epoch >> 32)) << 32) | (u32)__builtin_amdgcn_readfirstlane((int)epoch);
  bar_lds();
  STAMP(5);
#pragma unroll
  for (int u = 0; u < 3; u++) *(LDS_AS u32x4*)(L + L_W1L + w1d + u * 6144) = w1r[3 + u];

  const int T = w >> 1, Hh = w & 1;
  layer_norm_unit(L, lane, (4 * w + (lane & 3)) * 8, L_Y + T * 6144 + ((lane >> 4) * 32 + 16 * Hh + (lane & 15)) * 16, 2048);
  if (w == 11) layer_norm_unit(L, lane, 48 * 8, L_YR + ((lane >> 4) * 4 + (lane & 3)) * 16, 256);
  bar_lds();
  STAMP(6);

  const int g4 = lane >> 4, q4 = (lane >> 2) & 3, p4 = lane & 3;
  s16x8 hf[12];
  u32x4 w2r[6], xr[3], xrem;
  float gam;
  const rsrc_t w2rs = make_rsrc(w2, HID * CH * 2);
  const u32 w2o = (t >> 2) * (CH * 2) + (t & 3) * 16;
  const u32 remo = t < 48 ? (u32)(((y0 + 12 + t / 24) * IMG + x0 + 12) * (CH * 2) + (t % 24) * 16) : 0x80000000u;
  const int pidx = 4 * w + ((lane >> 2) & 3);
  const u32 ooff = (u32)(((y0 + 2 * (pidx / 7) + ((lane >> 1) & 1)) * IMG + x0 + 2 * (pidx % 7) + (lane & 1)) * (CH * 2) + g4 * 16);
  const f32x16 zero16 = {0.f, 0.f, 0.f, 0.f, 0.f, 0.f, 0.f, 0.f, 0.f, 0.f, 0.f, 0.f, 0.f, 0.f, 0.f, 0.f};
  {
    const u32 wl = (8 * (g4 >> 1) + q4) * 64 + (g4 & 1) * 32 + p4 * 8 + Hh * 3 * 6144;
    LDS_AS char* wE = L + L_W1E + wl;
    LDS_AS char* wL = L + L_W1L + wl;
    LDS_AS char* sw = L + L_SCR + w * SCR_W + (lane & 31) * 72 + (lane >> 5) * 8;
    LDS_AS char* sr = L + L_SCR + w * SCR_W + (8 * (g4 >> 1) + q4) * 72 + (4 * ((g4 & 1) ^ Hh) + p4) * 8;
    LDS_AS char* ssw = L + L_SS + ((2 * T + (lane >> 5)) * HID + Hh * 192 + (lane & 31)) * 4;
    s16x8 yf[6], wf[6];
    f32x16 acc = zero16;
    {
      LDS_AS char* wp = (T < 3 ? wE : wL) + (T % 3) * 6144;
      s16x8 yr[6], wq[6];
#pragma unroll
      for (int k = 0; k < 6; k++) { yr[k] = *(LDS_AS s16x8*)(L + L_YR + ((2 * k + (lane >> 5)) * 4 + (lane & 3)) * 16); wq[k] = tr_frag(wp + k * 1024, 256); }
#pragma unroll
      for (int k = 0; k < 6; k++) { yf[k] = *(LDS_AS s16x8*)(L + L_Y + T * 6144 + k * 1024 + lane * 16); wf[k] = tr_frag(wE + k * 1024, 256); }
      SB();
      f32x16 ra = zero16;
#pragma unroll
      for (int k = 0; k < 6; k++) ra = mfma32(yr[k], wq[k], ra);
#pragma unroll
      for (int k = 0; k < 6; k++) acc = mfma32(yf[k], wf[k], acc);
#pragma unroll
      for (int k = 0; k < 3; k++) wf[k] = tr_frag(wE + 6144 + k * 1024, 256);
      SB();
      const f32x2 a = gelu2(f32x2{ra[0], ra[1]}), b = gelu2(f32x2{ra[2], ra[3]});
      const f32x2 sq = __builtin_elementwise_fma(a, a, b * b);
      if (lane < 32) {
        const int hc = Hh * 192 + T * 32 + lane;
        *(LDS_AS u32x2*)(L + L_HREM + hc * 8) = u32x2{pack_bf16(a.x, a.y), pack_bf16(b.x, b.y)};
        *(LDS_AS float*)(L + L_SS + (12 * HID + hc) * 4) = sq.x + sq.y;
      }
      SB();
    }
#pragma unroll
    for (int s = 0; s < 6; s++) {
      LDS_AS char* wp = (s + 1 < 3 ? wE : wL) + ((s + 1) % 3) * 6144;
      LDS_AS char* wp2 = (s + 2 < 3 ? wE : wL) + ((s + 2) % 3) * 6144;
      if (s < 5) {
#pragma unroll
        for (int k = 3; k < 6; k++) wf[k] = tr_frag(wp + k * 1024, 256);
      }
      f32x2 xv[8], z[8];
#pragma unroll
      for (int i = 0; i < 8; i++) { xv[i] = f32x2{acc[2 * i], acc[2 * i + 1]}; z[i] = __builtin_elementwise_fma(xv[i], xv[i], f32x2{GK, GK}); }
      SB();
#pragma unroll
      for (int i = 0; i < 8; i++) z[i] *= xv[i];
      SB();
      f32x16 nxt = zero16;
#pragma unroll
      for (int i = 0; i < 16; i++) {
        if (s < 5 && i % 3 == 0 && i < 9) { nxt = mfma32(yf[i / 3], wf[i / 3], nxt); SB(); }
        if (s == 5 && i % 3 == 0) { w2r[i / 3] = ld128(w2rs, w2o + ((i / 3) & 1) * (192 * CH * 2) + (i / 6) * 64); SB(); }
        z[i >> 1][i & 1] = __builtin_amdgcn_exp2f(-z[i >> 1][i & 1]);
        if (i % 3 == 2 && (i < 9 || s == 5)) SB();
      }
      if (s < 4) {
#pragma unroll
        for (int k = 0; k < 3; k++) wf[k] = tr_frag(wp2 + k * 1024, 256);
      }
      SB();
#pragma unroll
      for (int i = 0; i < 8; i++) z[i] += 1.0f;
      SB();
#pragma unroll
      for (int i = 0; i < 16; i++) {
        if (s < 5 && i % 3 == 0 && i < 9) { nxt = mfma32(yf[3 + i / 3], wf[3 + i / 3], nxt); SB(); }
        if (s == 5 && i % 3 == 0 && i < 9) { xr[i / 3] = ld128(xrs, ooff + (i / 3) * 64); SB(); }
        if (s == 5 && i == 9) { xrem = ld128(xrs, remo); gam = bf_lo(grn_g[t < HID ? t : 0]); SB(); }
        z[i >> 1][i & 1] = __builtin_amdgcn_rcpf(z[i >> 1][i & 1]);
        if (i % 3 == 2 && i < 9) SB();
      }
      SB();
#pragma unroll
      for (int i = 0; i < 8; i++) z[i] *= xv[i];
      SB();
      f32x2 ssa = {0.f, 0.f}, ssb = {0.f, 0.f};
#pragma unroll
      for (int Q = 0; Q < 4; Q++) {
        ssa = __builtin_elementwise_fma(z[2 * Q], z[2 * Q], ssa);
        ssb = __builtin_elementwise_fma(z[2 * Q + 1], z[2 * Q + 1], ssb);
        *(volatile LDS_AS u32x2*)(sw + Q * 16) = u32x2{pack_bf16(z[2 * Q].x, z[2 * Q].y), pack_bf16(z[2 * Q + 1].x, z[2 * Q + 1].y)};
      }
      hf[2 * s] = tr_frag(sr, 4 * 72);
      hf[2 * s + 1] = tr_frag(sr + 16 * 72, 4 * 72);
      ssa += ssb;
      *(LDS_AS float*)(ssw + s * 128) = ssa.x + ssa.y;
      acc = nxt;
      SB();
    }
  }
  bar_lds();
  STAMP(7);

  const u64 tag = (epoch + 1) & ((1ull << 40) - 1);
  if (t < HID) {
    float s = 0.f;
#pragma unroll
    for (int i = 0; i < 13; i++) s += *(LDS_AS float*)(L + L_SS + (i * HID + t) * 4);
    __hip_atomic_store(gws + WS_PART + ((t >> 4) * 256 + bid) * 16 + (t & 15), granule(tag, s), RLX_AGENT);
  }

  if (bid < 24) {
    u64 gr[6];
#pragma unroll
    for (int i = 0; i < 6; i++) gr[i] = (t + i * NT < 4096) ? ~(tag << 24) : (tag << 24);
    for (int spin = 0; spin < SPIN_CAP; spin++) {
      bool ok = true;
#pragma unroll
      for (int i = 0; i < 6; i++) if ((gr[i] >> 24) != tag) gr[i] = __hip_atomic_load(gws + WS_PART + bid * 4096 + t + i * NT, RLX_AGENT);
#pragma unroll
      for (int i = 0; i < 6; i++) ok &= (gr[i] >> 24) == tag;
      if (__all(ok)) break;
    }
    float s = 0.f;
#pragma unroll
    for (int i = 0; i < 6; i++) s += granule_val(gr[i]);
    s = sum_xor32(sum_xor16(s));
    if (lane < 16) *(LDS_AS float*)(L + L_RED + (w * 16 + lane) * 4) = s;
    bar_lds();
    if (t < 128) {
      float tot = 0.f;
#pragma unroll
      for (int i = 0; i < 12; i++) tot += *(LDS_AS float*)(L + L_RED + (i * 16 + (t & 15)) * 4);
      const float gx = __builtin_amdgcn_sqrtf(__builtin_fmaf(tot, GA_INV2, 1e-6f));
      __hip_atomic_store(gws + WS_TOT + (t >> 4) * HID + bid * 16 + (t & 15), granule(tag, gx), RLX_AGENT);
    }
  }

#pragma unroll
  for (int i = 0; i < 6; i++) *(LDS_AS u32x4*)(L + L_W2 + (t + i * NT) * 16) = w2r[i];
  bar_lds();
  const int pq = 4 * (g4 & 1) + p4;
  const u32 w2l = (8 * (g4 >> 1) + q4) * 64 + (4 * (pq & 1) + 2 * (pq >> 2) + ((pq >> 1) & 1)) * 8;
  {
    LDS_AS char* ap = L + L_W2 + (w % 3) * 24576 + (w / 3) * 6144 + w2l;
    f32x16 acc = zero16;
    s16x8 af[6], bfr[6];
#pragma unroll
    for (int k = 0; k < 6; k++) { af[k] = tr_frag(ap + k * 1024, 256); bfr[k] = *(LDS_AS s16x8*)(L + L_B + (6 * (w / 3) + k) * 32 + (lane >> 5) * 16); }
    SB();
#pragma unroll
    for (int k = 0; k < 6; k++) acc = mfma32(af[k], bfr[k], acc);
    if ((lane & 31) == 0) {
      LDS_AS char* d = L + L_BP + ((w / 3) * CH + (w % 3) * 32 + (lane >> 5) * 16) * 4;
#pragma unroll
      for (int i = 0; i < 4; i++) *(LDS_AS f32x4*)(d + i * 16) = f32x4{acc[4 * i], acc[4 * i + 1], acc[4 * i + 2], acc[4 * i + 3]};
    }
  }
  bar_lds();
  f32x4 xb[6];
#pragma unroll
  for (int i = 0; i < 6; i++) {
    f32x4 a = f32x4{bf_lo(xr[i >> 1][2 * (i & 1)]), bf_hi(xr[i >> 1][2 * (i & 1)]), bf_lo(xr[i >> 1][2 * (i & 1) + 1]), bf_hi(xr[i >> 1][2 * (i & 1) + 1])};
#pragma unroll
    for (int k = 0; k < 4; k++) a += *(LDS_AS f32x4*)(L + L_BP + (k * CH + (i >> 1) * 32 + g4 * 8 + (i & 1) * 4) * 4);
    asm volatile("" : "+v"(a));
    xb[i] = a;
    SB();
  }
  f32x2 w2f[24];
#pragma unroll
  for (int i = 0; i < 24; i++) w2f[i] = f32x2{bf_lo(w2r[i >> 2][i & 3]), bf_hi(w2r[i >> 2][i & 3])};
#pragma unroll
  for (int i = 0; i < 24; i++) asm volatile("" : "+v"(w2f[i]));
  STAMP(8);

  if (t < HID) {
    u64 gr = ~(tag << 24);
    for (int spin = 0; spin < SPIN_CAP; spin++) {
      if ((gr >> 24) != tag) gr = __hip_atomic_load(gws + WS_TOT + (bid & 7) * HID + t, RLX_AGENT);
      if (__all((gr >> 24) == tag)) break;
    }
    const float gx = granule_val(gr);
    *(LDS_AS float*)(L + L_GG + t * 4) = gam * gx;
    float s = sum_xor32(sum_xor16(gx));
    s += __builtin_bit_cast(float, __builtin_amdgcn_update_dpp(0, __builtin_bit_cast(int, s), 0x128, 0xf, 0xf, false));
    s += __builtin_bit_cast(float, __builtin_amdgcn_update_dpp(0, __builtin_bit_cast(int, s), 0x124, 0xf, 0xf, false));
    s += __builtin_bit_cast(float, __builtin_amdgcn_update_dpp(0, __builtin_bit_cast(int, s), 0x4e, 0xf, 0xf, false));
    s += __builtin_bit_cast(float, __builtin_amdgcn_update_dpp(0, __builtin_bit_cast(int, s), 0xb1, 0xf, 0xf, false));
    if (lane == 0) *(LDS_AS float*)(L + L_PS + w * 4) = s;
  }
  bar_lds();
  STAMP(9);
  if (bid == 0 && t == 0) __hip_atomic_store(gws + WS_EPOCH, epoch + 1, RLX_AGENT);
  {
    const f32x4 pa = *(LDS_AS f32x4*)(L + L_PS);
    const f32x2 pb = *(LDS_AS f32x2*)(L + L_PS + 16);
    const float mean = (((pa[0] + pa[1]) + (pa[2] + pa[3])) + (pb[0] + pb[1])) * (1.0f / HID);
    const float inv = GA_INV * __builtin_amdgcn_rcpf(mean + 1e-6f);
    const float s0 = __builtin_fmaf(*(LDS_AS float*)(L + L_GG + (t >> 2) * 4), inv, GA_INV);
    const float s1 = __builtin_fmaf(*(LDS_AS float*)(L + L_GG + (192 + (t >> 2)) * 4), inv, GA_INV);
#pragma unroll
    for (int i = 0; i < 6; i++) {
      const float s = (i & 1) ? s1 : s0;
      u32x4 o;
#pragma unroll
      for (int j = 0; j < 4; j++) { const f32x2 v = w2f[4 * i + j] * s; o[j] = pack_bf16(v.x, v.y); }
      *(LDS_AS u32x4*)(L + L_W2 + (t + i * NT) * 16) = o;
    }
  }
  bar_lds();
  STAMP(10);

  {
    LDS_AS char* ap = L + L_W2 + Hh * 12 * 1024 + w2l;
    LDS_AS char* ew = L + L_E + w * 4096 + lane * 16;
    LDS_AS char* er = L + L_E + (w ^ 1) * 4096 + lane * 16;
    constexpr int PF = 4;
    const rsrc_t ors = make_rsrc(out, X_BYTES);
    s16x8 af[36];
#pragma unroll
    for (int i = 0; i < PF; i++) af[i] = tr_frag(ap + i * 1024, 256);
    {
      const int o16 = w % 6, kh = w / 6;
      LDS_AS char* rp = L + L_W2 + (o16 >> 1) * 24576 + (kh * 192 + 8 * g4 + q4) * 64 + (4 * (o16 & 1) + p4) * 8;
      LDS_AS char* hp = L + L_HREM + (kh * 192 + 8 * g4 + q4) * 8;
      s16x8 ra_a[6], ra_b[6];
#pragma unroll
      for (int k = 0; k < 6; k++) { ra_a[k] = tr_frag(rp + k * 2048, 256); ra_b[k] = tr_frag(hp + k * 256, 32); }
      SB();
      f32x16 acc = zero16;
      f32x4 ra = {0.f, 0.f, 0.f, 0.f};
      float keep[8];
#pragma unroll
      for (int i = 0; i < 36; i++) {
        const int ot = i / 12, k = i % 12;
        if (k == 0) acc = zero16;
        acc = mfma32(af[i], hf[k], acc);
        if (i + PF < 36) af[i + PF] = tr_frag(ap + ((i + PF) / 12) * 24576 + ((i + PF) % 12) * 1024, 256);
        if (i >= 4 && i < 10) ra = mfma16(ra_a[i - 4], ra_b[i - 4], ra);
        if (i == 11 && (lane & 15) < 4) *(LDS_AS f32x4*)(L + L_RP + ((kh * 4 + (lane & 3)) * CH + 16 * o16 + 4 * g4) * 4) = ra;
        SB();
        if (k == 6 && ot > 0) finish_tile(er + ((ot - 1) & 1) * 2048, keep, xb[2 * ot - 2], xb[2 * ot - 1], ors, ooff + (ot - 1) * 64);
        if (i == 19 && t < 48) {
          const int px = t / 12, oc = t % 12;
          const u32x4 xv = xrem;
          f32x4 r[2];
#pragma unroll
          for (int j = 0; j < 2; j++) {
            f32x4 a2 = f32x4{bf_lo(xv[2 * j]), bf_hi(xv[2 * j]), bf_lo(xv[2 * j + 1]), bf_hi(xv[2 * j + 1])};
#pragma unroll
            for (int kq = 0; kq < 4; kq++) a2 += *(LDS_AS f32x4*)(L + L_BP + (kq * CH + oc * 8 + j * 4) * 4);
            a2 += *(LDS_AS f32x4*)(L + L_RP + (px * CH + oc * 8 + j * 4) * 4);
            a2 += *(LDS_AS f32x4*)(L + L_RP + ((4 + px) * CH + oc * 8 + j * 4) * 4);
            r[j] = a2;
          }
          __builtin_amdgcn_raw_buffer_store_b128(u32x4{pack_bf16(r[0][0], r[0][1]), pack_bf16(r[0][2], r[0][3]), pack_bf16(r[1][0], r[1][1]), pack_bf16(r[1][2], r[1][3])}, ors, remo, 0, ST_AUX);
        }
        if (k == 11) {
          f32x4 send[2];
#pragma unroll
          for (int r = 0; r < 8; r++) {
            auto sw2 = __builtin_amdgcn_permlane16_swap(__float_as_uint(acc[r]), __float_as_uint(acc[r + 8]), false, false);
            keep[r] = __uint_as_float(sw2[0]);
            send[r >> 2][r & 3] = __uint_as_float(sw2[1]);
          }
          *(LDS_AS f32x4*)(ew + (ot & 1) * 2048) = send[0];
          *(LDS_AS f32x4*)(ew + (ot & 1) * 2048 + 1024) = send[1];
          SB();
        }
      }
      finish_tile(er, keep, xb[4], xb[5], ors, ooff + 128);
    }
  }
  STAMP(11);
}

extern "C" void kernel_launch(void* const* d_in, const int* in_sizes, int n_in,
                              void* d_out, int out_size, void* d_ws, size_t ws_size,
                              hipStream_t stream) {
  (void)in_sizes; (void)n_in; (void)out_size; (void)ws_size;
  (void)hipFuncSetAttribute((const void*)slak_block_kernel, hipFuncAttributeMaxDynamicSharedMemorySize, LDS_BYTES);
  slak_block_kernel<<<256, NT, LDS_BYTES, stream>>>(
      (const unsigned short*)d_in[0], (const unsigned short*)d_in[1], (const unsigned short*)d_in[2],
      (const unsigned short*)d_in[3], (const unsigned short*)d_in[4], (const unsigned short*)d_in[5],
      (unsigned short*)d_out, (u64*)d_ws);
}
```

```cpp
#include <hip/hip_runtime.h>
#include <stdint.h>
#define DIAG
#define NSTAMP 12

typedef unsigned long long u64;
typedef unsigned u32;
typedef float f32x2 __attribute__((ext_vector_type(2)));
typedef float f32x4 __attribute__((ext_vector_type(4)));
typedef float f32x16 __attribute__((ext_vector_type(16)));
typedef short s16x4 __attribute__((ext_vector_type(4)));
typedef short s16x8 __attribute__((ext_vector_type(8)));
typedef __bf16 bf16x2 __attribute__((ext_vector_type(2)));
typedef u32 u32x2 __attribute__((ext_vector_type(2)));
typedef u32 u32x4 __attribute__((ext_vector_type(4)));
#define LDS_AS __attribute__((address_space(3)))
typedef __attribute__((address_space(1))) u64 gu64;
#define RLX_AGENT __ATOMIC_RELAXED, __HIP_MEMORY_SCOPE_AGENT
#define DI __device__ __forceinline__

constexpr int IMG = 224, CH = 96, HID = 384, NT = 768;
constexpr u32 X_BYTES = IMG * IMG * CH * 2;

constexpr float GA = 0.4686686930532063f, GK = 4.91222953926424f;
constexpr float GA_INV = 2.1337034344781243f, GA_INV2 = 4.552690346303743f;

constexpr u32 L_X = 0, X_REC = 832;
constexpr u32 L_Y = 0, L_YR = 36864;
constexpr u32 L_W1L = 37632;
constexpr u32 L_SCR = 74496, SCR_W = 2304;
constexpr u32 L_SS = 102144;
constexpr u32 L_V = 83200, V_ROW = 416;
constexpr u32 L_T = 83200, T_ROW = 288;
constexpr u32 L_W1E = 123136;
constexpr u32 L_HREM = 160000;
constexpr u32 L_W2 = 0;
constexpr u32 L_E = 74496;
constexpr u32 L_B = 163072;
constexpr u32 L_RP = 123648, L_BP = 126720, L_GG = 128256, L_PS = 129792, L_RED = 129856;
constexpr u32 LDS_BYTES = 163840;

constexpr int WS_EPOCH = 0, WS_TOT = 64, WS_PART = 64 + 8 * HID;
#define CODE_LINES 160
constexpr int SPIN_CAP = 1 << 20;

#define SB() __builtin_amdgcn_sched_barrier(0)
DI void bar_lds() { asm volatile("s_waitcnt lgkmcnt(0)\n\ts_barrier" ::: "memory"); }
DI void bar_all() { asm volatile("s_waitcnt vmcnt(0) lgkmcnt(0)\n\ts_barrier" ::: "memory"); }
#define LD_AUX 0
#define ST_AUX 16
typedef __amdgpu_buffer_rsrc_t rsrc_t;
DI rsrc_t make_rsrc(const void* p, u32 bytes) { return __builtin_amdgcn_make_buffer_rsrc((void*)p, 0, bytes, 0x00020000); }
DI u32x4 ld128(rsrc_t r, u32 off) { return __builtin_amdgcn_raw_buffer_load_b128(r, off, 0, LD_AUX); }
DI u32 pack_bf16(float a, float b) { bf16x2 v = __builtin_convertvector(f32x2{a, b}, bf16x2); return __builtin_bit_cast(u32, v); }
DI float bf_lo(u32 v) { return __uint_as_float(v << 16); }
DI float bf_hi(u32 v) { return __uint_as_float(v & 0xffff0000u); }
DI u32 perm_lo(u32 a, u32 b) { return __builtin_amdgcn_perm(b, a, 0x05040100u); }
DI u32 perm_hi(u32 a, u32 b) { return __builtin_amdgcn_perm(b, a, 0x07060302u); }
DI s16x4 tr_read(LDS_AS char* p) { return __builtin_amdgcn_ds_read_tr16_b64_v4i16((LDS_AS s16x4*)p); }
DI s16x8 tr_frag(LDS_AS char* p, int step) {
  s16x4 a = tr_read(p), b = tr_read(p + step);
  return __builtin_shufflevector(a, b, 0, 1, 2, 3, 4, 5, 6, 7);
}
DI f32x16 mfma32(s16x8 a, s16x8 b, f32x16 c) { return __builtin_amdgcn_mfma_f32_32x32x16_bf16(a, b, c, 0, 0, 0); }
DI f32x4 mfma16(s16x8 a, s16x8 b, f32x4 c) { return __builtin_amdgcn_mfma_f32_16x16x32_bf16(a, b, c, 0, 0, 0); }
DI float sum_xor32(float v) { auto r = __builtin_amdgcn_permlane32_swap(__float_as_uint(v), __float_as_uint(v), false, false); return __uint_as_float(r[0]) + __uint_as_float(r[1]); }
DI float sum_xor16(float v) { auto r = __builtin_amdgcn_permlane16_swap(__float_as_uint(v), __float_as_uint(v), false, false); return __uint_as_float(r[0]) + __uint_as_float(r[1]); }
DI u64 granule(u64 tag, float v) { return (tag << 24) | (u64)(((__float_as_uint(v) + 0x40u) >> 7) & 0xffffffu); }
DI float granule_val(u64 g) { return __uint_as_float(((u32)g & 0xffffffu) << 7); }
DI int pos_channel(int p) { return 8 * ((p % 24) >> 1) + 2 * (p / 24) + (p & 1); }

DI f32x2 gelu2(f32x2 x) {
  f32x2 t = __builtin_elementwise_fma(x, x, f32x2{GK, GK});
  f32x2 z = t * x;
  f32x2 e = {__builtin_amdgcn_exp2f(-z.x), __builtin_amdgcn_exp2f(-z.y)};
  f32x2 d = e + 1.0f;
  f32x2 r = {__builtin_amdgcn_rcpf(d.x), __builtin_amdgcn_rcpf(d.y)};
  return x * r;
}

DI void layer_norm_unit(LDS_AS char* L, int lane, u32 vcol, u32 dst, u32 dst_step) {
  const int g = lane >> 4, q = (lane >> 2) & 3;
  LDS_AS char* p0 = L + L_V + (8 * g + 4 * (g & 1) + q) * V_ROW + vcol;
  LDS_AS char* p1 = L + L_V + (8 * g + 4 * ((g & 1) ^ 1) + q) * V_ROW + vcol;
  u32 v[12];
#pragma unroll
  for (int c = 0; c < 3; c++) {
    u32x2 a = __builtin_bit_cast(u32x2, tr_read(p0 + c * 32 * V_ROW));
    u32x2 b = __builtin_bit_cast(u32x2, tr_read(p1 + c * 32 * V_ROW));
    v[4 * c] = a.x; v[4 * c + 1] = a.y; v[4 * c + 2] = b.x; v[4 * c + 3] = b.y;
  }
  f32x2 f[12];
  f32x2 s = {0.f, 0.f}, ss = {0.f, 0.f};
#pragma unroll
  for (int i = 0; i < 12; i++) { f[i] = f32x2{bf_lo(v[i]), bf_hi(v[i])}; s += f[i]; ss = __builtin_elementwise_fma(f[i], f[i], ss); }
  const float sum = sum_xor32(sum_xor16(s.x + s.y)), sq = sum_xor32(sum_xor16(ss.x + ss.y));
  const float mu = sum * (1.0f / CH);
  const float var = fmaxf(__builtin_fmaf(-mu, mu, sq * (1.0f / CH)), 0.f);
  const float rs = GA * __builtin_amdgcn_rsqf(var + 1e-5f);
  const float off = -mu * rs;
#pragma unroll
  for (int c = 0; c < 3; c++) {
    u32x4 o;
#pragma unroll
    for (int i = 0; i < 4; i++) { f32x2 y = __builtin_elementwise_fma(f[4 * c + i], f32x2{rs, rs}, f32x2{off, off}); o[i] = pack_bf16(y.x, y.y); }
    *(LDS_AS u32x4*)(L + dst + c * dst_step) = o;
  }
}

DI float add1(float a, float b) { float r; asm("v_add_f32 %0, %1, %2" : "=v"(r) : "v"(a), "v"(b)); return r; }

DI void finish_tile(LDS_AS char* er, const float* keep, f32x4 xa, f32x4 xc, rsrc_t ors, u32 off) {
  bar_lds();
  const f32x4 pa = *(LDS_AS f32x4*)(er), pc = *(LDS_AS f32x4*)(er + 1024);
  float v[8];
#pragma unroll
  for (int i = 0; i < 4; i++) { v[i] = add1(add1(keep[i], xa[i]), pa[i]); v[4 + i] = add1(add1(keep[4 + i], xc[i]), pc[i]); }
  __builtin_amdgcn_raw_buffer_store_b128(u32x4{pack_bf16(v[0], v[1]), pack_bf16(v[2], v[3]), pack_bf16(v[4], v[5]), pack_bf16(v[6], v[7])}, ors, off, 0, ST_AUX);
  SB();
}

#define STAMP(i) do { SB(); st_r[i] = __builtin_amdgcn_s_memrealtime(); st_c[i] = __builtin_amdgcn_s_memtime(); SB(); } while (0)

__global__ void __launch_bounds__(NT, 3)
slak_block_kernel(const unsigned short* __restrict__ x, const unsigned short* __restrict__ dw, const unsigned short* __restrict__ w1,
                  const unsigned short* __restrict__ w2, const unsigned short* __restrict__ grn_g, const unsigned short* __restrict__ grn_b,
                  unsigned short* __restrict__ out, u64* ws) {
  extern __shared__ __attribute__((aligned(16))) char smem[];
  LDS_AS char* const L = (LDS_AS char*)smem;
  const int t = threadIdx.x, lane = t & 63, bid = blockIdx.x;
  const int w = __builtin_amdgcn_readfirstlane(t >> 6);
  u64* stamps = ws + (1 << 20);
  u64 st_r[NSTAMP], st_c[NSTAMP];
  STAMP(0);
  u32 code_pf = 0;
  if (t < CODE_LINES / 4) {
    const char* pc = (const char*)(__builtin_amdgcn_s_getpc() & ~255ull);
    code_pf = *(const u32*)(pc + (((bid >> 3) & 3) + 4 * t) * 128);
  }
  const int xcd = bid & 7, idx = bid >> 3;
  const int y0 = 14 * ((xcd >> 1) * 4 + (idx >> 3)), x0 = 14 * ((xcd & 1) * 8 + (idx & 7));
  const rsrc_t xrs = make_rsrc(x, X_BYTES);
  gu64* const gws = (gu64*)ws;

  u32x4 xin[2][4];
  u32 xdst[2];
  auto load_item = [&](int it) {
    const u32 item = t + it * NT, P = (item * 5462) >> 16, o = item - P * 12, Y = (P * 6554) >> 16, X = P - Y * 10;
    const u32 gy = y0 - 3 + 2 * Y, gx = x0 - 3 + 2 * X;
    u32 base = (u32)(__mul24((int)gy, IMG * CH * 2) + __mul24((int)gx, CH * 2)) + o * 16;
    asm volatile("" : "+v"(base));
    const bool live = item < 1200;
    const bool yk[2] = {live && gy < (u32)IMG, live && gy + 1 < (u32)IMG}, xk[2] = {gx < (u32)IMG, gx + 1 < (u32)IMG};
    xdst[it] = L_X + P * X_REC + o * 16;
#pragma unroll
    for (int k = 0; k < 4; k++) {
      const u32 off = (yk[k >> 1] && xk[k & 1]) ? base + (k >> 1) * (IMG * CH * 2) : 0x80000000u;
      xin[it][k] = ld128(xrs, off + (k & 1) * (CH * 2));
    }
  };
  auto store_item = [&](int it) {
#pragma unroll
    for (int j = 0; j < 4; j++)
      *(LDS_AS u32x4*)(L + xdst[it] + j * 192) = u32x4{perm_lo(xin[it][0][j], xin[it][1][j]), perm_lo(xin[it][2][j], xin[it][3][j]),
                                                       perm_hi(xin[it][0][j], xin[it][1][j]), perm_hi(xin[it][2][j], xin[it][3][j])};
  };
  const int cg = w >> 1, xh = w & 1;
  if (w >= 9) {
    const u32 tb = t - 576, r = (tb * 2731) >> 16, c4 = tb - r * 24;
    const rsrc_t drs = make_rsrc(dw, 49 * CH * 2);
    u32x2 dv[7];
#pragma unroll
    for (int j = 0; j < 7; j++) dv[j] = __builtin_amdgcn_raw_buffer_load_b64(drs, r * (7 * CH * 2) + c4 * 8 + j * (CH * 2), 0, LD_AUX);
    load_item(0);
    if (tb < 168) {
#pragma unroll
      for (int h = 0; h < 2; h++) {
        LDS_AS char* d = L + L_T + (24 * (2 * (c4 & 1) + h) + 2 * (c4 >> 1)) * T_ROW + (r + 1) * 16;
        *(LDS_AS u32x4*)(d) = u32x4{perm_lo(dv[0][h], dv[1][h]), perm_lo(dv[2][h], dv[3][h]), perm_lo(dv[4][h], dv[5][h]), dv[6][h] & 0xffffu};
        *(LDS_AS u32x4*)(d + 144) = u32x4{dv[0][h] << 16, perm_lo(dv[1][h], dv[2][h]), perm_lo(dv[3][h], dv[4][h]), perm_lo(dv[5][h], dv[6][h])};
        *(LDS_AS u32x4*)(d + T_ROW) = u32x4{perm_hi(dv[0][h], dv[1][h]), perm_hi(dv[2][h], dv[3][h]), perm_hi(dv[4][h], dv[5][h]), dv[6][h] >> 16};
        *(LDS_AS u32x4*)(d + T_ROW + 144) = u32x4{dv[0][h] & 0xffff0000u, perm_hi(dv[1][h], dv[2][h]), perm_hi(dv[3][h], dv[4][h]), perm_hi(dv[5][h], dv[6][h])};
      }
    }
  } else {
    load_item(0);
    if (t < 384) *(LDS_AS u32x4*)(L + L_T + (t >> 2) * T_ROW + ((t >> 1) & 1) * 144 + (t & 1) * 128) = u32x4{0, 0, 0, 0};
  }
  STAMP(1);
  bar_lds();
  STAMP(2);
  s16x4 wa[4][4];
  u32x4 w1r[6];
  u64 epoch;
  u32x4 aux16;
  {
    LDS_AS char* p = L + L_T + (16 * cg + (lane >> 2)) * T_ROW + (lane & 1) * 144 + (1 - ((lane >> 1) & 1)) * 16;
    u32x4 wr[8];
#pragma unroll
    for (int r = 0; r < 8; r++) wr[r] = *(LDS_AS u32x4*)(p + r * 16);
    if (w < 7) load_item(1);
    const int k = 16 * (w % 6) + (lane >> 2), rho = k ^ (((k >> 3) & 1) << 2);
    const u32 w1o = pos_channel(rho) * (HID * 2) + (w / 6) * 384 + (lane & 3) * 16;
    const rsrc_t w1rs = make_rsrc(w1, CH * HID * 2);
#pragma unroll
    for (int u = 0; u < 6; u++) w1r[u] = ld128(w1rs, w1o + u * 64);
    epoch = __hip_atomic_load(gws + WS_EPOCH, RLX_AGENT);
    aux16 = ld128(w == 7 ? make_rsrc(grn_b, HID * 2) : make_rsrc(w2, w == 8 ? HID * CH * 2 : 0),
                  w == 7 ? lane * 16 : (lane < 18 ? (idx * 18 + lane) * 128 : 0x80000000u));
#pragma unroll
    for (int i = 0; i < 16; i++) {
      u32x2 v = {wr[2 * (i >> 2)][i & 3], wr[2 * (i >> 2) + 1][i & 3]};
      asm volatile("" : "+v"(v));
      wa[i >> 2][i & 3] = __builtin_bit_cast(s16x4, v);
    }
  }
  STAMP(3);
  store_item(0);
  if (t < 432) store_item(1);
  bar_lds();
  STAMP(4);

  {
    LDS_AS char* xb = L + L_X + (4 * xh + (lane & 3)) * X_REC + (16 * cg + (lane >> 2)) * 8;
    LDS_AS char* xb2 = xb + 70 * X_REC;
    LDS_AS char* vb = L + L_V + (16 * cg + (lane >> 2)) * V_ROW + (4 * xh + (lane & 3)) * 8;
    const bool vok = 4 * xh + (lane & 3) < 7;
    f32x4 acc[7];
#pragma unroll
    for (int i = 0; i < 7; i++) acc[i] = f32x4{0.f, 0.f, 0.f, 0.f};
    s16x4 d[10][4];
    auto load_row = [&](int Y) {
#pragma unroll
      for (int sx = 0; sx < 4; sx++)
        d[Y][sx] = Y < 7 ? *(volatile LDS_AS s16x4*)(xb + (Y * 10 + sx) * X_REC) : *(volatile LDS_AS s16x4*)(xb2 + ((Y - 7) * 10 + sx) * X_REC);
    };
    load_row(0); load_row(1); load_row(2);
    SB();
#pragma unroll
    for (int Y = 0; Y < 10; Y++) {
      if (Y + 3 < 10) load_row(Y + 3);
#pragma unroll
      for (int sx = 0; sx < 4; sx++) {
#pragma unroll
        for (int sy = 3; sy >= 0; sy--) {
          const int Yo = Y - sy;
          if (Yo >= 0 && Yo < 7) acc[Yo] = __builtin_amdgcn_mfma_f32_4x4x4bf16_1k(wa[sy][sx], d[Y][sx], acc[Yo], 0, 0, 0);
        }
      }
      if (Y >= 4) {
        const f32x4 r = acc[Y - 4];
        if (vok) *(LDS_AS u32x2*)(vb + (Y - 4) * 56) = u32x2{pack_bf16(r[0], r[1]), pack_bf16(r[2], r[3])};
      }
      SB();
    }
    if (vok) *(LDS_AS u32x2*)(vb + 6 * 56) = u32x2{pack_bf16(acc[6][0], acc[6][1]), pack_bf16(acc[6][2], acc[6][3])};
  }
  const u32 w1d = (w / 6) * 3 * 6144 + (w % 6) * 1024 + lane * 16;
#pragma unroll
  for (int u = 0; u < 3; u++) *(LDS_AS u32x4*)(L + L_W1E + w1d + u * 6144) = w1r[u];
  if (w == 7 && lane < 48) *(LDS_AS u32x4*)(L + L_B + lane * 16) = aux16;
  asm volatile("" :: "v"(aux16), "v"(code_pf));
  epoch = ((u64)(u32)__builtin_amdgcn_readfirstlane((int)(epoch >> 32)) << 32) | (u32)__builtin_amdgcn_readfirstlane((int)epoch);
  bar_lds();
  STAMP(5);
#pragma unroll
  for (int u = 0; u < 3; u++) *(LDS_AS u32x4*)(L + L_W1L + w1d + u * 6144) = w1r[3 + u];

  const int T = w >> 1, Hh = w & 1;
  layer_norm_unit(L, lane, (4 * w + (lane & 3)) * 8, L_Y + T * 6144 + ((lane >> 4) * 32 + 16 * Hh + (lane & 15)) * 16, 2048);
  if (w == 11) layer_norm_unit(L, lane, 48 * 8, L_YR + ((lane >> 4) * 4 + (lane & 3)) * 16, 256);
  bar_lds();
  STAMP(6);

  const int g4 = lane >> 4, q4 = (lane >> 2) & 3, p4 = lane & 3;
  s16x8 hf[12];
  u32x4 w2r[6], xr[3], xrem;
  float gam;
  const rsrc_t w2rs = make_rsrc(w2, HID * CH * 2);
  const u32 w2o = (t >> 2) * (CH * 2) + (t & 3) * 16;
  const u32 remo = t < 48 ? (u32)(((y0 + 12 + t / 24) * IMG + x0 + 12) * (CH * 2) + (t % 24) * 16) : 0x80000000u;
  const int pidx = 4 * w + ((lane >> 2) & 3);
  const u32 ooff = (u32)(((y0 + 2 * (pidx / 7) + ((lane >> 1) & 1)) * IMG + x0 + 2 * (pidx % 7) + (lane & 1)) * (CH * 2) + g4 * 16);
  const f32x16 zero16 = {0.f, 0.f, 0.f, 0.f, 0.f, 0.f, 0.f, 0.f, 0.f, 0.f, 0.f, 0.f, 0.f, 0.f, 0.f, 0.f};
  {
    const u32 wl = (8 * (g4 >> 1) + q4) * 64 + (g4 & 1) * 32 + p4 * 8 + Hh * 3 * 6144;
    LDS_AS char* wE = L + L_W1E + wl;
    LDS_AS char* wL = L + L_W1L + wl;
    LDS_AS char* sw = L + L_SCR + w * SCR_W + (lane & 31) * 72 + (lane >> 5) * 8;
    LDS_AS char* sr = L + L_SCR + w * SCR_W + (8 * (g4 >> 1) + q4) * 72 + (4 * ((g4 & 1) ^ Hh) + p4) * 8;
    LDS_AS char* ssw = L + L_SS + ((2 * T + (lane >> 5)) * HID + Hh * 192 + (lane & 31)) * 4;
    s16x8 yf[6], wf[6];
    f32x16 acc = zero16;
    {
      LDS_AS char* wp = (T < 3 ? wE : wL) + (T % 3) * 6144;
      s16x8 yr[6], wq[6];
#pragma unroll
      for (int k = 0; k < 6; k++) { yr[k] = *(LDS_AS s16x8*)(L + L_YR + ((2 * k + (lane >> 5)) * 4 + (lane & 3)) * 16); wq[k] = tr_frag(wp + k * 1024, 256); }
#pragma unroll
      for (int k = 0; k < 6; k++) { yf[k] = *(LDS_AS s16x8*)(L + L_Y + T * 6144 + k * 1024 + lane * 16); wf[k] = tr_frag(wE + k * 1024, 256); }
      SB();
      f32x16 ra = zero16;
#pragma unroll
      for (int k = 0; k < 6; k++) ra = mfma32(yr[k], wq[k], ra);
#pragma unroll
      for (int k = 0; k < 6; k++) acc = mfma32(yf[k], wf[k], acc);
#pragma unroll
      for (int k = 0; k < 3; k++) wf[k] = tr_frag(wE + 6144 + k * 1024, 256);
      SB();
      const f32x2 a = gelu2(f32x2{ra[0], ra[1]}), b = gelu2(f32x2{ra[2], ra[3]});
      const f32x2 sq = __builtin_elementwise_fma(a, a, b * b);
      if (lane < 32) {
        const int hc = Hh * 192 + T * 32 + lane;
        *(LDS_AS u32x2*)(L + L_HREM + hc * 8) = u32x2{pack_bf16(a.x, a.y), pack_bf16(b.x, b.y)};
        *(LDS_AS float*)(L + L_SS + (12 * HID + hc) * 4) = sq.x + sq.y;
      }
      SB();
    }
#pragma unroll
    for (int s = 0; s < 6; s++) {
      LDS_AS char* wp = (s + 1 < 3 ? wE : wL) + ((s + 1) % 3) * 6144;
      LDS_AS char* wp2 = (s + 2 < 3 ? wE : wL) + ((s + 2) % 3) * 6144;
      if (s < 5) {
#pragma unroll
        for (int k = 3; k < 6; k++) wf[k] = tr_frag(wp + k * 1024, 256);
      }
      f32x2 xv[8], z[8];
#pragma unroll
      for (int i = 0; i < 8; i++) { xv[i] = f32x2{acc[2 * i], acc[2 * i + 1]}; z[i] = __builtin_elementwise_fma(xv[i], xv[i], f32x2{GK, GK}); }
      SB();
#pragma unroll
      for (int i = 0; i < 8; i++) z[i] *= xv[i];
      SB();
      f32x16 nxt = zero16;
#pragma unroll
      for (int i = 0; i < 16; i++) {
        if (s < 5 && i % 3 == 0 && i < 9) { nxt = mfma32(yf[i / 3], wf[i / 3], nxt); SB(); }
        if (s == 5 && i % 3 == 0) { w2r[i / 3] = ld128(w2rs, w2o + ((i / 3) & 1) * (192 * CH * 2) + (i / 6) * 64); SB(); }
        z[i >> 1][i & 1] = __builtin_amdgcn_exp2f(-z[i >> 1][i & 1]);
        if (i % 3 == 2 && (i < 9 || s == 5)) SB();
      }
      if (s < 4) {
#pragma unroll
        for (int k = 0; k < 3; k++) wf[k] = tr_frag(wp2 + k * 1024, 256);
      }
      SB();
#pragma unroll
      for (int i = 0; i < 8; i++) z[i] += 1.0f;
      SB();
#pragma unroll
      for (int i = 0; i < 16; i++) {
        if (s < 5 && i % 3 == 0 && i < 9) { nxt = mfma32(yf[3 + i / 3], wf[3 + i / 3], nxt); SB(); }
        if (s == 5 && i % 3 == 0 && i < 9) { xr[i / 3] = ld128(xrs, ooff + (i / 3) * 64); SB(); }
        if (s == 5 && i == 9) { xrem = ld128(xrs, remo); gam = bf_lo(grn_g[t < HID ? t : 0]); SB(); }
        z[i >> 1][i & 1] = __builtin_amdgcn_rcpf(z[i >> 1][i & 1]);
        if (i % 3 == 2 && i < 9) SB();
      }
      SB();
#pragma unroll
      for (int i = 0; i < 8; i++) z[i] *= xv[i];
      SB();
      f32x2 ssa = {0.f, 0.f}, ssb = {0.f, 0.f};
#pragma unroll
      for (int Q = 0; Q < 4; Q++) {
        ssa = __builtin_elementwise_fma(z[2 * Q], z[2 * Q], ssa);
        ssb = __builtin_elementwise_fma(z[2 * Q + 1], z[2 * Q + 1], ssb);
        *(volatile LDS_AS u32x2*)(sw + Q * 16) = u32x2{pack_bf16(z[2 * Q].x, z[2 * Q].y), pack_bf16(z[2 * Q + 1].x, z[2 * Q + 1].y)};
      }
      hf[2 * s] = tr_frag(sr, 4 * 72);
      hf[2 * s + 1] = tr_frag(sr + 16 * 72, 4 * 72);
      ssa += ssb;
      *(LDS_AS float*)(ssw + s * 128) = ssa.x + ssa.y;
      acc = nxt;
      SB();
    }
  }
  bar_lds();
  STAMP(7);

  const u64 tag = (epoch + 1) & ((1ull << 40) - 1);
  if (t < HID) {
    float s = 0.f;
#pragma unroll
    for (int i = 0; i < 13; i++) s += *(LDS_AS float*)(L + L_SS + (i * HID + t) * 4);
    __hip_atomic_store(gws + WS_PART + ((t >> 4) * 256 + bid) * 16 + (t & 15), granule(tag, s), RLX_AGENT);
  }

  if (bid < 24) {
    u64 gr[6];
#pragma unroll
    for (int i = 0; i < 6; i++) gr[i] = (t + i * NT < 4096) ? ~(tag << 24) : (tag << 24);
    for (int spin = 0; spin < SPIN_CAP; spin++) {
      bool ok = true;
#pragma unroll
      for (int i = 0; i < 6; i++) if ((gr[i] >> 24) != tag) gr[i] = __hip_atomic_load(gws + WS_PART + bid * 4096 + t + i * NT, RLX_AGENT);
#pragma unroll
      for (int i = 0; i < 6; i++) ok &= (gr[i] >> 24) == tag;
      if (__all(ok)) break;
    }
    float s = 0.f;
#pragma unroll
    for (int i = 0; i < 6; i++) s += granule_val(gr[i]);
    s = sum_xor32(sum_xor16(s));
    if (lane < 16) *(LDS_AS float*)(L + L_RED + (w * 16 + lane) * 4) = s;
    bar_lds();
    if (t < 128) {
      float tot = 0.f;
#pragma unroll
      for (int i = 0; i < 12; i++) tot += *(LDS_AS float*)(L + L_RED + (i * 16 + (t & 15)) * 4);
      const float gx = __builtin_amdgcn_sqrtf(__builtin_fmaf(tot, GA_INV2, 1e-6f));
      __hip_atomic_store(gws + WS_TOT + (t >> 4) * HID + bid * 16 + (t & 15), granule(tag, gx), RLX_AGENT);
    }
  }

#pragma unroll
  for (int i = 0; i < 6; i++) *(LDS_AS u32x4*)(L + L_W2 + (t + i * NT) * 16) = w2r[i];
  bar_lds();
  const int pq = 4 * (g4 & 1) + p4;
  const u32 w2l = (8 * (g4 >> 1) + q4) * 64 + (4 * (pq & 1) + 2 * (pq >> 2) + ((pq >> 1) & 1)) * 8;
  {
    LDS_AS char* ap = L + L_W2 + (w % 3) * 24576 + (w / 3) * 6144 + w2l;
    f32x16 acc = zero16;
    s16x8 af[6], bfr[6];
#pragma unroll
    for (int k = 0; k < 6; k++) { af[k] = tr_frag(ap + k * 1024, 256); bfr[k] = *(LDS_AS s16x8*)(L + L_B + (6 * (w / 3) + k) * 32 + (lane >> 5) * 16); }
    SB();
#pragma unroll
    for (int k = 0; k < 6; k++) acc = mfma32(af[k], bfr[k], acc);
    if ((lane & 31) == 0) {
      LDS_AS char* d = L + L_BP + ((w / 3) * CH + (w % 3) * 32 + (lane >> 5) * 16) * 4;
#pragma unroll
      for (int i = 0; i < 4; i++) *(LDS_AS f32x4*)(d + i * 16) = f32x4{acc[4 * i], acc[4 * i + 1], acc[4 * i + 2], acc[4 * i + 3]};
    }
  }
  bar_lds();
  f32x4 xb[6];
#pragma unroll
  for (int i = 0; i < 6; i++) {
    f32x4 a = f32x4{bf_lo(xr[i >> 1][2 * (i & 1)]), bf_hi(xr[i >> 1][2 * (i & 1)]), bf_lo(xr[i >> 1][2 * (i & 1) + 1]), bf_hi(xr[i >> 1][2 * (i & 1) + 1])};
#pragma unroll
    for (int k = 0; k < 4; k++) a += *(LDS_AS f32x4*)(L + L_BP + (k * CH + (i >> 1) * 32 + g4 * 8 + (i & 1) * 4) * 4);
    asm volatile("" : "+v"(a));
    xb[i] = a;
    SB();
  }
  f32x2 w2f[24];
#pragma unroll
  for (int i = 0; i < 24; i++) w2f[i] = f32x2{bf_lo(w2r[i >> 2][i & 3]), bf_hi(w2r[i >> 2][i & 3])};
#pragma unroll
  for (int i = 0; i < 24; i++) asm volatile("" : "+v"(w2f[i]));
  STAMP(8);

  if (t < HID) {
    u64 gr = ~(tag << 24);
    for (int spin = 0; spin < SPIN_CAP; spin++) {
      if ((gr >> 24) != tag) gr = __hip_atomic_load(gws + WS_TOT + (bid & 7) * HID + t, RLX_AGENT);
      if (__all((gr >> 24) == tag)) break;
    }
    const float gx = granule_val(gr);
    *(LDS_AS float*)(L + L_GG + t * 4) = gam * gx;
    float s = sum_xor32(sum_xor16(gx));
    s += __builtin_bit_cast(float, __builtin_amdgcn_update_dpp(0, __builtin_bit_cast(int, s), 0x128, 0xf, 0xf, false));
    s += __builtin_bit_cast(float, __builtin_amdgcn_update_dpp(0, __builtin_bit_cast(int, s), 0x124, 0xf, 0xf, false));
    s += __builtin_bit_cast(float, __builtin_amdgcn_update_dpp(0, __builtin_bit_cast(int, s), 0x4e, 0xf, 0xf, false));
    s += __builtin_bit_cast(float, __builtin_amdgcn_update_dpp(0, __builtin_bit_cast(int, s), 0xb1, 0xf, 0xf, false));
    if (lane == 0) *(LDS_AS float*)(L + L_PS + w * 4) = s;
  }
  bar_lds();
  STAMP(9);
  if (bid == 0 && t == 0) __hip_atomic_store(gws + WS_EPOCH, epoch + 1, RLX_AGENT);
  {
    const f32x4 pa = *(LDS_AS f32x4*)(L + L_PS);
    const f32x2 pb = *(LDS_AS f32x2*)(L + L_PS + 16);
    const float mean = (((pa[0] + pa[1]) + (pa[2] + pa[3])) + (pb[0] + pb[1])) * (1.0f / HID);
    const float inv = GA_INV * __builtin_amdgcn_rcpf(mean + 1e-6f);
    const float s0 = __builtin_fmaf(*(LDS_AS float*)(L + L_GG + (t >> 2) * 4), inv, GA_INV);
    const float s1 = __builtin_fmaf(*(LDS_AS float*)(L + L_GG + (192 + (t >> 2)) * 4), inv, GA_INV);
#pragma unroll
    for (int i = 0; i < 6; i++) {
      const float s = (i & 1) ? s1 : s0;
      u32x4 o;
#pragma unroll
      for (int j = 0; j < 4; j++) { const f32x2 v = w2f[4 * i + j] * s; o[j] = pack_bf16(v.x, v.y); }
      *(LDS_AS u32x4*)(L + L_W2 + (t + i * NT) * 16) = o;
    }
  }
  bar_lds();
  STAMP(10);

  {
    LDS_AS char* ap = L + L_W2 + Hh * 12 * 1024 + w2l;
    LDS_AS char* ew = L + L_E + w * 4096 + lane * 16;
    LDS_AS char* er = L + L_E + (w ^ 1) * 4096 + lane * 16;
    constexpr int PF = 4;
    const rsrc_t ors = make_rsrc(out, X_BYTES);
    s16x8 af[36];
#pragma unroll
    for (int i = 0; i < PF; i++) af[i] = tr_frag(ap + i * 1024, 256);
    {
      const int o16 = w % 6, kh = w / 6;
      LDS_AS char* rp = L + L_W2 + (o16 >> 1) * 24576 + (kh * 192 + 8 * g4 + q4) * 64 + (4 * (o16 & 1) + p4) * 8;
      LDS_AS char* hp = L + L_HREM + (kh * 192 + 8 * g4 + q4) * 8;
      s16x8 ra_a[6], ra_b[6];
#pragma unroll
      for (int k = 0; k < 6; k++) { ra_a[k] = tr_frag(rp + k * 2048, 256); ra_b[k] = tr_frag(hp + k * 256, 32); }
      SB();
      f32x16 acc = zero16;
      f32x4 ra = {0.f, 0.f, 0.f, 0.f};
      float keep[8];
#pragma unroll
      for (int i = 0; i < 36; i++) {
        const int ot = i / 12, k = i % 12;
        if (k == 0) acc = zero16;
        acc = mfma32(af[i], hf[k], acc);
        if (i + PF < 36) af[i + PF] = tr_frag(ap + ((i + PF) / 12) * 24576 + ((i + PF) % 12) * 1024, 256);
        if (i >= 4 && i < 10) ra = mfma16(ra_a[i - 4], ra_b[i - 4], ra);
        if (i == 11 && (lane & 15) < 4) *(LDS_AS f32x4*)(L + L_RP + ((kh * 4 + (lane & 3)) * CH + 16 * o16 + 4 * g4) * 4) = ra;
        SB();
        if (k == 6 && ot > 0) finish_tile(er + ((ot - 1) & 1) * 2048, keep, xb[2 * ot - 2], xb[2 * ot - 1], ors, ooff + (ot - 1) * 64);
        if (i == 19 && t < 48) {
          const int px = t / 12, oc = t % 12;
          const u32x4 xv = xrem;
          f32x4 r[2];
#pragma unroll
          for (int j = 0; j < 2; j++) {
            f32x4 a2 = f32x4{bf_lo(xv[2 * j]), bf_hi(xv[2 * j]), bf_lo(xv[2 * j + 1]), bf_hi(xv[2 * j + 1])};
#pragma unroll
            for (int kq = 0; kq < 4; kq++) a2 += *(LDS_AS f32x4*)(L + L_BP + (kq * CH + oc * 8 + j * 4) * 4);
            a2 += *(LDS_AS f32x4*)(L + L_RP + (px * CH + oc * 8 + j * 4) * 4);
            a2 += *(LDS_AS f32x4*)(L + L_RP + ((4 + px) * CH + oc * 8 + j * 4) * 4);
            r[j] = a2;
          }
          __builtin_amdgcn_raw_buffer_store_b128(u32x4{pack_bf16(r[0][0], r[0][1]), pack_bf16(r[0][2], r[0][3]), pack_bf16(r[1][0], r[1][1]), pack_bf16(r[1][2], r[1][3])}, ors, remo, 0, ST_AUX);
        }
        if (k == 11) {
          f32x4 send[2];
#pragma unroll
          for (int r = 0; r < 8; r++) {
            auto sw2 = __builtin_amdgcn_permlane16_swap(__float_as_uint(acc[r]), __float_as_uint(acc[r + 8]), false, false);
            keep[r] = __uint_as_float(sw2[0]);
            send[r >> 2][r & 3] = __uint_as_float(sw2[1]);
          }
          *(LDS_AS f32x4*)(ew + (ot & 1) * 2048) = send[0];
          *(LDS_AS f32x4*)(ew + (ot & 1) * 2048 + 1024) = send[1];
          SB();
        }
      }
      finish_tile(er, keep, xb[4], xb[5], ors, ooff + 128);
    }
  }
  STAMP(11);
  if (bid == 0 && t == 0) stamps[0] = __builtin_amdgcn_s_memrealtime();
}

__global__ void pre_k(u64* ws) { if (threadIdx.x == 0) ws[1 << 20] = __builtin_amdgcn_s_memrealtime(); }
__global__ void post_k(u64* ws, int amp) { const u64 n = __builtin_amdgcn_s_memrealtime(); u64 d = n - ws[1 << 20]; if (d > 3000) d = 3000; const u64 e = n + d * amp; while (__builtin_amdgcn_s_memrealtime() < e) __builtin_amdgcn_s_sleep(1); }
extern "C" void kernel_launch(void* const* d_in, const int* in_sizes, int n_in,
                              void* d_out, int out_size, void* d_ws, size_t ws_size,
                              hipStream_t stream) {
  (void)in_sizes; (void)n_in; (void)out_size; (void)ws_size;
  (void)hipFuncSetAttribute((const void*)slak_block_kernel, hipFuncAttributeMaxDynamicSharedMemorySize, LDS_BYTES);
  slak_block_kernel<<<256, NT, LDS_BYTES, stream>>>(
      (const unsigned short*)d_in[0], (const unsigned short*)d_in[1], (const unsigned short*)d_in[2],
      (const unsigned short*)d_in[3], (const unsigned short*)d_in[4], (const unsigned short*)d_in[5],
      (unsigned short*)d_out, (u64*)d_ws);
  post_k<<<1, 64, 0, stream>>>((u64*)d_ws, 0);
}
```

```cpp
#include <hip/hip_runtime.h>
#include <stdint.h>

typedef unsigned long long u64;
typedef unsigned u32;
typedef float f32x2 __attribute__((ext_vector_type(2)));
typedef float f32x4 __attribute__((ext_vector_type(4)));
typedef float f32x16 __attribute__((ext_vector_type(16)));
typedef short s16x4 __attribute__((ext_vector_type(4)));
typedef short s16x8 __attribute__((ext_vector_type(8)));
typedef __bf16 bf16x2 __attribute__((ext_vector_type(2)));
typedef u32 u32x2 __attribute__((ext_vector_type(2)));
typedef u32 u32x4 __attribute__((ext_vector_type(4)));
#define LDS_AS __attribute__((address_space(3)))
typedef __attribute__((address_space(1))) u64 gu64;
#define RLX_AGENT __ATOMIC_RELAXED, __HIP_MEMORY_SCOPE_AGENT
#define DI __device__ __forceinline__

constexpr int IMG = 224, CH = 96, HID = 384, NT = 768;
constexpr u32 X_BYTES = IMG * IMG * CH * 2;

constexpr float GA = 0.4686686930532063f, GK = 4.91222953926424f;
constexpr float GA_INV = 2.1337034344781243f, GA_INV2 = 4.552690346303743f;

constexpr u32 L_X = 0, X_REC = 832;
constexpr u32 L_Y = 0, L_YR = 36864;
constexpr u32 L_W1L = 37632;
constexpr u32 L_SCR = 74496, SCR_W = 2304;
constexpr u32 L_SS = 102144;
constexpr u32 L_V = 83200, V_ROW = 416;
constexpr u32 L_T = 83200, T_ROW = 288;
constexpr u32 L_W1E = 123136;
constexpr u32 L_HREM = 160000;
constexpr u32 L_CNT = 163072;
constexpr u32 L_W2 = 0;
constexpr u32 L_RP = 74496;
constexpr u32 L_BP = 77568;
constexpr u32 L_GG = 77952, L_PS = 79488, L_RED = 79552;
constexpr u32 L_FL = 80320;
constexpr u32 L_TR = 80384;
constexpr u32 LDS_BYTES = 163840;

constexpr u32 TOT_ROW = 512, TOT_BIAS = 384;
constexpr u32 WS_EPOCH = 0, WS_TOT = 64, WS_PART = 64 + 8 * TOT_ROW;
#define CODE_LINES 168
constexpr int SPIN_CAP = 1 << 20;

#define SB() __builtin_amdgcn_sched_barrier(0)
template <int V> struct int_c { static constexpr int value = V; };
template <int N, int I = 0, class F> DI void static_for(F&& f) { if constexpr (I < N) { f(int_c<I>{}); static_for<N, I + 1>(f); } }
DI void bar_lds() { asm volatile("s_waitcnt lgkmcnt(0)\n\ts_barrier" ::: "memory"); }
DI void bar_all() { asm volatile("s_waitcnt vmcnt(0) lgkmcnt(0)\n\ts_barrier" ::: "memory"); }
#define LD_AUX 0
#define POLL_SLEEP 0
#define G1_PRIO 1
#define G2_PRIO 1
#define G2_PF 4
#define ST_AUX 16
typedef __amdgpu_buffer_rsrc_t rsrc_t;
DI rsrc_t make_rsrc(const void* p, u32 bytes) { return __builtin_amdgcn_make_buffer_rsrc((void*)p, 0, bytes, 0x00020000); }
DI u32x4 ld128(rsrc_t r, u32 off) { return __builtin_amdgcn_raw_buffer_load_b128(r, off, 0, LD_AUX); }
DI void dma16(u32x4 rs, u32 voff, u32 soff, u32 lds) {
  u32 keep;
  asm volatile("s_mov_b32 %0, m0\n\ts_mov_b32 m0, %1\n\ts_nop 0\n\tbuffer_load_dwordx4 %2, %3, %4 offen lds\n\ts_mov_b32 m0, %0"
               : "=&s"(keep) : "s"(lds), "v"(voff), "s"(rs), "s"(soff) : "memory");
}
DI u32 pack_bf16(float a, float b) { bf16x2 v = __builtin_convertvector(f32x2{a, b}, bf16x2); return __builtin_bit_cast(u32, v); }
DI float bf_lo(u32 v) { return __uint_as_float(v << 16); }
DI float bf_hi(u32 v) { return __uint_as_float(v & 0xffff0000u); }
DI u32 perm_lo(u32 a, u32 b) { return __builtin_amdgcn_perm(b, a, 0x05040100u); }
DI u32 perm_hi(u32 a, u32 b) { return __builtin_amdgcn_perm(b, a, 0x07060302u); }
DI s16x4 tr_read(LDS_AS char* p) { return __builtin_amdgcn_ds_read_tr16_b64_v4i16((LDS_AS s16x4*)p); }
DI s16x8 tr_frag(LDS_AS char* p, int step) {
  s16x4 a = tr_read(p), b = tr_read(p + step);
  return __builtin_shufflevector(a, b, 0, 1, 2, 3, 4, 5, 6, 7);
}
DI f32x16 mfma32(s16x8 a, s16x8 b, f32x16 c) { return __builtin_amdgcn_mfma_f32_32x32x16_bf16(a, b, c, 0, 0, 0); }
DI f32x4 mfma16(s16x8 a, s16x8 b, f32x4 c) { return __builtin_amdgcn_mfma_f32_16x16x32_bf16(a, b, c, 0, 0, 0); }
DI float sum_xor32(float v) { auto r = __builtin_amdgcn_permlane32_swap(__float_as_uint(v), __float_as_uint(v), false, false); return __uint_as_float(r[0]) + __uint_as_float(r[1]); }
DI float sum_xor16(float v) { auto r = __builtin_amdgcn_permlane16_swap(__float_as_uint(v), __float_as_uint(v), false, false); return __uint_as_float(r[0]) + __uint_as_float(r[1]); }
typedef __attribute__((address_space(1))) char gchar;
DI u64 ws_load(u64* ws, u32 word) { return __hip_atomic_load((gu64*)((gchar*)ws + word * 8u), RLX_AGENT); }
DI void ws_store(u64* ws, u32 word, u64 v) { __hip_atomic_store((gu64*)((gchar*)ws + word * 8u), v, RLX_AGENT); }
DI u64 granule(u64 tag, float v) { return (tag << 24) | (u64)((__float_as_uint(v) + 0x80u) >> 8); }
DI float granule_val(u64 g) { return __uint_as_float((u32)g << 8); }
template <int CTRL> DI float dpp_add(float v) { return v + __builtin_bit_cast(float, __builtin_amdgcn_update_dpp(0, __builtin_bit_cast(int, v), CTRL, 0xf, 0xf, false)); }
DI float wave_sum(float v) { return dpp_add<0xb1>(dpp_add<0x4e>(dpp_add<0x124>(dpp_add<0x128>(sum_xor32(sum_xor16(v)))))); }
DI u32 wave_slot(int w) { return w < 6 ? L_Y + w * 6144 : L_W1E + (w - 6) * 6144; }
DI int pos_channel(int p) { return 8 * ((p % 24) >> 1) + 2 * (p / 24) + (p & 1); }

DI void layer_norm_unit(LDS_AS char* L, int lane, u32 vcol, u32 dst, u32 dst_step) {
  const int g = lane >> 4, q = (lane >> 2) & 3;
  LDS_AS char* p0 = L + L_V + (8 * g + 4 * (g & 1) + q) * V_ROW + vcol;
  LDS_AS char* p1 = L + L_V + (8 * g + 4 * ((g & 1) ^ 1) + q) * V_ROW + vcol;
  u32 v[12];
#pragma unroll
  for (int c = 0; c < 3; c++) {
    u32x2 a = __builtin_bit_cast(u32x2, tr_read(p0 + c * 32 * V_ROW));
    u32x2 b = __builtin_bit_cast(u32x2, tr_read(p1 + c * 32 * V_ROW));
    v[4 * c] = a.x; v[4 * c + 1] = a.y; v[4 * c + 2] = b.x; v[4 * c + 3] = b.y;
  }
  f32x2 f[12];
  f32x2 s = {0.f, 0.f}, ss = {0.f, 0.f};
#pragma unroll
  for (int i = 0; i < 12; i++) { f[i] = f32x2{bf_lo(v[i]), bf_hi(v[i])}; s += f[i]; ss = __builtin_elementwise_fma(f[i], f[i], ss); }
  const float sum = sum_xor32(sum_xor16(s.x + s.y)), sq = sum_xor32(sum_xor16(ss.x + ss.y));
  const float mu = sum * (1.0f / CH);
  const float var = fmaxf(__builtin_fmaf(-mu, mu, sq * (1.0f / CH)), 0.f);
  const float rs = GA * __builtin_amdgcn_rsqf(var + 1e-5f);
  const float off = -mu * rs;
#pragma unroll
  for (int c = 0; c < 3; c++) {
    u32x4 o;
#pragma unroll
    for (int i = 0; i < 4; i++) { f32x2 y = __builtin_elementwise_fma(f[4 * c + i], f32x2{rs, rs}, f32x2{off, off}); o[i] = pack_bf16(y.x, y.y); }
    *(LDS_AS u32x4*)(L + dst + c * dst_step) = o;
  }
}

DI float add1(float a, float b) { float r; asm("v_add_f32 %0, %1, %2" : "=v"(r) : "v"(a), "v"(b)); return r; }
DI void bar_only() { asm volatile("s_barrier" ::: "memory"); }

#define STAMP(i)

__global__ void __launch_bounds__(NT, 3)
slak_block_kernel(const unsigned short* __restrict__ x, const unsigned short* __restrict__ dw, const unsigned short* __restrict__ w1,
                  const unsigned short* __restrict__ w2, const unsigned short* __restrict__ grn_g, const unsigned short* __restrict__ grn_b,
                  unsigned short* __restrict__ out, u64* ws) {
  extern __shared__ __attribute__((aligned(16))) char smem[];
  LDS_AS char* const L = (LDS_AS char*)smem;
  const int t = threadIdx.x, lane = t & 63, bid = blockIdx.x;
  const int w = __builtin_amdgcn_readfirstlane(t >> 6);
  STAMP(0);
  u32 code_pf = 0;
  if (t < CODE_LINES / 4) {
    const char* pc = (const char*)(__builtin_amdgcn_s_getpc() & ~255ull);
    code_pf = *(const u32*)(pc + (((bid >> 3) & 3) + 4 * t) * 128);
  }
  const int xcd = bid & 7, idx = bid >> 3;
  const int y0 = 14 * ((xcd >> 1) * 4 + (idx >> 3)), x0 = 14 * ((xcd & 1) * 8 + (idx & 7));
  const rsrc_t xrs = make_rsrc(x, X_BYTES);

  u32x4 xin[2][4];
  u32 xdst[2];
  auto load_item = [&](int it) {
    const u32 item = t + it * (NT - 320), P = (item * 5462) >> 16, o = item - P * 12, Y = (P * 6554) >> 16, X = P - Y * 10;
    const u32 gy = y0 - 3 + 2 * Y, gx = x0 - 3 + 2 * X;
    u32 base = (u32)(__mul24((int)gy, IMG * CH * 2) + __mul24((int)gx, CH * 2)) + o * 16;
    asm volatile("" : "+v"(base));
    const bool live = item < 1200;
    const bool yk[2] = {live && gy < (u32)IMG, live && gy + 1 < (u32)IMG}, xk[2] = {gx < (u32)IMG, gx + 1 < (u32)IMG};
    xdst[it] = L_X + P * X_REC + o * 16;
#pragma unroll
    for (int k = 0; k < 4; k++) {
      const u32 off = (yk[k >> 1] && xk[k & 1]) ? base + (k >> 1) * (IMG * CH * 2) : 0x80000000u;
      xin[it][k] = ld128(xrs, off + (k & 1) * (CH * 2));
    }
  };
  auto store_item = [&](int it) {
#pragma unroll
    for (int j = 0; j < 4; j++)
      *(LDS_AS u32x4*)(L + xdst[it] + j * 192) = u32x4{perm_lo(xin[it][0][j], xin[it][1][j]), perm_lo(xin[it][2][j], xin[it][3][j]),
                                                       perm_hi(xin[it][0][j], xin[it][1][j]), perm_hi(xin[it][2][j], xin[it][3][j])};
  };
  const int cg = w >> 1, xh = w & 1;
  if (w < 3) {
    const u32 r = ((u32)t * 2731) >> 16, c4 = t - r * 24;
    const rsrc_t drs = make_rsrc(dw, 49 * CH * 2);
    u32x2 dv[7];
#pragma unroll
    for (int j = 0; j < 7; j++) dv[j] = __builtin_amdgcn_raw_buffer_load_b64(drs, r * (7 * CH * 2) + c4 * 8 + j * (CH * 2), 0, LD_AUX);
    load_item(0);
    if (t < 168) {
#pragma unroll
      for (int h = 0; h < 2; h++) {
        LDS_AS char* d = L + L_T + (24 * (2 * (c4 & 1) + h) + 2 * (c4 >> 1)) * T_ROW + (r + 1) * 16;
        *(LDS_AS u32x4*)(d) = u32x4{perm_lo(dv[0][h], dv[1][h]), perm_lo(dv[2][h], dv[3][h]), perm_lo(dv[4][h], dv[5][h]), dv[6][h] & 0xffffu};
        *(LDS_AS u32x4*)(d + 144) = u32x4{dv[0][h] << 16, perm_lo(dv[1][h], dv[2][h]), perm_lo(dv[3][h], dv[4][h]), perm_lo(dv[5][h], dv[6][h])};
        *(LDS_AS u32x4*)(d + T_ROW) = u32x4{perm_hi(dv[0][h], dv[1][h]), perm_hi(dv[2][h], dv[3][h]), perm_hi(dv[4][h], dv[5][h]), dv[6][h] >> 16};
        *(LDS_AS u32x4*)(d + T_ROW + 144) = u32x4{dv[0][h] & 0xffff0000u, perm_hi(dv[1][h], dv[2][h]), perm_hi(dv[3][h], dv[4][h]), perm_hi(dv[5][h], dv[6][h])};
      }
    }
  } else {
    load_item(0);
    if (w >= 5) load_item(1);
    const int z = t - 192;
    if (t == NT - 1) *(LDS_AS u32x2*)(L + L_CNT) = u32x2{0, 0};
    if (z < 384) *(LDS_AS u32x4*)(L + L_T + (z >> 2) * T_ROW + ((z >> 1) & 1) * 144 + (z & 1) * 128) = u32x4{0, 0, 0, 0};
  }
  STAMP(1);
  bar_lds();
  STAMP(2);
  s16x4 wa[4][4];
  {
    LDS_AS char* p = L + L_T + (16 * cg + (lane >> 2)) * T_ROW + (lane & 1) * 144 + (1 - ((lane >> 1) & 1)) * 16;
    u32x4 wr[8];
#pragma unroll
    for (int r = 0; r < 8; r++) wr[r] = *(LDS_AS u32x4*)(p + r * 16);
#pragma unroll
    for (int i = 0; i < 16; i++) {
      u32x2 v = {wr[2 * (i >> 2)][i & 3], wr[2 * (i >> 2) + 1][i & 3]};
      asm volatile("" : "+v"(v));
      wa[i >> 2][i & 3] = __builtin_bit_cast(s16x4, v);
    }
  }
  STAMP(3);
  store_item(0);
  if (w >= 5 && t < 752) store_item(1);
  SB();
  u32x4 w1r[6];
  {
    const int k = 16 * (w % 6) + (lane >> 2), rho = k ^ (((k >> 3) & 1) << 2);
    const u32 w1o = pos_channel(rho) * (HID * 2) + (w / 6) * 384 + (lane & 3) * 16;
    const rsrc_t w1rs = make_rsrc(w1, CH * HID * 2);
#pragma unroll
    for (int u = 0; u < 6; u++) w1r[u] = ld128(w1rs, w1o + u * 64);
  }
  u64 epoch = ws_load(ws, WS_EPOCH);
  u32 bw[6], bb[6];
  if (w == 4) {
#pragma unroll
    for (int i = 0; i < 6; i++) { bw[i] = w2[(lane + 64 * i) * CH + bid % CH]; bb[i] = grn_b[lane + 64 * i]; }
  }
  bar_lds();
  STAMP(4);

  {
    LDS_AS char* xb = L + L_X + (4 * xh + (lane & 3)) * X_REC + (16 * cg + (lane >> 2)) * 8;
    LDS_AS char* xb2 = xb + 70 * X_REC;
    LDS_AS char* vb = L + L_V + (16 * cg + (lane >> 2)) * V_ROW + (4 * xh + (lane & 3)) * 8;
    const bool vok = 4 * xh + (lane & 3) < 7;
    f32x4 acc[7];
#pragma unroll
    for (int i = 0; i < 7; i++) acc[i] = f32x4{0.f, 0.f, 0.f, 0.f};
    s16x4 d[10][4];
    auto load_row = [&](int Y) {
#pragma unroll
      for (int sx = 0; sx < 4; sx++)
        d[Y][sx] = Y < 7 ? *(volatile LDS_AS s16x4*)(xb + (Y * 10 + sx) * X_REC) : *(volatile LDS_AS s16x4*)(xb2 + ((Y - 7) * 10 + sx) * X_REC);
    };
    load_row(0); load_row(1); load_row(2);
    SB();
#pragma unroll
    for (int Y = 0; Y < 10; Y++) {
      if (Y + 3 < 10) load_row(Y + 3);
#pragma unroll
      for (int sx = 0; sx < 4; sx++) {
#pragma unroll
        for (int sy = 3; sy >= 0; sy--) {
          const int Yo = Y - sy;
          if (Yo >= 0 && Yo < 7) acc[Yo] = __builtin_amdgcn_mfma_f32_4x4x4bf16_1k(wa[sy][sx], d[Y][sx], acc[Yo], 0, 0, 0);
        }
      }
      if (Y >= 4) {
        const f32x4 r = acc[Y - 4];
        if (vok) *(LDS_AS u32x2*)(vb + (Y - 4) * 56) = u32x2{pack_bf16(r[0], r[1]), pack_bf16(r[2], r[3])};
      }
      SB();
    }
    if (vok) *(LDS_AS u32x2*)(vb + 6 * 56) = u32x2{pack_bf16(acc[6][0], acc[6][1]), pack_bf16(acc[6][2], acc[6][3])};
  }
  const u32 w1d = (w / 6) * 3 * 6144 + (w % 6) * 1024 + lane * 16;
#pragma unroll
  for (int u = 0; u < 3; u++) *(LDS_AS u32x4*)(L + L_W1E + w1d + u * 6144) = w1r[u];
  asm volatile("" :: "v"(code_pf));
  epoch = ((u64)(u32)__builtin_amdgcn_readfirstlane((int)(epoch >> 32)) << 32) | (u32)__builtin_amdgcn_readfirstlane((int)epoch);
  const u64 tag = (epoch + 1) & ((1ull << 40) - 1);
  if (w == 4) {
    float a = 0.f;
#pragma unroll
    for (int i = 0; i < 6; i++) a = __builtin_fmaf(bf_lo(bw[i]), bf_lo(bb[i]), a);
    a = wave_sum(a);
    if (bid < CH && lane < 8) ws_store(ws, WS_TOT + lane * TOT_ROW + TOT_BIAS + bid, granule(tag, a));
  }
  bar_lds();
  STAMP(5);
#pragma unroll
  for (int u = 0; u < 3; u++) *(LDS_AS u32x4*)(L + L_W1L + w1d + u * 6144) = w1r[3 + u];

  const int T = w >> 1, Hh = w & 1;
  layer_norm_unit(L, lane, (4 * w + (lane & 3)) * 8, L_Y + T * 6144 + ((lane >> 4) * 32 + 16 * Hh + (lane & 15)) * 16, 2048);
  if (w == 11) layer_norm_unit(L, lane, 48 * 8, L_YR + ((lane >> 4) * 4 + (lane & 3)) * 16, 256);
  bar_lds();
  STAMP(6);

  const int g4 = lane >> 4, q4 = (lane >> 2) & 3, p4 = lane & 3;
  s16x8 hk[6], hs[6];
  const u32 totw = WS_TOT + (bid & 7) * TOT_ROW;
  u32 ooff, remo;
  u32x4 xr[3], xrem;
  u32 gam;
  u64 bgr;
  const f32x16 zero16 = {0.f, 0.f, 0.f, 0.f, 0.f, 0.f, 0.f, 0.f, 0.f, 0.f, 0.f, 0.f, 0.f, 0.f, 0.f, 0.f};
  {
    const u32 wl = (8 * (g4 >> 1) + q4) * 64 + (g4 & 1) * 32 + p4 * 8 + Hh * 3 * 6144;
    LDS_AS char* wE = L + L_W1E + wl;
    LDS_AS char* wL = L + L_W1L + wl;
    LDS_AS char* sw = L + L_SCR + w * SCR_W + (lane & 31) * 72 + (lane >> 5) * 8;
    LDS_AS char* srk = L + L_SCR + w * SCR_W + (8 * g4 + q4) * 72 + (4 * Hh + p4) * 8;
    LDS_AS char* srs = L + L_SCR + w * SCR_W + (8 * g4 + q4) * 72 + (4 * (Hh ^ 1) + p4) * 8;
    LDS_AS char* ssw = L + L_SS + ((2 * T + (lane >> 5)) * HID + Hh * 192 + (lane & 31)) * 4;
    LDS_AS char* wr = (T < 3 ? wE : wL) + (T % 3) * 6144;
    LDS_AS char* yr = L + L_YR + ((lane >> 5) * 4 + (lane & 3)) * 16;
    s16x8 yf[6], wf[6], ry[2], rw[2];
    f32x16 acc = zero16, racc = zero16;
    if (G1_PRIO) __builtin_amdgcn_s_setprio(3);
#pragma unroll
    for (int k = 0; k < 6; k++) { yf[k] = *(LDS_AS s16x8*)(L + L_Y + T * 6144 + k * 1024 + lane * 16); wf[k] = tr_frag(wE + k * 1024, 256); }
    SB();
#pragma unroll
    for (int k = 0; k < 6; k++) {
      acc = mfma32(yf[k], wf[k], acc);
      if (k < 3) wf[k] = tr_frag(wE + 6144 + k * 1024, 256);
      SB();
    }
    const u32 w2raw = wave_slot(w);
    auto arrive = [&](int c) { if (lane == 0) asm volatile("ds_add_u32 %0, %1" :: "v"(L_CNT + 4 * c), "v"(1u) : "memory"); };
    auto fetch_w2 = [&](int c) {
#pragma unroll 1
      for (int spin = 0; spin < SPIN_CAP && __builtin_amdgcn_readfirstlane(*(volatile LDS_AS u32*)(L + L_CNT + 4 * c)) < 12; spin++) {}
      const u32x4 rs = {(u32)(u64)w2, (u32)((u64)w2 >> 32) & 0xffffu, HID * CH * 2, 0x00020000};
      const u32 vo = (32 * w + 4 * ((lane >> 2) & 7) + (lane >> 5)) * (CH * 2) + (lane & 3) * 16;
#pragma unroll
      for (int j = 0; j < 6; j++) dma16(rs, vo, (j & 1) * 384 + (j >> 1) * 64, w2raw + j * 1024);
    };
    arrive(0);
    static_for<6>([&](auto sc_) {
      constexpr int s = sc_.value, NP = s == 3 ? 10 : 8;
      LDS_AS char* wp = (s + 1 < 3 ? wE : wL) + ((s + 1) % 3) * 6144;
      LDS_AS char* wp2 = (s + 2 < 3 ? wE : wL) + ((s + 2) % 3) * 6144;
      if constexpr (G1_PRIO && s == 2) __builtin_amdgcn_s_setprio(2);
      if constexpr (G1_PRIO && s == 4) __builtin_amdgcn_s_setprio(1);
      if constexpr (G1_PRIO && s == 5) __builtin_amdgcn_s_setprio(0);
      if constexpr (s == 5) {
        const u32 pi = 4 * w + q4, py = (pi * 37) >> 8, r24 = lane >= 24;
        ooff = (u32)__mul24((y0 + 2 * py + (p4 >> 1)) * IMG + x0 + 2 * (pi - 7 * py) + (p4 & 1), CH * 2) + g4 * 16;
        remo = lane < 48 ? (u32)(((y0 + 12) * IMG + x0 + 12) * (CH * 2)) + r24 * (IMG * CH * 2 - 384) + lane * 16 : 0x80000000u;
      }
      if constexpr (s == 3) arrive(1);
      if constexpr (s == 1) { if (w < 6) fetch_w2(0); }
      if constexpr (s == 4) { if (w >= 6) fetch_w2(1); }
      if constexpr (s < 5) {
#pragma unroll
        for (int k = 3; k < 6; k++) wf[k] = tr_frag(wp + k * 1024, 256);
      }
      if constexpr (s < 3) {
#pragma unroll
        for (int j = 0; j < 2; j++) { ry[j] = *(LDS_AS s16x8*)(yr + (2 * s + j) * 128); rw[j] = tr_frag(wr + (2 * s + j) * 1024, 256); }
      }
      f32x2 xv[NP], z[NP];
#pragma unroll
      for (int i = 0; i < NP; i++) {
        xv[i] = i < 8 ? f32x2{acc[2 * (i & 7)], acc[2 * (i & 7) + 1]} : f32x2{racc[2 * (i & 7)], racc[2 * (i & 7) + 1]};
        z[i] = __builtin_elementwise_fma(xv[i], xv[i], f32x2{GK, GK});
      }
      SB();
#pragma unroll
      for (int i = 0; i < NP; i++) z[i] *= xv[i];
      SB();
      f32x16 nxt = zero16;
#pragma unroll
      for (int i = 0; i < 2 * NP; i++) {
        if (s < 5 && i % 3 == 0 && i < 9) { nxt = mfma32(yf[i / 3], wf[i / 3], nxt); SB(); }
        if (s < 3 && i == 9) { racc = mfma32(ry[0], rw[0], racc); SB(); }
        z[i >> 1][i & 1] = __builtin_amdgcn_exp2f(-z[i >> 1][i & 1]);
        if (i % 3 == 2 && i < 12) SB();
      }
      if constexpr (s < 4) {
#pragma unroll
        for (int k = 0; k < 3; k++) wf[k] = tr_frag(wp2 + k * 1024, 256);
      }
      SB();
#pragma unroll
      for (int i = 0; i < NP; i++) z[i] += 1.0f;
      SB();
#pragma unroll
      for (int i = 0; i < 2 * NP; i++) {
        if (s < 5 && i % 3 == 0 && i < 9) { nxt = mfma32(yf[3 + i / 3], wf[3 + i / 3], nxt); SB(); }
        if (s < 3 && i == 9) { racc = mfma32(ry[1], rw[1], racc); SB(); }
        if (s == 5 && i % 3 == 0 && i < 9) { xr[i / 3] = ld128(xrs, ooff + (i / 3) * 64); SB(); }
        if (s == 5 && i == 9) { xrem = ld128(xrs, remo); SB(); }
        if (s == 5 && i == 12) { gam = grn_g[t < HID ? t : 0]; bgr = ws_load(ws, totw + TOT_BIAS + (t & 127)); SB(); }
        z[i >> 1][i & 1] = __builtin_amdgcn_rcpf(z[i >> 1][i & 1]);
        if (i % 3 == 2 && i < 12) SB();
      }
      SB();
#pragma unroll
      for (int i = 0; i < NP; i++) z[i] *= xv[i];
      SB();
      f32x2 ssa = z[0] * z[0], ssb = z[1] * z[1];
#pragma unroll
      for (int Q = 0; Q < 4; Q++) {
        if (Q) { ssa = __builtin_elementwise_fma(z[2 * Q], z[2 * Q], ssa); ssb = __builtin_elementwise_fma(z[2 * Q + 1], z[2 * Q + 1], ssb); }
        *(volatile LDS_AS u32x2*)(sw + Q * 16) = u32x2{pack_bf16(z[2 * Q].x, z[2 * Q].y), pack_bf16(z[2 * Q + 1].x, z[2 * Q + 1].y)};
      }
      hk[s] = tr_frag(srk, 4 * 72);
      hs[s] = tr_frag(srs, 4 * 72);
      ssa += ssb;
      *(LDS_AS float*)(ssw + s * 128) = ssa.x + ssa.y;
      if constexpr (s == 3) {
        const f32x2 sq = __builtin_elementwise_fma(z[8], z[8], z[9] * z[9]);
        if (lane < 32) {
          const int hc = Hh * 192 + T * 32 + lane;
          const u32 p01 = pack_bf16(z[8].x, z[8].y), p23 = pack_bf16(z[9].x, z[9].y);
          LDS_AS unsigned short* hr = (LDS_AS unsigned short*)(L + L_HREM + (hc >> 3) * 64 + (hc & 7) * 2);
          hr[0] = (unsigned short)p01; hr[8] = (unsigned short)(p01 >> 16); hr[16] = (unsigned short)p23; hr[24] = (unsigned short)(p23 >> 16);
          *(LDS_AS float*)(L + L_SS + (12 * HID + hc) * 4) = sq.x + sq.y;
        }
      }
      acc = nxt;
      SB();
    });
  }
  asm volatile("" : "+v"(xr[0]), "+v"(xr[1]), "+v"(xr[2]), "+v"(xrem), "+v"(gam), "+v"(bgr));
  bar_all();
  STAMP(7);

  if (t < HID) {
    float v[13];
#pragma unroll
    for (int i = 0; i < 13; i++) v[i] = *(LDS_AS float*)(L + L_SS + (i * HID + t) * 4);
    const float s = (((v[0] + v[1]) + (v[2] + v[3])) + ((v[4] + v[5]) + (v[6] + v[7]))) + (((v[8] + v[9]) + (v[10] + v[11])) + v[12]);
    ws_store(ws, WS_PART + (((u32)t >> 4) * 256 + bid) * 16 + (t & 15), granule(tag, s));
  }
  const int o16 = w % 6, kh = w / 6;
  const f32x4 zero4 = {0.f, 0.f, 0.f, 0.f};
  u32 ap[4], rp, hp, rw, w2d0, w2d1, tw, tr, soff;
  f32x2 w2f[24];
  auto prepare = [&]() {
    SB();
    u32x2 w2r[12];
#pragma unroll
    for (int i = 0; i < 12; i++) w2r[i] = *(volatile LDS_AS u32x2*)(L + wave_slot(w) + lane * 8 + i * 512);
#pragma unroll
    for (int i = 0; i < 6; i++) *(volatile LDS_AS s16x8*)(L + wave_slot(w) + lane * 16 + i * 1024) = hs[i];
    if (t == 512) *(LDS_AS u32*)(L + L_FL) = 0;
    SB();
    const u32 w2d = L_W2 + ((t & 1) * 12 + w) * 1024 + (16 * g4 + ((t >> 1) & 3)) * 16 + (t & 8);
    const u32 aslot = L_W2 + (16 * g4 + q4) * 16;
    ap[0] = aslot + p4 * 64 + Hh * 6144; ap[1] = aslot + p4 * 64 + (Hh ^ 1) * 6144;
    ap[2] = aslot + (p4 ^ 1) * 64 + Hh * 6144 + 12288; ap[3] = aslot + (p4 ^ 1) * 64 + (Hh ^ 1) * 6144 + 12288;
    rp = aslot + (p4 ^ (o16 & 1)) * 64 + (o16 * 12 + kh * 6) * 1024;
    hp = L_HREM + (kh * 24 + g4) * 64 + p4 * 16;
    rw = L_RP + (kh * 4 * CH + 32 * (o16 >> 1) + 4 * (o16 & 1)) * 4 + p4 * (CH * 4) + g4 * 32;
    w2d0 = w2d + (t & 1) * 64; w2d1 = w2d + ((t & 1) ^ 1) * 64;
    tw = L_TR + w * 1024 + (lane & 15) * 64 + ((g4 ^ ((lane >> 1) & 3)) << 4);
    tr = L_TR + w * 1024 + (lane >> 2) * 64 + ((p4 ^ ((lane >> 3) & 3)) << 4);
    soff = (u32)__builtin_amdgcn_ds_bpermute(lane & 0x3c, (int)ooff) + p4 * 16;
    asm volatile("" : "+v"(ap[0]), "+v"(ap[1]), "+v"(ap[2]), "+v"(ap[3]), "+v"(rp), "+v"(hp), "+v"(rw), "+v"(tw), "+v"(tr), "+v"(w2d0), "+v"(w2d1), "+v"(soff));
#pragma unroll
    for (int i = 0; i < 24; i++) {
      const int ot = i >> 3, r = (i >> 1) & 3, kp = i & 1;
      const u32 a = w2r[4 * ot + 2 * kp][r >> 1], b = w2r[4 * ot + 2 * kp + 1][r >> 1];
      w2f[i] = (r & 1) ? f32x2{bf_hi(a), bf_hi(b)} : f32x2{bf_lo(a), bf_lo(b)};
      asm volatile("" : "+v"(w2f[i]));
    }
    SB();
  };
  {
    const bool mine = (u32)(t - HID) < CH;
    if (!__all(!mine || (bgr >> 24) == tag)) {
#pragma unroll 1
      for (int spin = 0; spin < SPIN_CAP; spin++) {
        bgr = ws_load(ws, totw + TOT_BIAS + (t & 127));
        if (__all(!mine || (bgr >> 24) == tag)) break;
      }
    }
    if (mine) *(LDS_AS float*)(L + L_BP + (t - HID) * 4) = granule_val(bgr);
  }
  if (bid < 24) {
    u64 gr[6];
    const u32 partw = WS_PART + bid * 4096 + t;
#pragma unroll
    for (int i = 0; i < 6; i++) gr[i] = ws_load(ws, partw + (i < 5 || t < 256 ? i * NT : 0));
    prepare();
#pragma unroll 1
    for (int spin = 0; spin < SPIN_CAP; spin++) {
      bool ok = true;
#pragma unroll
      for (int i = 0; i < 6; i++) ok &= (gr[i] >> 24) == tag;
      if (__all(ok)) break;
#pragma unroll
      for (int i = 0; i < 6; i++)
        if ((gr[i] >> 24) != tag) gr[i] = ws_load(ws, partw + (i < 5 || t < 256 ? i * NT : 0));
    }
    float s = ((granule_val(gr[0]) + granule_val(gr[1])) + (granule_val(gr[2]) + granule_val(gr[3]))) + (granule_val(gr[4]) + (t < 256 ? granule_val(gr[5]) : 0.f));
    s = sum_xor32(sum_xor16(s));
    if (lane < 16) *(LDS_AS float*)(L + L_RED + (lane * 12 + w) * 4) = s;
  } else {
    prepare();
  }
  bar_lds();
  if (bid < 24 && w < 2) {
    LDS_AS char* q = L + L_RED + (t & 15) * 48;
    const f32x4 a = *(LDS_AS f32x4*)(q) + *(LDS_AS f32x4*)(q + 16) + *(LDS_AS f32x4*)(q + 32);
    const float gx = __builtin_amdgcn_sqrtf(__builtin_fmaf((a[0] + a[1]) + (a[2] + a[3]), GA_INV2, 1e-6f));
    ws_store(ws, WS_TOT + (t >> 4) * TOT_ROW + bid * 16 + (t & 15), granule(tag, gx));
  }
  SB();
#pragma unroll
  for (int i = 0; i < 6; i++) hs[i] = *(LDS_AS s16x8*)(L + wave_slot(w ^ 1) + lane * 16 + i * 1024);
  f32x4 acc2[3][2];
#pragma unroll
  for (int i = 0; i < 6; i++) acc2[i >> 1][i & 1] = *(LDS_AS f32x4*)(L + L_BP + g4 * 32 + (i >> 1) * 128 + (i & 1) * 16);
  SB();
#pragma unroll
  for (int i = 0; i < 6; i++) {
    const u32x4 xv = xr[i >> 1];
    acc2[i >> 1][i & 1] += f32x4{bf_lo(xv[2 * (i & 1)]), bf_hi(xv[2 * (i & 1)]), bf_lo(xv[2 * (i & 1) + 1]), bf_hi(xv[2 * (i & 1) + 1])};
    asm volatile("" : "+v"(acc2[i >> 1][i & 1]));
  }
#pragma unroll
  for (int i = 0; i < 6; i++) asm volatile("" : "+v"(hs[i]));
  STAMP(8);

  if (t < HID) {
    u64 g;
#pragma unroll 1
    for (int spin = 0; spin < SPIN_CAP; spin++) {
      g = ws_load(ws, totw + t);
      if (__all((g >> 24) == tag)) break;
      if (POLL_SLEEP) __builtin_amdgcn_s_sleep(POLL_SLEEP);
    }
    const float gx = granule_val(g);
    *(LDS_AS float*)(L + L_GG + t * 4) = bf_lo(gam) * gx;
    const float s = wave_sum(gx);
    if (lane == 0) *(LDS_AS float*)(L + L_PS + w * 4) = s;
  }
  bar_lds();
  STAMP(9);
  {
    const f32x4 pa = *(LDS_AS f32x4*)(L + L_PS);
    const f32x2 pb = *(LDS_AS f32x2*)(L + L_PS + 16);
    const f32x4 gg = *(LDS_AS f32x4*)(L + L_GG + (t >> 3) * 16);
    if (bid == 0 && t == NT - 1) ws_store(ws, WS_EPOCH, epoch + 1);
    const float mean = (((pa[0] + pa[1]) + (pa[2] + pa[3])) + (pb[0] + pb[1])) * (1.0f / HID);
    const float inv = GA_INV * __builtin_amdgcn_rcpf(mean + 1e-6f);
    const f32x2 s01 = f32x2{gg[0], gg[1]} * inv + GA_INV, s23 = f32x2{gg[2], gg[3]} * inv + GA_INV;
#pragma unroll
    for (int i = 0; i < 12; i++) {
      const f32x2 a = w2f[2 * i] * s01, b = w2f[2 * i + 1] * s23;
      *(volatile LDS_AS u32x2*)(L + (i & 1 ? w2d1 : w2d0) + ((i >> 1) & 1) * 128 + (i >> 2) * 24576) = u32x2{pack_bf16(a.x, a.y), pack_bf16(b.x, b.y)};
    }
  }
  bar_lds();
  STAMP(10);

  {
    const rsrc_t ors = make_rsrc(out, X_BYTES);
    constexpr int PF = G2_PF;
    s16x8 af[72], ra_a[6], ra_b[6];
    f32x4 ra = zero4;
    u32x4 outv;
    u32 rem_cnt = 0;
    auto load_a = [&](int i) { af[i] = *(volatile LDS_AS s16x8*)(L + ap[(i / 6) & 3] + (i / 24) * 24576 + (i % 6) * 1024); };
    auto pack_tile = [&](int ot) {
      *(volatile LDS_AS u32x4*)(L + tw) = u32x4{pack_bf16(acc2[ot][0][0], acc2[ot][0][1]), pack_bf16(acc2[ot][0][2], acc2[ot][0][3]),
                                                pack_bf16(acc2[ot][1][0], acc2[ot][1][1]), pack_bf16(acc2[ot][1][2], acc2[ot][1][3])};
      outv = *(volatile LDS_AS u32x4*)(L + tr);
    };
    auto store_tile = [&](int ot) { __builtin_amdgcn_raw_buffer_store_b128(outv, ors, soff + ot * 64, 0, ST_AUX); };
    if (G2_PRIO) __builtin_amdgcn_s_setprio(3);
#pragma unroll
    for (int i = 0; i < PF; i++) load_a(i);
    SB();
    static_for<72>([&](auto ic) {
      constexpr int i = ic.value, ot = i / 24, rt = (i / 12) & 1, k = i % 12;
      acc2[ot][rt] = mfma16(af[i], k < 6 ? hk[k % 6] : hs[k % 6], acc2[ot][rt]);
      if constexpr (i + PF < 72) load_a(i + PF);
      if constexpr (G2_PRIO && i % 18 == 17 && i < 60) __builtin_amdgcn_s_setprio(2 - i / 18);
      if constexpr (i >= 2 && i < 38 && i % 6 == 2) { ra_a[i / 6] = *(volatile LDS_AS s16x8*)(L + rp + (i / 6) * 1024); ra_b[i / 6] = *(volatile LDS_AS s16x8*)(L + hp + (i / 6) * 256); }
      if constexpr (i >= 10 && i < 46 && i % 6 == 4) ra = mfma16(ra_a[i / 6 - 1], ra_b[i / 6 - 1], ra);
      if constexpr (i == 43) {
        if (q4 == 0) *(volatile LDS_AS f32x4*)(L + rw) = ra;
        if (lane == 0) asm volatile("ds_add_rtn_u32 %0, %1, %2" : "=v"(rem_cnt) : "v"(L_FL), "v"(1u) : "memory");
      }
      if constexpr (i == 25 || i == 49) pack_tile(ot - 1);
      if constexpr (i == 31 || i == 55) store_tile(ot - 1);
      if constexpr (i == 23) { STAMP(11); }
      if constexpr (i == 47) { STAMP(12); }
      if constexpr (i == 58) {
        asm volatile("" : "+v"(rem_cnt));
        if (__builtin_amdgcn_readfirstlane(rem_cnt) == 11 && lane < 48) {
          f32x4 xq[2];
#pragma unroll
          for (int j = 0; j < 2; j++) {
            const f32x4 a = *(volatile LDS_AS f32x4*)(L + L_RP + (lane * 8 + j * 4) * 4), b = *(volatile LDS_AS f32x4*)(L + L_RP + (4 * CH + lane * 8 + j * 4) * 4);
            const f32x4 c = *(volatile LDS_AS f32x4*)(L + L_BP + (lane - 12 * ((lane * 43) >> 9)) * 32 + j * 16);
#pragma unroll
            for (int e = 0; e < 4; e++) xq[j][e] = add1(add1((e & 1) ? bf_hi(xrem[2 * j + (e >> 1)]) : bf_lo(xrem[2 * j + (e >> 1)]), c[e]), add1(a[e], b[e]));
          }
          __builtin_amdgcn_raw_buffer_store_b128(u32x4{pack_bf16(xq[0][0], xq[0][1]), pack_bf16(xq[0][2], xq[0][3]), pack_bf16(xq[1][0], xq[1][1]), pack_bf16(xq[1][2], xq[1][3])}, ors, remo, 0, ST_AUX);
        }
      }
      SB();
    });
    STAMP(13);
    pack_tile(2);
    store_tile(2);
  }
  STAMP(14);
}

extern "C" void kernel_launch(void* const* d_in, const int* in_sizes, int n_in,
                              void* d_out, int out_size, void* d_ws, size_t ws_size,
                              hipStream_t stream) {
  (void)in_sizes; (void)n_in; (void)out_size; (void)ws_size;
  (void)hipFuncSetAttribute((const void*)slak_block_kernel, hipFuncAttributeMaxDynamicSharedMemorySize, LDS_BYTES);
  slak_block_kernel<<<256, NT, LDS_BYTES, stream>>>(
      (const unsigned short*)d_in[0], (const unsigned short*)d_in[1], (const unsigned short*)d_in[2],
      (const unsigned short*)d_in[3], (const unsigned short*)d_in[4], (const unsigned short*)d_in[5],
      (unsigned short*)d_out, (u64*)d_ws);
}
```

```cpp
#include <hip/hip_runtime.h>
#include <stdint.h>

typedef unsigned long long u64;
typedef unsigned u32;
typedef float f32x2 __attribute__((ext_vector_type(2)));
typedef float f32x4 __attribute__((ext_vector_type(4)));
typedef float f32x16 __attribute__((ext_vector_type(16)));
typedef short s16x4 __attribute__((ext_vector_type(4)));
typedef short s16x8 __attribute__((ext_vector_type(8)));
typedef __bf16 bf16x2 __attribute__((ext_vector_type(2)));
typedef u32 u32x2 __attribute__((ext_vector_type(2)));
typedef u32 u32x4 __attribute__((ext_vector_type(4)));
#define LDS_AS __attribute__((address_space(3)))
typedef __attribute__((address_space(1))) u64 gu64;
typedef __attribute__((address_space(1))) unsigned short gu16;
typedef __attribute__((address_space(1))) const unsigned short gcu16;
#define RLX_AGENT __ATOMIC_RELAXED, __HIP_MEMORY_SCOPE_AGENT
#define DI __device__ __forceinline__

constexpr int IMG = 224, CH = 96, HID = 384, NT = 768;
constexpr u32 X_BYTES = IMG * IMG * CH * 2;

constexpr float GA = 0.4686686930532063f, GK = 4.91222953926424f;
constexpr float GC = 0x1p-30f, GS = 30.f;
constexpr float GA_INV = 2.1337034344781243f * 0x1p-30f, GA_INV2 = 4.552690346303743f * 0x1p-60f;

constexpr u32 L_X = 0, X_REC = 1600;
constexpr u32 L_Y = 0, L_YR = 36864;
constexpr u32 L_W1L = 37632;
constexpr u32 L_SCR = 74496, SCR_W = 2304;
constexpr u32 L_SS = 102144;
constexpr u32 L_V = 83200, V_ROW = 416;
constexpr u32 L_T = 83200, T_ROW = 144;
constexpr u32 L_W1E = 123136;
constexpr u32 L_HREM = 160000;
constexpr u32 L_CNT = 163072;
constexpr u32 L_W2 = 0;
constexpr u32 L_RP = 74496;
constexpr u32 L_BP = 77568;
constexpr u32 L_GG = 77952, L_PS = 79488, L_RED = 79552;
constexpr u32 L_FL = 80320;
constexpr u32 LDS_BYTES = 163840;

constexpr u32 TOT_ROW = 512, TOT_BIAS = 384;
constexpr u32 WS_EPOCH = 0, WS_TOT = 64, WS_PART = 64 + 8 * TOT_ROW;
#define CODE_LINES 172
constexpr int SPIN_CAP = 1 << 20;

#define SB() __builtin_amdgcn_sched_barrier(0)
#define PIN(v) asm volatile("" : "+v"(v))
template <int V> struct int_c { static constexpr int value = V; };
template <int N, int I = 0, class F> DI void static_for(F&& f) { if constexpr (I < N) { f(int_c<I>{}); static_for<N, I + 1>(f); } }
DI void bar_lds() { asm volatile("s_waitcnt lgkmcnt(0)\n\ts_barrier" ::: "memory"); }
DI void bar_all() { asm volatile("s_waitcnt vmcnt(0) lgkmcnt(0)\n\ts_barrier" ::: "memory"); }
#define LD_AUX 0
#define FE_CODE 1
#define FE_WARM 1
#define LN_LAST 2
#define POLL_SLEEP 0
#define G1_PRIO 0x1af
#define G2_PRIO 1
#define G2_PF 4
#define ST_AUX 16
typedef __amdgpu_buffer_rsrc_t rsrc_t;
DI rsrc_t make_rsrc(gcu16* p, u32 bytes) { return __builtin_amdgcn_make_buffer_rsrc((void*)p, 0, bytes, 0x00020000); }
DI u32x4 ld128(rsrc_t r, u32 off) { return __builtin_amdgcn_raw_buffer_load_b128(r, off, 0, LD_AUX); }
DI void dma16(u32x4 rs, u32 voff, u32 soff, u32 lds) {
  u32 keep;
  asm volatile("s_mov_b32 %0, m0\n\ts_mov_b32 m0, %1\n\ts_nop 0\n\tbuffer_load_dwordx4 %2, %3, %4 offen lds\n\ts_mov_b32 m0, %0"
               : "=&s"(keep) : "s"(lds), "v"(voff), "s"(rs), "s"(soff) : "memory");
}
DI u32 pack_bf16(float a, float b) { bf16x2 v = __builtin_convertvector(f32x2{a, b}, bf16x2); return __builtin_bit_cast(u32, v); }
DI float bf_lo(u32 v) { return __uint_as_float(v << 16); }
DI float bf_hi(u32 v) { return __uint_as_float(v & 0xffff0000u); }
DI u32 perm_lo(u32 a, u32 b) { return __builtin_amdgcn_perm(b, a, 0x05040100u); }
DI u32 perm_hi(u32 a, u32 b) { return __builtin_amdgcn_perm(b, a, 0x07060302u); }
DI s16x4 tr_read(LDS_AS char* p) { return __builtin_amdgcn_ds_read_tr16_b64_v4i16((LDS_AS s16x4*)p); }
DI s16x8 tr_frag(LDS_AS char* p, int step) {
  s16x4 a = tr_read(p), b = tr_read(p + step);
  return __builtin_shufflevector(a, b, 0, 1, 2, 3, 4, 5, 6, 7);
}
DI f32x16 mfma32(s16x8 a, s16x8 b, f32x16 c) { return __builtin_amdgcn_mfma_f32_32x32x16_bf16(a, b, c, 0, 0, 0); }
DI f32x4 mfma16(s16x8 a, s16x8 b, f32x4 c) { return __builtin_amdgcn_mfma_f32_16x16x32_bf16(a, b, c, 0, 0, 0); }
DI float sum_xor32(float v) { auto r = __builtin_amdgcn_permlane32_swap(__float_as_uint(v), __float_as_uint(v), false, false); return __uint_as_float(r[0]) + __uint_as_float(r[1]); }
DI float sum_xor16(float v) { auto r = __builtin_amdgcn_permlane16_swap(__float_as_uint(v), __float_as_uint(v), false, false); return __uint_as_float(r[0]) + __uint_as_float(r[1]); }
typedef __attribute__((address_space(1))) char gchar;
DI u64 ws_load(gu64* ws, u32 word) { return __hip_atomic_load((gu64*)((gchar*)ws + word * 8u), RLX_AGENT); }
DI void ws_store(gu64* ws, u32 word, u64 v) { __hip_atomic_store((gu64*)((gchar*)ws + word * 8u), v, RLX_AGENT); }
DI u64 granule(u64 tag, float v) { return (tag << 24) | (u64)((__float_as_uint(v) + 0x80u) >> 8); }
DI float granule_val(u64 g) { return __uint_as_float((u32)g << 8); }
template <int CTRL> DI float dpp_add(float v) { return v + __builtin_bit_cast(float, __builtin_amdgcn_update_dpp(0, __builtin_bit_cast(int, v), CTRL, 0xf, 0xf, false)); }
DI float wave_sum(float v) { return dpp_add<0xb1>(dpp_add<0x4e>(dpp_add<0x124>(dpp_add<0x128>(sum_xor32(sum_xor16(v)))))); }
DI u32 wave_slot(int w) { return w < 6 ? L_Y + w * 6144 : L_W1E + (w - 6) * 6144; }
DI u32 pos_channel(u32 p) { const u32 e = (p * 43) >> 9; return 8 * (p - 12 * e) + e; }

template <class F> DI void layer_norm_unit(LDS_AS char* L, int lane, u32 vcol, u32 dst, u32 dst_step, F&& between) {
  const int g = lane >> 4, q = (lane >> 2) & 3;
  LDS_AS char* p0 = L + L_V + (8 * g + 4 * (g & 1) + q) * V_ROW + vcol;
  LDS_AS char* p1 = L + L_V + (8 * g + 4 * ((g & 1) ^ 1) + q) * V_ROW + vcol;
  s16x8 vf[3];
#pragma unroll
  for (int c = 0; c < 3; c++)
    vf[c] = __builtin_shufflevector(tr_read(p0 + c * 32 * V_ROW), tr_read(p1 + c * 32 * V_ROW), 0, 1, 2, 3, 4, 5, 6, 7);
  between();
  const s16x8 ones = {0x3f80, 0x3f80, 0x3f80, 0x3f80, 0x3f80, 0x3f80, 0x3f80, 0x3f80};
  f32x4 ms = {0.f, 0.f, 0.f, 0.f}, mq = {0.f, 0.f, 0.f, 0.f};
#pragma unroll
  for (int c = 0; c < 3; c++) { mq = mfma16(vf[c], vf[c], mq); ms = mfma16(ones, vf[c], ms); }
  f32x2 f[12];
#pragma unroll
  for (int i = 0; i < 12; i++) { const u32 v = __builtin_bit_cast(u32x4, vf[i >> 2])[i & 3]; f[i] = f32x2{bf_lo(v), bf_hi(v)}; }
  const float dg = (lane & 2) ? ((lane & 1) ? mq[3] : mq[2]) : ((lane & 1) ? mq[1] : mq[0]);
  const float sq = __int_as_float(__builtin_amdgcn_ds_bpermute(((lane & 15) + (lane & 12) * 4) * 4, __float_as_int(dg))), sum = ms[0];
  const float mu = sum * (1.0f / CH);
  const float var = fmaxf(__builtin_fmaf(-mu, mu, sq * (1.0f / CH)), 0.f);
  const float rs = GA * __builtin_amdgcn_rsqf(var + 1e-5f);
  const float off = -mu * rs;
#pragma unroll
  for (int c = 0; c < 3; c++) {
    u32x4 o;
#pragma unroll
    for (int i = 0; i < 4; i++) { f32x2 y = __builtin_elementwise_fma(f[4 * c + i], f32x2{rs, rs}, f32x2{off, off}); o[i] = pack_bf16(y.x, y.y); }
    *(LDS_AS u32x4*)(L + dst + c * dst_step) = o;
  }
}

DI void layer_norm_last(LDS_AS char* L, int lane) {
  const int cq = lane >> 2, q = lane & 3, oq = 4 * (lane >> 4) + (lane & 3);
  const u32x2 a = __builtin_bit_cast(u32x2, tr_read(L + L_V + (4 * oq + ((lane >> 2) & 3)) * V_ROW + 48 * 8));
  const u32x2 b = __builtin_bit_cast(u32x2, tr_read(L + L_V + (64 + 4 * (oq & 7) + ((lane >> 2) & 3)) * V_ROW + 48 * 8));
  f32x2 f[4] = {{bf_lo(a.x), bf_hi(a.x)}, {bf_lo(a.y), bf_hi(a.y)}, {bf_lo(b.x), bf_hi(b.x)}, {bf_lo(b.y), bf_hi(b.y)}};
  const float m = cq < 8 ? 1.f : 0.f;
  const f32x2 s = (f[0] + f[1]) + (f[2] + f[3]) * m;
  const f32x2 ss = __builtin_elementwise_fma(f[0], f[0], f[1] * f[1]) + __builtin_elementwise_fma(f[2], f[2], f[3] * f[3]) * m;
  const float sum = dpp_add<0x124>(dpp_add<0x128>(sum_xor32(sum_xor16(s.x + s.y))));
  const float sq = dpp_add<0x124>(dpp_add<0x128>(sum_xor32(sum_xor16(ss.x + ss.y))));
  const float mu = sum * (1.0f / CH);
  const float var = fmaxf(__builtin_fmaf(-mu, mu, sq * (1.0f / CH)), 0.f);
  const float rs = GA * __builtin_amdgcn_rsqf(var + 1e-5f);
  const float off = -mu * rs;
#pragma unroll
  for (int i = 0; i < 4; i++) f[i] = __builtin_elementwise_fma(f[i], f32x2{rs, rs}, f32x2{off, off});
  LDS_AS char* d = L + L_YR + ((cq >> 1) * 4 + q) * 16 + ((cq ^ (cq >> 1)) & 1) * 8;
  *(LDS_AS u32x2*)d = u32x2{pack_bf16(f[0].x, f[0].y), pack_bf16(f[1].x, f[1].y)};
  if (cq < 8) *(LDS_AS u32x2*)(d + 512) = u32x2{pack_bf16(f[2].x, f[2].y), pack_bf16(f[3].x, f[3].y)};
}

DI float add1(float a, float b) { float r; asm("v_add_f32 %0, %1, %2" : "=v"(r) : "v"(a), "v"(b)); return r; }
DI void bar_only() { asm volatile("s_barrier" ::: "memory"); }

#define STAMP(i)

__global__ void __launch_bounds__(NT, 3)
slak_block_kernel(gcu16* x, gcu16* dw, gcu16* w1, gcu16* w2, gcu16* grn_g, gcu16* grn_b, gu16* out, gu64* ws) {
  extern __shared__ __attribute__((aligned(16))) char smem[];
  LDS_AS char* const L = (LDS_AS char*)smem;
  const int t = threadIdx.x, lane = t & 63, bid = blockIdx.x;
  const int w = __builtin_amdgcn_readfirstlane(t >> 6);
  STAMP(0);
  asm volatile("" : "+s"(x), "+s"(dw), "+s"(w1), "+s"(w2), "+s"(grn_g), "+s"(grn_b), "+s"(out), "+s"(ws));
  const int xcd = bid & 7, idx = bid >> 3;
  const int y0 = 14 * ((xcd >> 1) * 4 + (idx >> 3)), x0 = 14 * ((xcd & 1) * 8 + (idx & 7));
  const rsrc_t xrs = make_rsrc(x, X_BYTES);

  const int cg = w >> 1, xh = w & 1;
  u32x2 dv[2][7];
  const u32 tq = ((u32)t * 2731) >> 16, c4 = t - tq * 24;
  u32 warm = 0;
  if (w < 3) {
    if (FE_CODE && w == 0 && lane < CODE_LINES / 4)
      warm = *(const __attribute__((address_space(1))) u32*)((__builtin_amdgcn_s_getpc() & ~255ull) + (((bid >> 3) & 3) + 4 * lane) * 128);
    const rsrc_t drs = make_rsrc(dw, 49 * CH * 2);
    const u32 r0 = 2 * (tq & 3) - (tq >> 2);
#pragma unroll
    for (int h = 0; h < 2; h++) {
      const u32 off = r0 + h < 7u ? (r0 + h) * (7 * CH * 2) + c4 * 8 : 0x80000000u;
#pragma unroll
      for (int j = 0; j < 7; j++) dv[h][j] = __builtin_amdgcn_raw_buffer_load_b64(drs, off + j * (CH * 2), 0, LD_AUX);
    }
    if (FE_WARM && w == 1 && lane < 36) warm = *(const __attribute__((address_space(1))) u32*)((lane < 18 ? w1 : w2 - 18 * 64) + (idx * 18 + lane) * 64);
  }
  u32x4 xin[2][4];
  u32 xdst[2];
  auto load_item = [&](int it) {
    const u32 item = t + it * NT, P = (item * 5462) >> 16, o = item - P * 12, Y = (P * 6554) >> 16, X = P - Y * 10;
    const u32 gy = y0 - 3 + 2 * Y, gx = (it == 0 || item < 1200) ? x0 - 3 + 2 * X : 1024u;
    u32 base = (u32)(__mul24((int)gy, IMG * CH * 2) + __mul24((int)gx, CH * 2)) + o * 16;
    asm volatile("" : "+v"(base));
    const u32 b0 = gx < (u32)IMG ? base : 0x80000000u, b1 = gx + 1 < (u32)IMG ? base + CH * 2 : 0x80000000u;
    xdst[it] = L_X + ((Y >> 1) * 10 + X) * X_REC + o * 16 + (Y & 1) * 8;
#pragma unroll
    for (int k = 0; k < 4; k++)
      xin[it][k] = ld128(xrs, gy + (k >> 1) < (u32)IMG ? ((k & 1) ? b1 : b0) + (k >> 1) * (IMG * CH * 2) : 0x80000000u);
  };
  auto store_item = [&](int it) {
#pragma unroll
    for (int e = 0; e < 8; e++) {
      const u32x4* v = xin[it];
      *(volatile LDS_AS u32x2*)(L + xdst[it] + e * 192) = (e & 1) ? u32x2{perm_hi(v[0][e >> 1], v[1][e >> 1]), perm_hi(v[2][e >> 1], v[3][e >> 1])}
                                                                  : u32x2{perm_lo(v[0][e >> 1], v[1][e >> 1]), perm_lo(v[2][e >> 1], v[3][e >> 1])};
    }
  };
  load_item(0);
  SB();
  const u32x4 w1rs = {(u32)(u64)w1, (u32)((u64)w1 >> 32) & 0xffffu, CH * HID * 2, 0x00020000};
  const u32 w1d = (w / 6) * 3 * 6144 + (w % 6) * 1024;
  const u32 k1 = 16 * (w % 6) + (lane >> 2);
  u32 w1o = __umul24(pos_channel(k1 ^ (((k1 >> 3) & 1) << 2)), HID * 2) + (w / 6) * 384 + (lane & 3) * 16;
  u32 tp = L_T + (64 * cg + lane) * T_ROW;
  u32 xb = L_X + (4 * xh + (lane & 3)) * X_REC + (16 * cg + (lane >> 2)) * 16, xb2 = xb + 40 * X_REC;
  u32 vb = 4 * xh + (lane & 3) < 7 ? L_V + (16 * cg + (lane >> 2)) * V_ROW + (4 * xh + (lane & 3)) * 8 : L_HREM + lane * 8;
  asm volatile("" : "+v"(w1o), "+v"(tp), "+v"(xb), "+v"(xb2), "+v"(vb));
  SB();
  if (w < 3) {
#pragma unroll
    for (int e = 0; e < 4; e++) {
      u32 q[2][2][4];
#pragma unroll
      for (int h = 0; h < 2; h++) {
        u32 v[7];
#pragma unroll
        for (int j = 0; j < 7; j++) v[j] = dv[h][j][e >> 1];
#pragma unroll
        for (int sx = 0; sx < 3; sx++) {
          q[0][h][sx] = (e & 1) ? perm_hi(v[2 * sx], v[2 * sx + 1]) : perm_lo(v[2 * sx], v[2 * sx + 1]);
          q[1][h][sx + 1] = (e & 1) ? perm_hi(v[2 * sx + 1], v[2 * sx + 2]) : perm_lo(v[2 * sx + 1], v[2 * sx + 2]);
        }
        q[0][h][3] = (e & 1) ? v[6] >> 16 : v[6] & 0xffffu;
        q[1][h][0] = (e & 1) ? v[0] & 0xffff0000u : v[0] << 16;
      }
      LDS_AS char* d = L + L_T + ((12 * (4 * (c4 & 1) + e) + (c4 >> 1)) * 4 + 2 * (tq >> 2)) * T_ROW + (tq & 3) * 32;
#pragma unroll
      for (int ox = 0; ox < 2; ox++) {
        *(LDS_AS u32x4*)(d + ox * T_ROW) = u32x4{q[ox][0][0], q[ox][1][0], q[ox][0][1], q[ox][1][1]};
        *(LDS_AS u32x4*)(d + ox * T_ROW + 16) = u32x4{q[ox][0][2], q[ox][1][2], q[ox][0][3], q[ox][1][3]};
      }
    }
  }
  if (t == NT - 1) *(LDS_AS u32x4*)(L + L_CNT) = u32x4{0, 0, 0, 0};
  STAMP(1);
  asm volatile("" : "+v"(xin[0][0]), "+v"(xin[0][1]), "+v"(xin[0][2]), "+v"(xin[0][3]));
  if (w < 7) load_item(1);
  SB();
  store_item(0);
  STAMP(2);
  bar_lds();
  STAMP(3);

  u64 epoch;
  {
    s16x8 wq[8], d[4][5];
    auto load_x = [&](int sx, int yp) {
      d[sx][yp] = yp < 4 ? *(volatile LDS_AS s16x8*)(L + xb + (yp * 10 + sx) * X_REC) : *(volatile LDS_AS s16x8*)(L + xb2 + sx * X_REC);
    };
    auto load_w = [&](int i) { wq[i] = *(volatile LDS_AS s16x8*)(L + tp + i * 16); };
    auto half = [](s16x8 v, int h) { return h ? __builtin_shufflevector(v, v, 4, 5, 6, 7) : __builtin_shufflevector(v, v, 0, 1, 2, 3); };
    auto conv_mfma = [&](f32x4& acc, int sx, int sy, int Yo) {
      acc = __builtin_amdgcn_mfma_f32_4x4x4bf16_1k(half(wq[2 * sy + (sx >> 1)], sx & 1), half(d[sx][(Yo + sy) >> 1], (Yo + sy) & 1), acc, 0, 0, 0);
    };
    auto put_row = [&](const f32x4& r, int Yo) { *(LDS_AS u32x2*)(L + vb + Yo * 56) = u32x2{pack_bf16(r[0], r[1]), pack_bf16(r[2], r[3])}; };
    load_w(0); load_x(0, 0); load_x(0, 1); load_w(2); load_w(4); load_x(0, 2); load_w(6);
    f32x4 acc[7];
#pragma unroll
    for (int i = 0; i < 7; i++) acc[i] = f32x4{0.f, 0.f, 0.f, 0.f};
    SB();
    static_for<16>([&](auto ic) {
      constexpr int i = ic.value, sx = i >> 2, sy = i & 3;
#pragma unroll
      for (int Yo = 0; Yo + sy < 6; Yo++) conv_mfma(acc[Yo], sx, sy, Yo);
      if constexpr (i < 9) load_x(1 + i / 3, i % 3);
      if constexpr (i < 4) load_w(2 * i + 1);
      SB();
    });
    if (t < 432) store_item(1);
    bar_lds();
    STAMP(4);
#pragma unroll
    for (int i = 0; i < 8; i++) load_x(i & 3, 3 + (i >> 2));
    epoch = ws_load(ws, WS_EPOCH);
#pragma unroll
    for (int Yo = 0; Yo < 3; Yo++) put_row(acc[Yo], Yo);
    SB();
    static_for<16>([&](auto ic) {
      constexpr int i = ic.value, sx = i & 3, Y = 6 + (i >> 2);
#pragma unroll
      for (int Yo = Y - 3; Yo < 7; Yo++) conv_mfma(acc[Yo], sx, Y - Yo, Yo);
      if constexpr (i < 3) dma16(w1rs, w1o, i * 64, L_W1E + w1d + i * 6144);
      if constexpr (i == 5 || i == 10 || i == 14) put_row(acc[Y - 4], Y - 4);
      SB();
    });
    put_row(acc[6], 6);
  }
  bar_lds();
  STAMP(5);

  const int T = w >> 1, Hh = w & 1;
  const bool bias_wave = w == 4 && bid < CH;
  u32 bw[6], bb[6];
  if (bias_wave) {
#pragma unroll
    for (int i = 0; i < 6; i++) { bw[i] = w2[(lane + 64 * i) * CH + bid]; bb[i] = grn_b[lane + 64 * i]; }
  }
  SB();
  layer_norm_unit(L, lane, (4 * w + (lane & 3)) * 8, L_Y + T * 6144 + ((lane >> 4) * 32 + 16 * Hh + (lane & 15)) * 16, 2048, [&]() {
#pragma unroll
    for (int u = 0; u < 3; u++) dma16(w1rs, w1o, 192 + u * 64, L_W1L + w1d + u * 6144);
  });
  if (w == LN_LAST) layer_norm_last(L, lane);
  asm volatile("s_waitcnt vmcnt(0)" :: "v"(warm) : "memory");
  epoch = ((u64)(u32)__builtin_amdgcn_readfirstlane((int)(epoch >> 32)) << 32) | (u32)__builtin_amdgcn_readfirstlane((int)epoch);
  const u64 tag = (epoch + 1) & ((1ull << 40) - 1);
  float bias_val = 0.f;
  if (bias_wave) {
    float a = 0.f;
#pragma unroll
    for (int i = 0; i < 6; i++) { asm volatile("" : "+v"(bw[i]), "+v"(bb[i])); a = __builtin_fmaf(bf_lo(bw[i]), bf_lo(bb[i]), a); }
    bias_val = __int_as_float(__builtin_amdgcn_readfirstlane(__float_as_int(wave_sum(a))));
  }
  bar_lds();
  STAMP(6);

  const int g4 = lane >> 4, q4 = (lane >> 2) & 3, p4 = lane & 3;
  s16x8 hk[6], hs[6];
  const u32 totw = WS_TOT + (bid & 7) * TOT_ROW;
  u32 ooff, remo;
  u32x4 xr[3], xrem;
  u32 gam;
  u64 bgr;
  const f32x16 zero16 = {0.f, 0.f, 0.f, 0.f, 0.f, 0.f, 0.f, 0.f, 0.f, 0.f, 0.f, 0.f, 0.f, 0.f, 0.f, 0.f};
  {
    const u32 wl = (8 * (g4 >> 1) + q4) * 64 + (g4 & 1) * 32 + p4 * 8 + Hh * 3 * 6144;
    LDS_AS char* wE = L + L_W1E + wl;
    LDS_AS char* wL = L + L_W1L + wl;
    LDS_AS char* sw = L + L_SCR + w * SCR_W + (lane & 31) * 72 + (lane >> 5) * 8;
    LDS_AS char* srk = L + L_SCR + w * SCR_W + (8 * g4 + q4) * 72 + (4 * Hh + p4) * 8;
    LDS_AS char* srs = L + L_SCR + w * SCR_W + (8 * g4 + q4) * 72 + (4 * (Hh ^ 1) + p4) * 8;
    LDS_AS char* wr = L + (T < 3 ? L_W1E : L_W1L) + (Hh * 3 + T % 3) * 6144 + (8 * g4 + q4) * 64 + p4 * 8;
    LDS_AS char* yr = L + L_YR + (g4 * 4 + p4) * 16;
    s16x8 yf[6], wf[6], ry, rw[2];
    f32x4 racc[2] = {{0.f, 0.f, 0.f, 0.f}, {0.f, 0.f, 0.f, 0.f}};
    float ssv[6];
    f32x16 acc = zero16;
    auto arrive = [&](int c) { if (lane == 0) asm volatile("ds_add_u32 %0, %1" :: "v"(L_CNT + 4 * c), "v"(1u) : "memory"); };
    auto await = [&](int c) {
#pragma unroll 1
      for (int spin = 0; spin < SPIN_CAP && __builtin_amdgcn_readfirstlane(*(volatile LDS_AS u32*)(L + L_CNT + 4 * c)) < 12; spin++) {}
    };
    __builtin_amdgcn_s_setprio(3);
#pragma unroll
    for (int k = 0; k < 6; k++) { yf[k] = *(LDS_AS s16x8*)(L + L_Y + T * 6144 + k * 1024 + lane * 16); wf[k] = tr_frag(wE + k * 1024, 256); }
    SB();
#pragma unroll
    for (int k = 0; k < 6; k++) {
      acc = mfma32(yf[k], wf[k], acc);
      wf[k] = tr_frag(wE + 6144 + k * 1024, 256);
      SB();
    }
    const u32 w2raw = wave_slot(w);
    auto fetch_w2 = [&](int c) {
      await(c);
      const u32x4 rs = {(u32)(u64)w2, (u32)((u64)w2 >> 32) & 0xffffu, HID * CH * 2, 0x00020000};
      const u32 vo = (32 * w + 4 * ((lane >> 2) & 7) + (lane >> 5)) * (CH * 2) + (lane & 3) * 16;
#pragma unroll
      for (int j = 0; j < 6; j++) dma16(rs, vo, (j & 1) * 384 + (j >> 1) * 64, w2raw + j * 1024);
    };
    arrive(0);
    static_for<6>([&](auto sc_) {
      constexpr int s = sc_.value, NP = s == 3 ? 10 : 8;
      LDS_AS char* wp2 = (s + 2 < 3 ? wE : wL) + ((s + 2) % 3) * 6144;
      __builtin_amdgcn_s_setprio((G1_PRIO >> (2 * s)) & 3);
      if constexpr (s == 5) {
        const u32 pi = 4 * w + q4, py = (pi * 37) >> 8, r24 = lane >= 24;
        ooff = (u32)__mul24((y0 + 2 * py + (p4 >> 1)) * IMG + x0 + 2 * (pi - 7 * py) + (p4 & 1), CH * 2) + g4 * 16;
        remo = lane < 48 ? (u32)(((y0 + 12) * IMG + x0 + 12) * (CH * 2)) + r24 * (IMG * CH * 2 - 384) + lane * 16 : 0x80000000u;
      }
      if constexpr (s == 0) { if (bias_wave && lane < 8) ws_store(ws, WS_TOT + lane * TOT_ROW + TOT_BIAS + bid, granule(tag, bias_val)); }
      if constexpr (s == 3) arrive(1);
      if constexpr (s == 1) { if (w < 6) fetch_w2(0); }
      if constexpr (s == 4) { if (w >= 6) fetch_w2(1); }
      if constexpr (s < 3) { ry = *(LDS_AS s16x8*)(yr + s * 256); rw[0] = tr_frag(wr + s * 2048, 256); rw[1] = tr_frag(wr + s * 2048 + 32, 256); }
      f32x2 xv[NP], z[NP];
#pragma unroll
      for (int i = 0; i < NP; i++) {
        if (i < 8) xv[i] = f32x2{acc[2 * (i & 7)], acc[2 * (i & 7) + 1]};
        else xv[i] = g4 == 0 ? f32x2{racc[0][2 * (i & 1)], racc[0][2 * (i & 1) + 1]} : f32x2{racc[1][2 * (i & 1)], racc[1][2 * (i & 1) + 1]};
        z[i] = __builtin_elementwise_fma(xv[i], xv[i], f32x2{GK, GK});
      }
#pragma unroll
      for (int i = 0; i < NP; i++) PIN(z[i]);
      SB();
#pragma unroll
      for (int i = 0; i < NP; i++) z[i] = __builtin_elementwise_fma(-z[i], xv[i], f32x2{-GS, -GS});
#pragma unroll
      for (int i = 0; i < NP; i++) PIN(z[i]);
      SB();
      f32x16 nxt = zero16;
      float e[2 * NP];
#pragma unroll
      for (int i = 0; i < 2 * NP; i++) {
        if (s < 5 && i % 3 == 0 && i < 15) { nxt = mfma32(yf[i / 3], wf[i / 3], nxt); PIN(nxt); SB(); }
        if (s < 3 && i == 15) { racc[0] = mfma16(ry, rw[0], racc[0]); PIN(racc[0]); SB(); }
        if (s == 5 && i % 3 == 0 && i < 9) { xr[i / 3] = ld128(xrs, ooff + (i / 3) * 64); SB(); }
        if (s == 5 && i == 9) { xrem = ld128(xrs, remo); SB(); }
        if (s == 5 && i == 12) { gam = grn_g[t < HID ? t : 0]; bgr = ws_load(ws, totw + TOT_BIAS + (t & 127)); SB(); }
        e[i] = __builtin_amdgcn_fmed3f(__builtin_amdgcn_exp2f(z[i >> 1][i & 1]), 0.f, 1.f);
        PIN(e[i]);
      }
      SB();
      if constexpr (s < 4) {
#pragma unroll
        for (int k = 0; k < 5; k++) wf[k] = tr_frag(wp2 + k * 1024, 256);
      }
#pragma unroll
      for (int i = 0; i < NP; i++) z[i] = f32x2{e[2 * i], e[2 * i + 1]} + GC;
#pragma unroll
      for (int i = 0; i < NP; i++) PIN(z[i]);
      SB();
      f32x2 pp[NP / 2], rr[2];
#pragma unroll
      for (int i = 0; i < NP / 2; i++) pp[i] = z[2 * i] * z[2 * i + 1];
      xv[0] *= z[1]; xv[1] *= z[0];
#pragma unroll
      for (int i = 0; i < NP / 2; i++) PIN(pp[i]);
      PIN(xv[0]); PIN(xv[1]);
      SB();
      rr[0] = pp[0] * pp[1]; rr[1] = pp[2] * pp[3];
      PIN(rr[0]); PIN(rr[1]);
      SB();
#pragma unroll
      for (int i = 2; i < NP; i++) xv[i] *= z[i ^ 1];
#pragma unroll
      for (int i = 2; i < NP; i++) PIN(xv[i]);
      SB();
      if constexpr (s < 5) { nxt = mfma32(yf[5], wf[5], nxt); PIN(nxt); SB(); }
      float rc[6];
#pragma unroll
      for (int i = 0; i < (NP == 10 ? 6 : 4); i++) {
        if (s < 3 && i == 3) { racc[1] = mfma16(ry, rw[1], racc[1]); PIN(racc[1]); SB(); }
        rc[i] = __builtin_amdgcn_rcpf(i < 4 ? rr[(i >> 1) & 1][i & 1] : pp[NP / 2 - 1][i & 1]);
        PIN(rc[i]);
      }
      SB();
      if constexpr (s < 4) wf[5] = tr_frag(wp2 + 5 * 1024, 256);
      f32x2 ip[NP / 2];
#pragma unroll
      for (int i = 0; i < 4; i++) ip[i] = f32x2{rc[i & 2], rc[(i & 2) + 1]} * pp[i ^ 1];
      if constexpr (NP == 10) ip[4] = f32x2{rc[4], rc[5]};
#pragma unroll
      for (int i = 0; i < 4; i++) PIN(ip[i]);
      SB();
#pragma unroll
      for (int i = 0; i < NP; i++) xv[i] *= ip[i >> 1];
#pragma unroll
      for (int i = 0; i < NP; i++) PIN(xv[i]);
      SB();
      f32x2 ssa = xv[0] * xv[0], ssb = xv[1] * xv[1];
#pragma unroll
      for (int i = 2; i < 8; i += 2) { ssa = __builtin_elementwise_fma(xv[i], xv[i], ssa); ssb = __builtin_elementwise_fma(xv[i + 1], xv[i + 1], ssb); }
      ssa += ssb;
      PIN(ssa);
      SB();
#pragma unroll
      for (int Q = 0; Q < 4; Q++)
        *(volatile LDS_AS u32x2*)(sw + Q * 16) = u32x2{pack_bf16(xv[2 * Q].x, xv[2 * Q].y), pack_bf16(xv[2 * Q + 1].x, xv[2 * Q + 1].y)};
      ssv[s] = ssa.x + ssa.y;
      asm volatile("" : "+v"(ssv[s]));
      hk[s] = tr_frag(srk, 4 * 72);
      hs[s] = tr_frag(srs, 4 * 72);
      if constexpr (s == 3) {
        const f32x2 sq = __builtin_elementwise_fma(xv[8], xv[8], xv[9] * xv[9]);
        if (lane < 32) {
          const int hc = Hh * 192 + T * 32 + lane;
          const u32 p01 = pack_bf16(xv[8].x, xv[8].y), p23 = pack_bf16(xv[9].x, xv[9].y);
          LDS_AS unsigned short* hr = (LDS_AS unsigned short*)(L + L_HREM + (hc >> 3) * 64 + (hc & 7) * 2);
          hr[0] = (unsigned short)p01; hr[8] = (unsigned short)(p01 >> 16); hr[16] = (unsigned short)p23; hr[24] = (unsigned short)(p23 >> 16);
          *(LDS_AS float*)(L + L_SS + 12 * 1536 + ((Hh * 32 + lane) * 6 + T) * 4) = sq.x + sq.y;
        }
      }
      acc = nxt;
      SB();
    });
    LDS_AS char* ssw = L + L_SS + (2 * T + (lane >> 5)) * 1536 + (Hh * 32 + (lane & 31)) * 24;
#pragma unroll
    for (int i = 0; i < 3; i++) *(LDS_AS f32x2*)(ssw + i * 8) = f32x2{ssv[2 * i], ssv[2 * i + 1]};
  }
  asm volatile("" : "+v"(xr[0]), "+v"(xr[1]), "+v"(xr[2]), "+v"(xrem), "+v"(gam), "+v"(bgr));
  bar_all();
  STAMP(7);

  if (t < HID) {
    float v[13];
#pragma unroll
    for (int i = 0; i < 13; i++) v[i] = *(LDS_AS float*)(L + L_SS + i * 1536 + (((w / 3) * 32 + (lane & 31)) * 6 + 2 * (w % 3) + (lane >> 5)) * 4);
    const float s = (((v[0] + v[1]) + (v[2] + v[3])) + ((v[4] + v[5]) + (v[6] + v[7]))) + (((v[8] + v[9]) + (v[10] + v[11])) + v[12]);
    ws_store(ws, WS_PART + (((u32)t >> 4) * 256 + bid) * 16 + (t & 15), granule(tag, s));
  }
  const int o16 = w % 6, kh = w / 6;
  const f32x4 zero4 = {0.f, 0.f, 0.f, 0.f};
  u32 ap[4], rp, hp, rw, w2d0, w2d1;
  f32x2 w2f[24];
  auto prepare = [&]() {
    SB();
    u32x2 w2r[12];
#pragma unroll
    for (int i = 0; i < 12; i++) w2r[i] = *(volatile LDS_AS u32x2*)(L + wave_slot(w) + lane * 8 + i * 512);
#pragma unroll
    for (int i = 0; i < 6; i++) *(volatile LDS_AS s16x8*)(L + wave_slot(w) + lane * 16 + i * 1024) = hs[i];
    if (t == 512) *(LDS_AS u32*)(L + L_FL) = 0;
    SB();
    const u32 w2d = L_W2 + ((t & 1) * 12 + w) * 1024 + (16 * g4 + ((t >> 1) & 3)) * 16 + (t & 8);
    const u32 aslot = L_W2 + (16 * g4 + q4) * 16;
    ap[0] = aslot + p4 * 64 + Hh * 6144; ap[1] = aslot + p4 * 64 + (Hh ^ 1) * 6144;
    ap[2] = aslot + (p4 ^ 1) * 64 + Hh * 6144 + 12288; ap[3] = aslot + (p4 ^ 1) * 64 + (Hh ^ 1) * 6144 + 12288;
    rp = aslot + (p4 ^ (o16 & 1)) * 64 + (o16 * 12 + kh * 6) * 1024;
    hp = L_HREM + (kh * 24 + g4) * 64 + p4 * 16;
    rw = L_RP + (kh * 4 * CH + 32 * (o16 >> 1) + 4 * (o16 & 1)) * 4 + p4 * (CH * 4) + g4 * 32;
    w2d0 = w2d + (t & 1) * 64; w2d1 = w2d + ((t & 1) ^ 1) * 64;
    asm volatile("" : "+v"(ap[0]), "+v"(ap[1]), "+v"(ap[2]), "+v"(ap[3]), "+v"(rp), "+v"(hp), "+v"(rw), "+v"(w2d0), "+v"(w2d1));
#pragma unroll
    for (int i = 0; i < 24; i++) {
      const int ot = i >> 3, r = (i >> 1) & 3, kp = i & 1;
      const u32 a = w2r[4 * ot + 2 * kp][r >> 1], b = w2r[4 * ot + 2 * kp + 1][r >> 1];
      w2f[i] = (r & 1) ? f32x2{bf_hi(a), bf_hi(b)} : f32x2{bf_lo(a), bf_lo(b)};
      asm volatile("" : "+v"(w2f[i]));
    }
    SB();
  };
  {
    const bool mine = (u32)(t - HID) < CH;
    if (!__all(!mine || (bgr >> 24) == tag)) {
#pragma unroll 1
      for (int spin = 0; spin < SPIN_CAP; spin++) {
        bgr = ws_load(ws, totw + TOT_BIAS + (t & 127));
        if (__all(!mine || (bgr >> 24) == tag)) break;
      }
    }
    if (mine) *(LDS_AS float*)(L + L_BP + (t - HID) * 4) = granule_val(bgr);
  }
  if (bid < 24) {
    u64 gr[6];
    const u32 partw = WS_PART + bid * 4096 + t;
#pragma unroll
    for (int i = 0; i < 6; i++) gr[i] = ws_load(ws, partw + (i < 5 || t < 256 ? i * NT : 0));
    prepare();
#pragma unroll 1
    for (int spin = 0; spin < SPIN_CAP; spin++) {
      bool ok = true;
#pragma unroll
      for (int i = 0; i < 6; i++) ok &= (gr[i] >> 24) == tag;
      if (__all(ok)) break;
#pragma unroll
      for (int i = 0; i < 6; i++)
        if ((gr[i] >> 24) != tag) gr[i] = ws_load(ws, partw + (i < 5 || t < 256 ? i * NT : 0));
    }
    float s = ((granule_val(gr[0]) + granule_val(gr[1])) + (granule_val(gr[2]) + granule_val(gr[3]))) + (granule_val(gr[4]) + (t < 256 ? granule_val(gr[5]) : 0.f));
    s = sum_xor32(sum_xor16(s));
    if (lane < 16) *(LDS_AS float*)(L + L_RED + (lane * 12 + w) * 4) = s;
  } else {
    prepare();
  }
  bar_lds();
  if (bid < 24 && w < 2) {
    LDS_AS char* q = L + L_RED + (t & 15) * 48;
    const f32x4 a = *(LDS_AS f32x4*)(q) + *(LDS_AS f32x4*)(q + 16) + *(LDS_AS f32x4*)(q + 32);
    const float gx = __builtin_amdgcn_sqrtf(__builtin_fmaf((a[0] + a[1]) + (a[2] + a[3]), GA_INV2, 1e-6f));
    ws_store(ws, WS_TOT + (t >> 4) * TOT_ROW + bid * 16 + (t & 15), granule(tag, gx));
  }
  SB();
#pragma unroll
  for (int i = 0; i < 6; i++) hs[i] = *(LDS_AS s16x8*)(L + wave_slot(w ^ 1) + lane * 16 + i * 1024);
  f32x4 acc2[3][2];
#pragma unroll
  for (int i = 0; i < 6; i++) acc2[i >> 1][i & 1] = *(LDS_AS f32x4*)(L + L_BP + g4 * 32 + (i >> 1) * 128 + (i & 1) * 16);
  SB();
#pragma unroll
  for (int i = 0; i < 6; i++) {
    const u32x4 xv = xr[i >> 1];
    acc2[i >> 1][i & 1] += f32x4{bf_lo(xv[2 * (i & 1)]), bf_hi(xv[2 * (i & 1)]), bf_lo(xv[2 * (i & 1) + 1]), bf_hi(xv[2 * (i & 1) + 1])};
    asm volatile("" : "+v"(acc2[i >> 1][i & 1]));
  }
#pragma unroll
  for (int i = 0; i < 6; i++) asm volatile("" : "+v"(hs[i]));
  STAMP(8);

  if (t < HID) {
    u64 g;
#pragma unroll 1
    for (int spin = 0; spin < SPIN_CAP; spin++) {
      g = ws_load(ws, totw + t);
      if (__all((g >> 24) == tag)) break;
      if (POLL_SLEEP) __builtin_amdgcn_s_sleep(POLL_SLEEP);
    }
    const float gx = granule_val(g);
    *(LDS_AS float*)(L + L_GG + t * 4) = bf_lo(gam) * gx;
    const float s = wave_sum(gx);
    if (lane == 0) *(LDS_AS float*)(L + L_PS + w * 4) = s;
  }
  bar_lds();
  STAMP(9);
  {
    const f32x4 pa = *(LDS_AS f32x4*)(L + L_PS);
    const f32x2 pb = *(LDS_AS f32x2*)(L + L_PS + 16);
    const f32x4 gg = *(LDS_AS f32x4*)(L + L_GG + (t >> 3) * 16);
    if (bid == 0 && t == NT - 1) ws_store(ws, WS_EPOCH, epoch + 1);
    const float mean = (((pa[0] + pa[1]) + (pa[2] + pa[3])) + (pb[0] + pb[1])) * (1.0f / HID);
    const float inv = GA_INV * __builtin_amdgcn_rcpf(mean + 1e-6f);
    const f32x2 s01 = f32x2{gg[0], gg[1]} * inv + GA_INV, s23 = f32x2{gg[2], gg[3]} * inv + GA_INV;
#pragma unroll
    for (int i = 0; i < 12; i++) {
      const f32x2 a = w2f[2 * i] * s01, b = w2f[2 * i + 1] * s23;
      *(volatile LDS_AS u32x2*)(L + (i & 1 ? w2d1 : w2d0) + ((i >> 1) & 1) * 128 + (i >> 2) * 24576) = u32x2{pack_bf16(a.x, a.y), pack_bf16(b.x, b.y)};
    }
  }
  bar_lds();
  STAMP(10);

  {
    const rsrc_t ors = make_rsrc(out, X_BYTES);
    constexpr int PF = G2_PF;
    s16x8 af[72], ra_a[6], ra_b[6];
    f32x4 ra = zero4;
    u32 rem_cnt = 0;
    auto load_a = [&](int i) { af[i] = *(volatile LDS_AS s16x8*)(L + ap[(i / 6) & 3] + (i / 24) * 24576 + (i % 6) * 1024); };
    auto store_tile = [&](int ot) {
      __builtin_amdgcn_raw_buffer_store_b128(u32x4{pack_bf16(acc2[ot][0][0], acc2[ot][0][1]), pack_bf16(acc2[ot][0][2], acc2[ot][0][3]),
                                                   pack_bf16(acc2[ot][1][0], acc2[ot][1][1]), pack_bf16(acc2[ot][1][2], acc2[ot][1][3])},
                                             ors, ooff + ot * 64, 0, ST_AUX);
    };
    if (G2_PRIO) __builtin_amdgcn_s_setprio(3);
#pragma unroll
    for (int i = 0; i < PF; i++) load_a(i);
    SB();
    static_for<72>([&](auto ic) {
      constexpr int i = ic.value, ot = i / 24, rt = (i / 12) & 1, k = i % 12;
      acc2[ot][rt] = mfma16(af[i], k < 6 ? hk[k % 6] : hs[k % 6], acc2[ot][rt]);
      if constexpr (i + PF < 72) load_a(i + PF);
      if constexpr (G2_PRIO && i % 18 == 17 && i < 60) __builtin_amdgcn_s_setprio(2 - i / 18);
      if constexpr (i >= 2 && i < 38 && i % 6 == 2) { ra_a[i / 6] = *(volatile LDS_AS s16x8*)(L + rp + (i / 6) * 1024); ra_b[i / 6] = *(volatile LDS_AS s16x8*)(L + hp + (i / 6) * 256); }
      if constexpr (i >= 10 && i < 46 && i % 6 == 4) ra = mfma16(ra_a[i / 6 - 1], ra_b[i / 6 - 1], ra);
      if constexpr (i == 43) {
        if (q4 == 0) *(volatile LDS_AS f32x4*)(L + rw) = ra;
        if (lane == 0) asm volatile("ds_add_rtn_u32 %0, %1, %2" : "=v"(rem_cnt) : "v"(L_FL), "v"(1u) : "memory");
      }
      if constexpr (i == 25 || i == 49) store_tile(ot - 1);
      if constexpr (i == 23) { STAMP(11); }
      if constexpr (i == 47) { STAMP(12); }
      if constexpr (i == 58) {
        asm volatile("" : "+v"(rem_cnt));
        if (__builtin_amdgcn_readfirstlane(rem_cnt) == 11 && lane < 48) {
          f32x4 xq[2];
#pragma unroll
          for (int j = 0; j < 2; j++) {
            const f32x4 a = *(volatile LDS_AS f32x4*)(L + L_RP + (lane * 8 + j * 4) * 4), b = *(volatile LDS_AS f32x4*)(L + L_RP + (4 * CH + lane * 8 + j * 4) * 4);
            const f32x4 c = *(volatile LDS_AS f32x4*)(L + L_BP + (lane - 12 * ((lane * 43) >> 9)) * 32 + j * 16);
#pragma unroll
            for (int e = 0; e < 4; e++) xq[j][e] = add1(add1((e & 1) ? bf_hi(xrem[2 * j + (e >> 1)]) : bf_lo(xrem[2 * j + (e >> 1)]), c[e]), add1(a[e], b[e]));
          }
          __builtin_amdgcn_raw_buffer_store_b128(u32x4{pack_bf16(xq[0][0], xq[0][1]), pack_bf16(xq[0][2], xq[0][3]), pack_bf16(xq[1][0], xq[1][1]), pack_bf16(xq[1][2], xq[1][3])}, ors, remo, 0, ST_AUX);
        }
      }
      SB();
    });
    STAMP(13);
    store_tile(2);
  }
  STAMP(14);
}

extern "C" void kernel_launch(void* const* d_in, const int* in_sizes, int n_in,
                              void* d_out, int out_size, void* d_ws, size_t ws_size,
                              hipStream_t stream) {
  (void)in_sizes; (void)n_in; (void)out_size; (void)ws_size;
  (void)hipFuncSetAttribute((const void*)slak_block_kernel, hipFuncAttributeMaxDynamicSharedMemorySize, LDS_BYTES);
  slak_block_kernel<<<256, NT, LDS_BYTES, stream>>>(
      (gcu16*)d_in[0], (gcu16*)d_in[1], (gcu16*)d_in[2], (gcu16*)d_in[3], (gcu16*)d_in[4], (gcu16*)d_in[5], (gu16*)d_out, (gu64*)d_ws);
}
```

```cpp
#include <hip/hip_runtime.h>
#include <stdint.h>

typedef unsigned long long u64;
typedef unsigned u32;
typedef float f32x2 __attribute__((ext_vector_type(2)));
typedef float f32x4 __attribute__((ext_vector_type(4)));
typedef float f32x16 __attribute__((ext_vector_type(16)));
typedef short s16x4 __attribute__((ext_vector_type(4)));
typedef short s16x8 __attribute__((ext_vector_type(8)));
typedef __bf16 bf16x2 __attribute__((ext_vector_type(2)));
typedef u32 u32x2 __attribute__((ext_vector_type(2)));
typedef u32 u32x4 __attribute__((ext_vector_type(4)));
typedef u64 u64x8 __attribute__((ext_vector_type(8)));
#define LDS_AS __attribute__((address_space(3)))
typedef __attribute__((address_space(1))) u64 gu64;
typedef __attribute__((address_space(1))) unsigned short gu16;
typedef __attribute__((address_space(1))) const unsigned short gcu16;
#define RLX_AGENT __ATOMIC_RELAXED, __HIP_MEMORY_SCOPE_AGENT
#define DI __device__ __forceinline__

constexpr int IMG = 224, CH = 96, HID = 384, NT = 768;
constexpr u32 X_BYTES = IMG * IMG * CH * 2;

constexpr float GA = 0.4686686930532063f, GK = 4.91222953926424f;
constexpr float GC = 0x1p-30f, GS = 30.f;
constexpr float GA_INV = 2.1337034344781243f * 0x1p-30f, GA_INV2 = 4.552690346303743f * 0x1p-60f;

constexpr u32 L_X = 0, X_REC = 1600;
constexpr u32 L_Y = 0, L_YR = 36864;
constexpr u32 L_W1L = 37632;
constexpr u32 L_SCR = 74496, SCR_W = 2304;
constexpr u32 L_SS = 102144;
constexpr u32 L_V = 83200, V_ROW = 416;
constexpr u32 L_T = 83200, T_ROW = 144;
constexpr u32 L_W1E = 123136;
constexpr u32 L_HREM = 160000;
constexpr u32 L_CNT = 163072;
constexpr u32 L_W2 = 0;
constexpr u32 L_RP = 74496;
constexpr u32 L_BP = 77568;
constexpr u32 L_GG = 77952, L_PS = 79488, L_RED = 79552;
constexpr u32 L_FL = 80320;
constexpr u32 LDS_BYTES = 163840;

constexpr u32 TOT_ROW = 512, TOT_BIAS = 384;
constexpr u32 WS_EPOCH = 0, WS_TOT = 64, WS_PART = 64 + 8 * TOT_ROW;
#define CODE_LINES 172
constexpr int SPIN_CAP = 1 << 20;

#define SB() __builtin_amdgcn_sched_barrier(0)
#define PIN(v) asm volatile("" : "+v"(v))
template <int V> struct int_c { static constexpr int value = V; };
template <int N, int I = 0, class F> DI void static_for(F&& f) { if constexpr (I < N) { f(int_c<I>{}); static_for<N, I + 1>(f); } }
DI void bar_lds() { asm volatile("s_waitcnt lgkmcnt(0)\n\ts_barrier" ::: "memory"); }
DI void bar_all() { asm volatile("s_waitcnt vmcnt(0) lgkmcnt(0)\n\ts_barrier" ::: "memory"); }
#define LD_AUX 0
#define FE_CODE 1
#define FE_WARM 1
#define LN_LAST 2
#define POLL_SLEEP 8
#define POLL_DEPTH 1
#define G1_PRIO 0x1af
#define G2_PRIO 1
#define W2_EARLY 8
#define G2_PF 3
#define ST_AUX 16
typedef __amdgpu_buffer_rsrc_t rsrc_t;
DI rsrc_t make_rsrc(gcu16* p, u32 bytes) { return __builtin_amdgcn_make_buffer_rsrc((void*)p, 0, bytes, 0x00020000); }
DI u32x4 ld128(rsrc_t r, u32 off) { return __builtin_amdgcn_raw_buffer_load_b128(r, off, 0, LD_AUX); }
DI void dma16(u32x4 rs, u32 voff, u32 soff, u32 lds) {
  u32 keep;
  asm volatile("s_mov_b32 %0, m0\n\ts_mov_b32 m0, %1\n\ts_nop 0\n\tbuffer_load_dwordx4 %2, %3, %4 offen lds\n\ts_mov_b32 m0, %0"
               : "=&s"(keep) : "s"(lds), "v"(voff), "s"(rs), "s"(soff) : "memory");
}
DI u32 pack_bf16(float a, float b) { bf16x2 v = __builtin_convertvector(f32x2{a, b}, bf16x2); return __builtin_bit_cast(u32, v); }
DI float bf_lo(u32 v) { return __uint_as_float(v << 16); }
DI float bf_hi(u32 v) { return __uint_as_float(v & 0xffff0000u); }
DI u32 perm_lo(u32 a, u32 b) { return __builtin_amdgcn_perm(b, a, 0x05040100u); }
DI u32 perm_hi(u32 a, u32 b) { return __builtin_amdgcn_perm(b, a, 0x07060302u); }
DI s16x4 tr_read(LDS_AS char* p) { return __builtin_amdgcn_ds_read_tr16_b64_v4i16((LDS_AS s16x4*)p); }
DI s16x8 tr_frag(LDS_AS char* p, int step) {
  s16x4 a = tr_read(p), b = tr_read(p + step);
  return __builtin_shufflevector(a, b, 0, 1, 2, 3, 4, 5, 6, 7);
}
DI f32x16 mfma32(s16x8 a, s16x8 b, f32x16 c) { return __builtin_amdgcn_mfma_f32_32x32x16_bf16(a, b, c, 0, 0, 0); }
DI f32x4 mfma16(s16x8 a, s16x8 b, f32x4 c) { return __builtin_amdgcn_mfma_f32_16x16x32_bf16(a, b, c, 0, 0, 0); }
DI float sum_xor32(float v) { auto r = __builtin_amdgcn_permlane32_swap(__float_as_uint(v), __float_as_uint(v), false, false); return __uint_as_float(r[0]) + __uint_as_float(r[1]); }
DI float sum_xor16(float v) { auto r = __builtin_amdgcn_permlane16_swap(__float_as_uint(v), __float_as_uint(v), false, false); return __uint_as_float(r[0]) + __uint_as_float(r[1]); }
typedef __attribute__((address_space(1))) char gchar;
DI u64 ws_load(gu64* ws, u32 word) { return __hip_atomic_load((gu64*)((gchar*)ws + word * 8u), RLX_AGENT); }
DI void ws_store(gu64* ws, u32 word, u64 v) { __hip_atomic_store((gu64*)((gchar*)ws + word * 8u), v, RLX_AGENT); }
DI u64 granule(u64 tag, float v) { return (tag << 24) | (u64)((__float_as_uint(v) + 0x80u) >> 8); }
DI float granule_val(u64 g) { return __uint_as_float((u32)g << 8); }
template <int CTRL> DI float dpp_add(float v) { return v + __builtin_bit_cast(float, __builtin_amdgcn_update_dpp(0, __builtin_bit_cast(int, v), CTRL, 0xf, 0xf, false)); }
DI float wave_sum(float v) { return dpp_add<0xb1>(dpp_add<0x4e>(dpp_add<0x124>(dpp_add<0x128>(sum_xor32(sum_xor16(v)))))); }
DI u32 wave_slot(int w) { return w < 6 ? L_Y + w * 6144 : L_W1E + (w - 6) * 6144; }
DI u32 pos_channel(u32 p) { const u32 e = (p * 43) >> 9; return 8 * (p - 12 * e) + e; }

template <class F> DI void layer_norm_unit(LDS_AS char* L, int lane, u32 vcol, u32 dst, u32 dst_step, F&& between) {
  const int g = lane >> 4, q = (lane >> 2) & 3;
  LDS_AS char* p0 = L + L_V + (8 * g + 4 * (g & 1) + q) * V_ROW + vcol;
  LDS_AS char* p1 = L + L_V + (8 * g + 4 * ((g & 1) ^ 1) + q) * V_ROW + vcol;
  s16x8 vf[3];
#pragma unroll
  for (int c = 0; c < 3; c++)
    vf[c] = __builtin_shufflevector(tr_read(p0 + c * 32 * V_ROW), tr_read(p1 + c * 32 * V_ROW), 0, 1, 2, 3, 4, 5, 6, 7);
  between();
  const s16x8 ones = {0x3f80, 0x3f80, 0x3f80, 0x3f80, 0x3f80, 0x3f80, 0x3f80, 0x3f80};
  f32x4 ms = {0.f, 0.f, 0.f, 0.f}, mq = {0.f, 0.f, 0.f, 0.f};
#pragma unroll
  for (int c = 0; c < 3; c++) { mq = mfma16(vf[c], vf[c], mq); ms = mfma16(ones, vf[c], ms); }
  f32x2 f[12];
#pragma unroll
  for (int i = 0; i < 12; i++) { const u32 v = __builtin_bit_cast(u32x4, vf[i >> 2])[i & 3]; f[i] = f32x2{bf_lo(v), bf_hi(v)}; }
  const float dg = (lane & 2) ? ((lane & 1) ? mq[3] : mq[2]) : ((lane & 1) ? mq[1] : mq[0]);
  const float sq = __int_as_float(__builtin_amdgcn_ds_bpermute(((lane & 15) + (lane & 12) * 4) * 4, __float_as_int(dg))), sum = ms[0];
  const float mu = sum * (1.0f / CH);
  const float var = fmaxf(__builtin_fmaf(-mu, mu, sq * (1.0f / CH)), 0.f);
  const float rs = GA * __builtin_amdgcn_rsqf(var + 1e-5f);
  const float off = -mu * rs;
#pragma unroll
  for (int c = 0; c < 3; c++) {
    u32x4 o;
#pragma unroll
    for (int i = 0; i < 4; i++) { f32x2 y = __builtin_elementwise_fma(f[4 * c + i], f32x2{rs, rs}, f32x2{off, off}); o[i] = pack_bf16(y.x, y.y); }
    *(LDS_AS u32x4*)(L + dst + c * dst_step) = o;
  }
}

DI void layer_norm_last(LDS_AS char* L, int lane) {
  const int cq = lane >> 2, q = lane & 3, oq = 4 * (lane >> 4) + (lane & 3);
  const u32x2 a = __builtin_bit_cast(u32x2, tr_read(L + L_V + (4 * oq + ((lane >> 2) & 3)) * V_ROW + 48 * 8));
  const u32x2 b = __builtin_bit_cast(u32x2, tr_read(L + L_V + (64 + 4 * (oq & 7) + ((lane >> 2) & 3)) * V_ROW + 48 * 8));
  f32x2 f[4] = {{bf_lo(a.x), bf_hi(a.x)}, {bf_lo(a.y), bf_hi(a.y)}, {bf_lo(b.x), bf_hi(b.x)}, {bf_lo(b.y), bf_hi(b.y)}};
  const float m = cq < 8 ? 1.f : 0.f;
  const f32x2 s = (f[0] + f[1]) + (f[2] + f[3]) * m;
  const f32x2 ss = __builtin_elementwise_fma(f[0], f[0], f[1] * f[1]) + __builtin_elementwise_fma(f[2], f[2], f[3] * f[3]) * m;
  const float sum = dpp_add<0x124>(dpp_add<0x128>(sum_xor32(sum_xor16(s.x + s.y))));
  const float sq = dpp_add<0x124>(dpp_add<0x128>(sum_xor32(sum_xor16(ss.x + ss.y))));
  const float mu = sum * (1.0f / CH);
  const float var = fmaxf(__builtin_fmaf(-mu, mu, sq * (1.0f / CH)), 0.f);
  const float rs = GA * __builtin_amdgcn_rsqf(var + 1e-5f);
  const float off = -mu * rs;
#pragma unroll
  for (int i = 0; i < 4; i++) f[i] = __builtin_elementwise_fma(f[i], f32x2{rs, rs}, f32x2{off, off});
  LDS_AS char* d = L + L_YR + ((cq >> 1) * 4 + q) * 16 + ((cq ^ (cq >> 1)) & 1) * 8;
  *(LDS_AS u32x2*)d = u32x2{pack_bf16(f[0].x, f[0].y), pack_bf16(f[1].x, f[1].y)};
  if (cq < 8) *(LDS_AS u32x2*)(d + 512) = u32x2{pack_bf16(f[2].x, f[2].y), pack_bf16(f[3].x, f[3].y)};
}

DI float add1(float a, float b) { float r; asm("v_add_f32 %0, %1, %2" : "=v"(r) : "v"(a), "v"(b)); return r; }
DI void bar_only() { asm volatile("s_barrier" ::: "memory"); }

#define STAMP(i)

__global__ void __launch_bounds__(NT, 3)
slak_block_kernel(gcu16*, gcu16*, gcu16*, gcu16*, gcu16*, gcu16*, gu16*, gu64*) {
  u64x8 karg;
  asm volatile("s_load_dwordx16 %0, %1, 0x0" : "=s"(karg) : "s"(__builtin_amdgcn_kernarg_segment_ptr()));
  extern __shared__ __attribute__((aligned(16))) char smem[];
  LDS_AS char* const L = (LDS_AS char*)smem;
  const int t = threadIdx.x, lane = t & 63, bid = blockIdx.x;
  const int w = __builtin_amdgcn_readfirstlane(t >> 6);
  STAMP(0);
  const int xcd = bid & 7, idx = bid >> 3;
  const int y0 = 14 * ((xcd >> 1) * 4 + (idx >> 3)), x0 = 14 * ((xcd & 1) * 8 + (idx & 7));

  const int cg = w >> 1, xh = w & 1;
  const u32 tq = ((u32)t * 2731) >> 16, c4 = t - tq * 24;
  u32 dwo[2], xo[4], xdst[2];
  {
    const u32 r0 = 2 * (tq & 3) - (tq >> 2);
#pragma unroll
    for (int h = 0; h < 2; h++) dwo[h] = r0 + h < 7u ? (r0 + h) * (7 * CH * 2) + c4 * 8 : 0x80000000u;
  }
  auto item_offsets = [&](int it) {
    const u32 item = t + it * NT, P = (item * 5462) >> 16, o = item - P * 12, Y = (P * 6554) >> 16, X = P - Y * 10;
    const u32 gy = y0 - 3 + 2 * Y, gx = (it == 0 || item < 1200) ? x0 - 3 + 2 * X : 1024u;
    u32 base = (u32)(__mul24((int)gy, IMG * CH * 2) + __mul24((int)gx, CH * 2)) + o * 16;
    PIN(base);
    const u32 b0 = gx < (u32)IMG ? base : 0x80000000u, b1 = gx + 1 < (u32)IMG ? base + CH * 2 : 0x80000000u;
    xdst[it] = L_X + ((Y >> 1) * 10 + X) * X_REC + o * 16 + (Y & 1) * 8;
#pragma unroll
    for (int k = 0; k < 4; k++) xo[k] = gy + (k >> 1) < (u32)IMG ? ((k & 1) ? b1 : b0) + (k >> 1) * (IMG * CH * 2) : 0x80000000u;
  };
  item_offsets(0);
  u32 pfo = ((w == 0 ? idx & 3 : idx * 18) + lane * (w == 0 ? 4 : 1)) * 128;
  asm volatile("" : "+v"(dwo[0]), "+v"(dwo[1]), "+v"(xo[0]), "+v"(xo[1]), "+v"(xo[2]), "+v"(xo[3]), "+v"(pfo));
  SB();
  asm volatile("s_waitcnt lgkmcnt(0)" : "+s"(karg));
  gcu16* const x = (gcu16*)karg[0]; gcu16* const dw = (gcu16*)karg[1]; gcu16* const w1 = (gcu16*)karg[2]; gcu16* const w2 = (gcu16*)karg[3];
  gcu16* const grn_g = (gcu16*)karg[4]; gcu16* const grn_b = (gcu16*)karg[5]; gu16* const out = (gu16*)karg[6]; gu64* const ws = (gu64*)karg[7];
  const rsrc_t xrs = make_rsrc(x, X_BYTES);
  u32x2 dv[2][7];
  u32 warm = 0;
  if (w < 3) {
    if (FE_CODE && w == 0 && lane < CODE_LINES / 4)
      warm = *(const __attribute__((address_space(1))) u32*)((__builtin_amdgcn_s_getpc() & ~255ull) + pfo);
    const rsrc_t drs = make_rsrc(dw, 49 * CH * 2);
#pragma unroll
    for (int i = 0; i < 14; i++) dv[i / 7][i % 7] = __builtin_amdgcn_raw_buffer_load_b64(drs, dwo[i / 7] + (i % 7) * (CH * 2), 0, LD_AUX);
  }
  u32x4 xin[2][4];
  auto load_item = [&](int it) {
#pragma unroll
    for (int k = 0; k < 4; k++) xin[it][k] = ld128(xrs, xo[k]);
  };
  auto store_item = [&](int it) {
#pragma unroll
    for (int e = 0; e < 8; e++) {
      const u32x4* v = xin[it];
      *(volatile LDS_AS u32x2*)(L + xdst[it] + e * 192) = (e & 1) ? u32x2{perm_hi(v[0][e >> 1], v[1][e >> 1]), perm_hi(v[2][e >> 1], v[3][e >> 1])}
                                                                  : u32x2{perm_lo(v[0][e >> 1], v[1][e >> 1]), perm_lo(v[2][e >> 1], v[3][e >> 1])};
    }
  };
  load_item(0);
  if (FE_WARM && w == 1 && lane < 36) warm = *(const __attribute__((address_space(1))) u32*)((gchar*)(lane < 18 ? w1 : w2 - 18 * 64) + pfo);
  SB();
  if (w < 7) item_offsets(1);
  const u32x4 w1rs = {(u32)(u64)w1, (u32)((u64)w1 >> 32) & 0xffffu, CH * HID * 2, 0x00020000};
  const u32 w1d = (w / 6) * 3 * 6144 + (w % 6) * 1024;
  const u32 k1 = 16 * (w % 6) + (lane >> 2);
  u32 w1o = __umul24(pos_channel(k1 ^ (((k1 >> 3) & 1) << 2)), HID * 2) + (w / 6) * 384 + (lane & 3) * 16;
  u32 tp = L_T + (16 * cg + (cg >= 3 ? 4 : 0) + (lane >> 2) + 100 * (lane & 3)) * T_ROW;
  u32 xb = L_X + (4 * xh + (lane & 3)) * X_REC + (16 * cg + (lane >> 2)) * 16, xb2 = xb + 40 * X_REC;
  u32 vb = 4 * xh + (lane & 3) < 7 ? L_V + (16 * cg + (lane >> 2)) * V_ROW + (4 * xh + (lane & 3)) * 8 : L_HREM + lane * 8;
  asm volatile("" : "+v"(w1o), "+v"(tp), "+v"(xb), "+v"(xb2), "+v"(vb));
  SB();
  if (w < 3) {
#pragma unroll
    for (int e = 0; e < 4; e++) {
      u32 q[2][2][4];
#pragma unroll
      for (int h = 0; h < 2; h++) {
        u32 v[7];
#pragma unroll
        for (int j = 0; j < 7; j++) v[j] = dv[h][j][e >> 1];
#pragma unroll
        for (int sx = 0; sx < 3; sx++) {
          q[0][h][sx] = (e & 1) ? perm_hi(v[2 * sx], v[2 * sx + 1]) : perm_lo(v[2 * sx], v[2 * sx + 1]);
          q[1][h][sx + 1] = (e & 1) ? perm_hi(v[2 * sx + 1], v[2 * sx + 2]) : perm_lo(v[2 * sx + 1], v[2 * sx + 2]);
        }
        q[0][h][3] = (e & 1) ? v[6] >> 16 : v[6] & 0xffffu;
        q[1][h][0] = (e & 1) ? v[0] & 0xffff0000u : v[0] << 16;
      }
      LDS_AS char* d = L + L_T + (52 * (c4 & 1) + 12 * e + (c4 >> 1) + 200 * (tq >> 2)) * T_ROW + (tq & 3) * 32;
#pragma unroll
      for (int ox = 0; ox < 2; ox++) {
        *(LDS_AS u32x4*)(d + ox * 100 * T_ROW) = u32x4{q[ox][0][0], q[ox][1][0], q[ox][0][1], q[ox][1][1]};
        *(LDS_AS u32x4*)(d + ox * 100 * T_ROW + 16) = u32x4{q[ox][0][2], q[ox][1][2], q[ox][0][3], q[ox][1][3]};
      }
    }
  }
  if (t == NT - 1) *(LDS_AS u32x4*)(L + L_CNT) = u32x4{0, 0, 0, 0};
  STAMP(1);
  asm volatile("" : "+v"(xin[0][0]), "+v"(xin[0][1]), "+v"(xin[0][2]), "+v"(xin[0][3]));
  if (w < 7) load_item(1);
  SB();
  store_item(0);
  STAMP(2);
  bar_lds();
  STAMP(3);

  u64 epoch;
  {
    s16x8 wq[8], d[4][5];
    auto load_x = [&](int sx, int yp) {
      d[sx][yp] = yp < 4 ? *(volatile LDS_AS s16x8*)(L + xb + (yp * 10 + sx) * X_REC) : *(volatile LDS_AS s16x8*)(L + xb2 + sx * X_REC);
    };
    auto load_w = [&](int i) { wq[i] = *(volatile LDS_AS s16x8*)(L + tp + i * 16); };
    auto half = [](s16x8 v, int h) { return h ? __builtin_shufflevector(v, v, 4, 5, 6, 7) : __builtin_shufflevector(v, v, 0, 1, 2, 3); };
    auto conv_mfma = [&](f32x4& acc, int sx, int sy, int Yo) {
      acc = __builtin_amdgcn_mfma_f32_4x4x4bf16_1k(half(wq[2 * sy + (sx >> 1)], sx & 1), half(d[sx][(Yo + sy) >> 1], (Yo + sy) & 1), acc, 0, 0, 0);
    };
    auto put_row = [&](const f32x4& r, int Yo) { *(LDS_AS u32x2*)(L + vb + Yo * 56) = u32x2{pack_bf16(r[0], r[1]), pack_bf16(r[2], r[3])}; };
    load_w(0); load_x(0, 0); load_x(0, 1); load_w(2); load_w(4); load_x(0, 2); load_w(6);
    f32x4 acc[7];
#pragma unroll
    for (int i = 0; i < 7; i++) acc[i] = f32x4{0.f, 0.f, 0.f, 0.f};
    SB();
    static_for<16>([&](auto ic) {
      constexpr int i = ic.value, sx = i >> 2, sy = i & 3;
#pragma unroll
      for (int Yo = 0; Yo + sy < 6; Yo++) conv_mfma(acc[Yo], sx, sy, Yo);
      if constexpr (i < 9) load_x(1 + i / 3, i % 3);
      if constexpr (i < 4) load_w(2 * i + 1);
      SB();
    });
    if (t < 432) store_item(1);
    bar_lds();
    STAMP(4);
#pragma unroll
    for (int i = 0; i < 8; i++) load_x(i & 3, 3 + (i >> 2));
    epoch = ws_load(ws, WS_EPOCH);
#pragma unroll
    for (int Yo = 0; Yo < 3; Yo++) put_row(acc[Yo], Yo);
    SB();
    static_for<16>([&](auto ic) {
      constexpr int i = ic.value, sx = i & 3, Y = 6 + (i >> 2);
#pragma unroll
      for (int Yo = Y - 3; Yo < 7; Yo++) conv_mfma(acc[Yo], sx, Y - Yo, Yo);
      if constexpr (i < 3) dma16(w1rs, w1o, i * 64, L_W1E + w1d + i * 6144);
      if constexpr (i == 5 || i == 10 || i == 14) put_row(acc[Y - 4], Y - 4);
      SB();
    });
    put_row(acc[6], 6);
  }
  bar_lds();
  STAMP(5);

  const int T = w >> 1, Hh = w & 1;
  const bool bias_wave = w == 4 && bid < CH;
  u32 bw[6], bb[6];
  if (bias_wave) {
#pragma unroll
    for (int i = 0; i < 6; i++) { bw[i] = w2[(lane + 64 * i) * CH + bid]; bb[i] = grn_b[lane + 64 * i]; }
  }
  SB();
  layer_norm_unit(L, lane, (4 * w + (lane & 3)) * 8, L_Y + T * 6144 + ((lane >> 4) * 32 + 16 * Hh + (lane & 15)) * 16, 2048, [&]() {
#pragma unroll
    for (int u = 0; u < 3; u++) dma16(w1rs, w1o, 192 + u * 64, L_W1L + w1d + u * 6144);
  });
  if (w == LN_LAST) layer_norm_last(L, lane);
  asm volatile("s_waitcnt vmcnt(0)" :: "v"(warm) : "memory");
  epoch = ((u64)(u32)__builtin_amdgcn_readfirstlane((int)(epoch >> 32)) << 32) | (u32)__builtin_amdgcn_readfirstlane((int)epoch);
  const u64 tag = (epoch + 1) & ((1ull << 40) - 1);
  float bias_val = 0.f;
  if (bias_wave) {
    float a = 0.f;
#pragma unroll
    for (int i = 0; i < 6; i++) { asm volatile("" : "+v"(bw[i]), "+v"(bb[i])); a = __builtin_fmaf(bf_lo(bw[i]), bf_lo(bb[i]), a); }
    bias_val = __int_as_float(__builtin_amdgcn_readfirstlane(__float_as_int(wave_sum(a))));
  }
  bar_lds();
  STAMP(6);

  const int g4 = lane >> 4, q4 = (lane >> 2) & 3, p4 = lane & 3;
  s16x8 hk[6], hs[6];
  const int rq = T / 3, otj[3] = {T % 3, (T + 1) % 3, (T + 2) % 3};
  const u32 totw = WS_TOT + (bid & 7) * TOT_ROW;
  u32 ooff, remo;
  u32x4 xr[3], xrem;
  u32 gam;
  u64 bgr;
  const f32x16 zero16 = {0.f, 0.f, 0.f, 0.f, 0.f, 0.f, 0.f, 0.f, 0.f, 0.f, 0.f, 0.f, 0.f, 0.f, 0.f, 0.f};
  {
    const u32 wl = (8 * (g4 >> 1) + q4) * 64 + (g4 & 1) * 32 + p4 * 8 + Hh * 3 * 6144;
    LDS_AS char* wE = L + L_W1E + wl;
    LDS_AS char* wL = L + L_W1L + wl;
    LDS_AS char* sw = L + L_SCR + w * SCR_W + (lane & 31) * 72 + (lane >> 5) * 8;
    LDS_AS char* srk = L + L_SCR + w * SCR_W + (8 * g4 + q4) * 72 + (4 * Hh + p4) * 8;
    LDS_AS char* srs = L + L_SCR + w * SCR_W + (8 * g4 + q4) * 72 + (4 * (Hh ^ 1) + p4) * 8;
    LDS_AS char* wr = L + (T < 3 ? L_W1E : L_W1L) + (Hh * 3 + T % 3) * 6144 + (8 * g4 + q4) * 64 + p4 * 8;
    LDS_AS char* yr = L + L_YR + (g4 * 4 + p4) * 16;
    s16x8 yf[6], wf[6], ry, rw[2];
    f32x4 racc[2] = {{0.f, 0.f, 0.f, 0.f}, {0.f, 0.f, 0.f, 0.f}};
    float ssv[6];
    f32x16 acc = zero16;
    auto arrive = [&](int c) { if (lane == 0) asm volatile("ds_add_u32 %0, %1" :: "v"(L_CNT + 4 * c), "v"(1u) : "memory"); };
    auto await = [&](int c) {
#pragma unroll 1
      for (int spin = 0; spin < SPIN_CAP && __builtin_amdgcn_readfirstlane(*(volatile LDS_AS u32*)(L + L_CNT + 4 * c)) < 12; spin++) {}
    };
    __builtin_amdgcn_s_setprio(3);
#pragma unroll
    for (int k = 0; k < 6; k++) { yf[k] = *(LDS_AS s16x8*)(L + L_Y + T * 6144 + k * 1024 + lane * 16); wf[k] = tr_frag(wE + k * 1024, 256); }
    SB();
#pragma unroll
    for (int k = 0; k < 6; k++) {
      acc = mfma32(yf[k], wf[k], acc);
      wf[k] = tr_frag(wE + 6144 + k * 1024, 256);
      SB();
    }
    const u32 w2raw = wave_slot(w);
    auto fetch_w2 = [&](int c) {
      await(c);
      const u32x4 rs = {(u32)(u64)w2, (u32)((u64)w2 >> 32) & 0xffffu, HID * CH * 2, 0x00020000};
      const u32 vo = (32 * w + 4 * ((lane >> 2) & 7) + (lane >> 5)) * (CH * 2) + (lane & 3) * 16;
#pragma unroll
      for (int j = 0; j < 6; j++) dma16(rs, vo, (j & 1) * 384 + (j >> 1) * 64, w2raw + j * 1024);
    };
    arrive(0);
    static_for<6>([&](auto sc_) {
      constexpr int s = sc_.value, NP = s == 3 ? 10 : 8;
      LDS_AS char* wp2 = (s + 2 < 3 ? wE : wL) + ((s + 2) % 3) * 6144;
      __builtin_amdgcn_s_setprio((G1_PRIO >> (2 * s)) & 3);
      if constexpr (s == 5) {
        const u32 pi = 4 * w + q4, py = (pi * 37) >> 8, r24 = lane >= 24;
        ooff = (u32)__mul24((y0 + 2 * py + (p4 >> 1)) * IMG + x0 + 2 * (pi - 7 * py) + (p4 & 1), CH * 2) + g4 * 16;
        remo = lane < 48 ? (u32)(((y0 + 12) * IMG + x0 + 12) * (CH * 2)) + r24 * (IMG * CH * 2 - 384) + lane * 16 : 0x80000000u;
      }
      if constexpr (s == 0) { if (bias_wave && lane < 8) ws_store(ws, WS_TOT + lane * TOT_ROW + TOT_BIAS + bid, granule(tag, bias_val)); }
      if constexpr (s == 3) arrive(1);
      if constexpr (s == 1) { if (w < 6) fetch_w2(0); }
      if constexpr (s == 4) { if (w >= 6) fetch_w2(1); }
      if constexpr (s < 3) { ry = *(LDS_AS s16x8*)(yr + s * 256); rw[0] = tr_frag(wr + s * 2048, 256); rw[1] = tr_frag(wr + s * 2048 + 32, 256); }
      f32x2 xv[NP], z[NP];
#pragma unroll
      for (int i = 0; i < NP; i++) {
        if (i < 8) xv[i] = f32x2{acc[2 * (i & 7)], acc[2 * (i & 7) + 1]};
        else xv[i] = g4 == 0 ? f32x2{racc[0][2 * (i & 1)], racc[0][2 * (i & 1) + 1]} : f32x2{racc[1][2 * (i & 1)], racc[1][2 * (i & 1) + 1]};
        z[i] = __builtin_elementwise_fma(xv[i], xv[i], f32x2{GK, GK});
      }
#pragma unroll
      for (int i = 0; i < NP; i++) PIN(z[i]);
      SB();
#pragma unroll
      for (int i = 0; i < NP; i++) z[i] = __builtin_elementwise_fma(-z[i], xv[i], f32x2{-GS, -GS});
#pragma unroll
      for (int i = 0; i < NP; i++) PIN(z[i]);
      SB();
      f32x16 nxt = zero16;
      float e[2 * NP];
#pragma unroll
      for (int i = 0; i < 2 * NP; i++) {
        if (s < 5 && i % 3 == 0 && i < 15) { nxt = mfma32(yf[i / 3], wf[i / 3], nxt); PIN(nxt); SB(); }
        if (s < 3 && i == 15) { racc[0] = mfma16(ry, rw[0], racc[0]); PIN(racc[0]); SB(); }
        if (s == 5 && i % 3 == 0 && i < 9) { xr[i / 3] = __builtin_amdgcn_raw_buffer_load_b128(xrs, ooff, otj[i / 3] * 64, LD_AUX); SB(); }
        if (s == 5 && i == 9) { xrem = ld128(xrs, remo); SB(); }
        if (s == 5 && i == 12) { gam = grn_g[t < HID ? t : 0]; bgr = ws_load(ws, totw + TOT_BIAS + (t & 127)); SB(); }
        e[i] = __builtin_amdgcn_fmed3f(__builtin_amdgcn_exp2f(z[i >> 1][i & 1]), 0.f, 1.f);
        PIN(e[i]);
      }
      SB();
      if constexpr (s < 4) {
#pragma unroll
        for (int k = 0; k < 5; k++) wf[k] = tr_frag(wp2 + k * 1024, 256);
      }
#pragma unroll
      for (int i = 0; i < NP; i++) z[i] = f32x2{e[2 * i], e[2 * i + 1]} + GC;
#pragma unroll
      for (int i = 0; i < NP; i++) PIN(z[i]);
      SB();
      f32x2 pp[NP / 2], rr[2];
#pragma unroll
      for (int i = 0; i < NP / 2; i++) pp[i] = z[2 * i] * z[2 * i + 1];
      xv[0] *= z[1]; xv[1] *= z[0];
#pragma unroll
      for (int i = 0; i < NP / 2; i++) PIN(pp[i]);
      PIN(xv[0]); PIN(xv[1]);
      SB();
      rr[0] = pp[0] * pp[1]; rr[1] = pp[2] * pp[3];
      PIN(rr[0]); PIN(rr[1]);
      SB();
#pragma unroll
      for (int i = 2; i < NP; i++) xv[i] *= z[i ^ 1];
#pragma unroll
      for (int i = 2; i < NP; i++) PIN(xv[i]);
      SB();
      if constexpr (s < 5) { nxt = mfma32(yf[5], wf[5], nxt); PIN(nxt); SB(); }
      float rc[6];
#pragma unroll
      for (int i = 0; i < (NP == 10 ? 6 : 4); i++) {
        if (s < 3 && i == 3) { racc[1] = mfma16(ry, rw[1], racc[1]); PIN(racc[1]); SB(); }
        rc[i] = __builtin_amdgcn_rcpf(i < 4 ? rr[(i >> 1) & 1][i & 1] : pp[NP / 2 - 1][i & 1]);
        PIN(rc[i]);
      }
      SB();
      if constexpr (s < 4) wf[5] = tr_frag(wp2 + 5 * 1024, 256);
      f32x2 ip[NP / 2];
#pragma unroll
      for (int i = 0; i < 4; i++) ip[i] = f32x2{rc[i & 2], rc[(i & 2) + 1]} * pp[i ^ 1];
      if constexpr (NP == 10) ip[4] = f32x2{rc[4], rc[5]};
#pragma unroll
      for (int i = 0; i < 4; i++) PIN(ip[i]);
      SB();
#pragma unroll
      for (int i = 0; i < NP; i++) xv[i] *= ip[i >> 1];
#pragma unroll
      for (int i = 0; i < NP; i++) PIN(xv[i]);
      SB();
      f32x2 ssa = xv[0] * xv[0], ssb = xv[1] * xv[1];
#pragma unroll
      for (int i = 2; i < 8; i += 2) { ssa = __builtin_elementwise_fma(xv[i], xv[i], ssa); ssb = __builtin_elementwise_fma(xv[i + 1], xv[i + 1], ssb); }
      ssa += ssb;
      PIN(ssa);
      SB();
#pragma unroll
      for (int Q = 0; Q < 4; Q++)
        *(volatile LDS_AS u32x2*)(sw + Q * 16) = u32x2{pack_bf16(xv[2 * Q].x, xv[2 * Q].y), pack_bf16(xv[2 * Q + 1].x, xv[2 * Q + 1].y)};
      ssv[s] = ssa.x + ssa.y;
      asm volatile("" : "+v"(ssv[s]));
      hk[s] = tr_frag(srk, 4 * 72);
      hs[s] = tr_frag(srs, 4 * 72);
      if constexpr (s == 3) {
        const f32x2 sq = __builtin_elementwise_fma(xv[8], xv[8], xv[9] * xv[9]);
        if (lane < 32) {
          const int hc = Hh * 192 + T * 32 + lane;
          const u32 p01 = pack_bf16(xv[8].x, xv[8].y), p23 = pack_bf16(xv[9].x, xv[9].y);
          LDS_AS unsigned short* hr = (LDS_AS unsigned short*)(L + L_HREM + (hc >> 3) * 64 + (hc & 7) * 2);
          hr[0] = (unsigned short)p01; hr[8] = (unsigned short)(p01 >> 16); hr[16] = (unsigned short)p23; hr[24] = (unsigned short)(p23 >> 16);
          *(LDS_AS float*)(L + L_SS + 12 * 1536 + ((Hh * 32 + lane) * 6 + T) * 4) = sq.x + sq.y;
        }
      }
      acc = nxt;
      SB();
    });
    LDS_AS char* ssw = L + L_SS + (2 * T + (lane >> 5)) * 1536 + (Hh * 32 + (lane & 31)) * 24;
#pragma unroll
    for (int i = 0; i < 3; i++) *(LDS_AS f32x2*)(ssw + i * 8) = f32x2{ssv[2 * i], ssv[2 * i + 1]};
  }
  asm volatile("" : "+v"(xr[0]), "+v"(xr[1]), "+v"(xr[2]), "+v"(xrem), "+v"(gam), "+v"(bgr));
  bar_all();
  STAMP(7);

  if (t < HID) {
    float v[13];
#pragma unroll
    for (int i = 0; i < 13; i++) v[i] = *(LDS_AS float*)(L + L_SS + i * 1536 + (((w / 3) * 32 + (lane & 31)) * 6 + 2 * (w % 3) + (lane >> 5)) * 4);
    const float s = (((v[0] + v[1]) + (v[2] + v[3])) + ((v[4] + v[5]) + (v[6] + v[7]))) + (((v[8] + v[9]) + (v[10] + v[11])) + v[12]);
    ws_store(ws, WS_PART + (((u32)t >> 4) * 256 + bid) * 16 + (t & 15), granule(tag, s));
  }
  u32x2 w2r[12];
  u32 ap[4], ab[2], hp, rw, w2d0, w2d1;
  f32x2 w2f[24];
  auto unpack_w2 = [&](int i) {
    const int ot = i >> 3, r = (i >> 1) & 3, kp = i & 1;
    const u32 a = w2r[4 * ot + 2 * kp][r >> 1], b = w2r[4 * ot + 2 * kp + 1][r >> 1];
    w2f[i] = (r & 1) ? f32x2{bf_hi(a), bf_hi(b)} : f32x2{bf_lo(a), bf_lo(b)};
    PIN(w2f[i]);
  };
  auto swap_out = [&]() {
    SB();
#pragma unroll
    for (int i = 0; i < 12; i++) w2r[i] = *(volatile LDS_AS u32x2*)(L + wave_slot(w) + lane * 8 + i * 512);
#pragma unroll
    for (int i = 0; i < 6; i++) *(volatile LDS_AS s16x8*)(L + wave_slot(w) + lane * 16 + i * 1024) = hs[i];
    if (t == 512) *(LDS_AS u32*)(L + L_FL) = 0;
    SB();
    const u32 w2d = L_W2 + ((t & 1) * 12 + w) * 1024 + (16 * g4 + ((t >> 1) & 3)) * 16 + (t & 8);
    const u32 aslot = L_W2 + (16 * g4 + q4) * 16;
    ab[0] = aslot + p4 * 64; ab[1] = aslot + (p4 ^ 1) * 64 + 12288;
#pragma unroll
    for (int i = 0; i < 4; i++) ap[i] = (((i >> 1) ^ rq) ? ab[1] : ab[0]) + (otj[0] * 24576 + ((i & 1) ^ Hh) * 6144);
    hp = L_HREM + (Hh * 24 + g4) * 64 + p4 * 16;
    rw = L_RP + (Hh * 4 * CH + 32 * otj[0] + 4 * rq) * 4 + p4 * (CH * 4) + g4 * 32;
    w2d0 = w2d + (t & 1) * 64; w2d1 = w2d + ((t & 1) ^ 1) * 64;
    asm volatile("" : "+v"(ap[0]), "+v"(ap[1]), "+v"(ap[2]), "+v"(ap[3]), "+v"(ab[0]), "+v"(ab[1]), "+v"(hp), "+v"(rw), "+v"(w2d0), "+v"(w2d1));
#pragma unroll
    for (int i = 0; i < W2_EARLY; i++) unpack_w2(i);
    SB();
  };
  {
    const bool mine = (u32)(t - HID) < CH;
    if (!__all(!mine || (bgr >> 24) == tag)) {
#pragma unroll 1
      for (int spin = 0; spin < SPIN_CAP; spin++) {
        bgr = ws_load(ws, totw + TOT_BIAS + (t & 127));
        if (__all(!mine || (bgr >> 24) == tag)) break;
      }
    }
    if (mine) *(LDS_AS float*)(L + L_BP + (t - HID) * 4) = granule_val(bgr);
  }
  if (bid < 24) {
    u64 gr[6];
    const u32 partw = WS_PART + bid * 4096 + t;
#pragma unroll
    for (int i = 0; i < 6; i++) gr[i] = ws_load(ws, partw + (i < 5 || t < 256 ? i * NT : 0));
    swap_out();
#pragma unroll 1
    for (int spin = 0; spin < SPIN_CAP; spin++) {
      bool ok = true;
#pragma unroll
      for (int i = 0; i < 6; i++) ok &= (gr[i] >> 24) == tag;
      if (__all(ok)) break;
#pragma unroll
      for (int i = 0; i < 6; i++)
        if ((gr[i] >> 24) != tag) gr[i] = ws_load(ws, partw + (i < 5 || t < 256 ? i * NT : 0));
    }
    float s = ((granule_val(gr[0]) + granule_val(gr[1])) + (granule_val(gr[2]) + granule_val(gr[3]))) + (granule_val(gr[4]) + (t < 256 ? granule_val(gr[5]) : 0.f));
    s = sum_xor32(sum_xor16(s));
    if (lane < 16) *(LDS_AS float*)(L + L_RED + (lane * 12 + w) * 4) = s;
  } else {
    swap_out();
  }
  bar_lds();
  if (bid < 24 && w < 2) {
    LDS_AS char* q = L + L_RED + (t & 15) * 48;
    const f32x4 a = *(LDS_AS f32x4*)(q) + *(LDS_AS f32x4*)(q + 16) + *(LDS_AS f32x4*)(q + 32);
    const float gx = __builtin_amdgcn_sqrtf(__builtin_fmaf((a[0] + a[1]) + (a[2] + a[3]), GA_INV2, 1e-6f));
    ws_store(ws, WS_TOT + (t >> 4) * TOT_ROW + bid * 16 + (t & 15), granule(tag, gx));
  }
  SB();
#pragma unroll
  for (int i = 0; i < 6; i++) hs[i] = *(LDS_AS s16x8*)(L + wave_slot(w ^ 1) + lane * 16 + i * 1024);
  f32x4 acc2[3][2];
#pragma unroll
  for (int i = 0; i < 6; i++) acc2[i >> 1][i & 1] = *(LDS_AS f32x4*)(L + L_BP + g4 * 32 + otj[i >> 1] * 128 + ((i & 1) ^ (i < 2 ? rq : 0)) * 16);
  SB();
#pragma unroll
  for (int i = 0; i < 6; i++) {
    const u32x4 xv = i < 2 && rq ? u32x4{xr[0][2], xr[0][3], xr[0][0], xr[0][1]} : xr[i >> 1];
    acc2[i >> 1][i & 1] += f32x4{bf_lo(xv[2 * (i & 1)]), bf_hi(xv[2 * (i & 1)]), bf_lo(xv[2 * (i & 1) + 1]), bf_hi(xv[2 * (i & 1) + 1])};
    asm volatile("" : "+v"(acc2[i >> 1][i & 1]));
  }
  SB();
  const f32x4 zero4 = {0.f, 0.f, 0.f, 0.f};
#pragma unroll
  for (int i = W2_EARLY; i < 24; i++) unpack_w2(i);
#pragma unroll
  for (int i = 0; i < 6; i++) asm volatile("" : "+v"(hs[i]));
  STAMP(8);

  if (t < HID) {
    u64 g;
    if (POLL_DEPTH == 1) {
#pragma unroll 1
      for (int spin = 0; spin < SPIN_CAP; spin++) {
        g = ws_load(ws, totw + t);
        if (__all((g >> 24) == tag)) break;
      }
    } else {
      u64 ga = ws_load(ws, totw + t);
      __builtin_amdgcn_s_sleep(POLL_SLEEP);
      u64 gb = ws_load(ws, totw + t);
#pragma unroll 1
      for (int spin = 0; spin < SPIN_CAP; spin++) {
        g = ga;
        if (__all((g >> 24) == tag)) break;
        ga = ws_load(ws, totw + t);
        g = gb;
        if (__all((g >> 24) == tag)) break;
        gb = ws_load(ws, totw + t);
      }
    }
    const float gx = granule_val(g);
    *(LDS_AS float*)(L + L_GG + t * 4) = bf_lo(gam) * gx;
    const float s = wave_sum(gx);
    if (lane == 0) *(LDS_AS float*)(L + L_PS + w * 4) = s;
  }
  bar_lds();
  STAMP(9);
  {
    const f32x4 pa = *(LDS_AS f32x4*)(L + L_PS);
    const f32x2 pb = *(LDS_AS f32x2*)(L + L_PS + 16);
    const f32x4 gg = *(LDS_AS f32x4*)(L + L_GG + (t >> 3) * 16);
    if (bid == 0 && t == NT - 1) ws_store(ws, WS_EPOCH, epoch + 1);
    const float mean = (((pa[0] + pa[1]) + (pa[2] + pa[3])) + (pb[0] + pb[1])) * (1.0f / HID);
    const float inv = GA_INV * __builtin_amdgcn_rcpf(mean + 1e-6f);
    const f32x2 s01 = f32x2{gg[0], gg[1]} * inv + GA_INV, s23 = f32x2{gg[2], gg[3]} * inv + GA_INV;
#pragma unroll
    for (int i = 0; i < 12; i++) {
      const f32x2 a = w2f[2 * i] * s01, b = w2f[2 * i + 1] * s23;
      *(volatile LDS_AS u32x2*)(L + (i & 1 ? w2d1 : w2d0) + ((i >> 1) & 1) * 128 + (i >> 2) * 24576) = u32x2{pack_bf16(a.x, a.y), pack_bf16(b.x, b.y)};
    }
  }

  {
    const rsrc_t ors = make_rsrc(out, X_BYTES);
    constexpr int PF = G2_PF;
    s16x8 af[72], rb[6];
    f32x4 ra = zero4;
    u32 rem_cnt = 0;
    auto load_a = [&](int i) {
      if (i % 24 == 0 && i) {
#pragma unroll
        for (int j = 0; j < 4; j++) ap[j] = ab[j >> 1] + (otj[i / 24] * 24576 + ((j & 1) ^ Hh) * 6144);
      }
      af[i] = *(volatile LDS_AS s16x8*)(L + ap[(i / 6) & 3] + (i % 6) * 1024);
    };
    auto store_tile = [&](int j) {
      const u32x2 a = {pack_bf16(acc2[j][0][0], acc2[j][0][1]), pack_bf16(acc2[j][0][2], acc2[j][0][3])};
      const u32x2 b = {pack_bf16(acc2[j][1][0], acc2[j][1][1]), pack_bf16(acc2[j][1][2], acc2[j][1][3])};
      __builtin_amdgcn_raw_buffer_store_b128(j == 0 && rq ? u32x4{b.x, b.y, a.x, a.y} : u32x4{a.x, a.y, b.x, b.y}, ors, ooff, otj[j] * 64, ST_AUX);
    };
    bar_lds();
    STAMP(10);
    if (G2_PRIO) __builtin_amdgcn_s_setprio(3);
#pragma unroll
    for (int i = 0; i < PF; i++) load_a(i);
    SB();
    static_for<72>([&](auto ic) {
      constexpr int i = ic.value, ot = i / 24, rt = (i / 12) & 1, k = i % 12;
      acc2[ot][rt] = mfma16(af[i], k < 6 ? hk[k % 6] : hs[k % 6], acc2[ot][rt]);
      if constexpr (i + PF < 72) load_a(i + PF);
      if constexpr (G2_PRIO && i % 18 == 17 && i < 60) __builtin_amdgcn_s_setprio(2 - i / 18);
      if constexpr (i < 12 && (i & 1)) rb[i >> 1] = *(volatile LDS_AS s16x8*)(L + hp + (i >> 1) * 256);
      if constexpr (i >= 6 && i < 18 && (i & 1) == 0) ra = mfma16(af[(i - 6) >> 1], rb[(i - 6) >> 1], ra);
      if constexpr (i == 19) {
        if (q4 == 0) *(volatile LDS_AS f32x4*)(L + rw) = ra;
        u32 fl = L_FL;
        PIN(fl);
        if (lane == 0) rem_cnt = __hip_atomic_fetch_add((LDS_AS u32*)(L + fl), 1u, __ATOMIC_RELAXED, __HIP_MEMORY_SCOPE_WORKGROUP);
      }
      if constexpr (i == 25 || i == 49) store_tile(ot - 1);
      if constexpr (i == 23) { STAMP(11); }
      if constexpr (i == 47) { STAMP(12); }
      if constexpr (i == 40) {
        if (__builtin_amdgcn_readfirstlane(rem_cnt) == 11 && lane < 48) {
          f32x4 xq[2];
#pragma unroll
          for (int j = 0; j < 2; j++) {
            const f32x4 a = *(volatile LDS_AS f32x4*)(L + L_RP + (lane * 8 + j * 4) * 4), b = *(volatile LDS_AS f32x4*)(L + L_RP + (4 * CH + lane * 8 + j * 4) * 4);
            const f32x4 c = *(volatile LDS_AS f32x4*)(L + L_BP + (lane - 12 * ((lane * 43) >> 9)) * 32 + j * 16);
#pragma unroll
            for (int e = 0; e < 4; e++) xq[j][e] = add1(add1((e & 1) ? bf_hi(xrem[2 * j + (e >> 1)]) : bf_lo(xrem[2 * j + (e >> 1)]), c[e]), add1(a[e], b[e]));
          }
          __builtin_amdgcn_raw_buffer_store_b128(u32x4{pack_bf16(xq[0][0], xq[0][1]), pack_bf16(xq[0][2], xq[0][3]), pack_bf16(xq[1][0], xq[1][1]), pack_bf16(xq[1][2], xq[1][3])}, ors, remo, 0, ST_AUX);
        }
      }
      SB();
    });
    STAMP(13);
    store_tile(2);
  }
  STAMP(14);
}

extern "C" void kernel_launch(void* const* d_in, const int* in_sizes, int n_in,
                              void* d_out, int out_size, void* d_ws, size_t ws_size,
                              hipStream_t stream) {
  (void)in_sizes; (void)n_in; (void)out_size; (void)ws_size;
  (void)hipFuncSetAttribute((const void*)slak_block_kernel, hipFuncAttributeMaxDynamicSharedMemorySize, LDS_BYTES);
  slak_block_kernel<<<256, NT, LDS_BYTES, stream>>>(
      (gcu16*)d_in[0], (gcu16*)d_in[1], (gcu16*)d_in[2], (gcu16*)d_in[3], (gcu16*)d_in[4], (gcu16*)d_in[5], (gu16*)d_out, (gu64*)d_ws);
}
```

```cpp
#include <hip/hip_runtime.h>
#include <stdint.h>
#define DIAG
#define NSTAMP 16
#define STAMP_MASK 1
#define STAMP_RT 1

typedef unsigned long long u64;
typedef unsigned u32;
typedef float f32x2 __attribute__((ext_vector_type(2)));
typedef float f32x4 __attribute__((ext_vector_type(4)));
typedef float f32x16 __attribute__((ext_vector_type(16)));
typedef short s16x4 __attribute__((ext_vector_type(4)));
typedef short s16x8 __attribute__((ext_vector_type(8)));
typedef __bf16 bf16x2 __attribute__((ext_vector_type(2)));
typedef u32 u32x2 __attribute__((ext_vector_type(2)));
typedef u32 u32x4 __attribute__((ext_vector_type(4)));
typedef u64 u64x8 __attribute__((ext_vector_type(8)));
#define LDS_AS __attribute__((address_space(3)))
typedef __attribute__((address_space(1))) u64 gu64;
typedef __attribute__((address_space(1))) unsigned short gu16;
typedef __attribute__((address_space(1))) const unsigned short gcu16;
#define RLX_AGENT __ATOMIC_RELAXED, __HIP_MEMORY_SCOPE_AGENT
#define DI __device__ __forceinline__

constexpr int IMG = 224, CH = 96, HID = 384, NT = 768;
constexpr u32 X_BYTES = IMG * IMG * CH * 2;

constexpr float GA = 0.4686686930532063f, GK = 4.91222953926424f;
constexpr float GC = 0x1p-30f, GS = 30.f;
constexpr float GA_INV = 2.1337034344781243f * 0x1p-30f, GA_INV2 = 4.552690346303743f * 0x1p-60f;

constexpr u32 L_X = 0, X_REC = 1600;
constexpr u32 L_Y = 0, L_YR = 36864;
constexpr u32 L_W1L = 37632;
constexpr u32 L_SCR = 74496, SCR_W = 2304;
constexpr u32 L_SS = 102144;
constexpr u32 L_V = 83200, V_ROW = 416;
constexpr u32 L_T = 83200, T_ROW = 144;
constexpr u32 L_W1E = 123136;
constexpr u32 L_HREM = 160000;
constexpr u32 L_CNT = 163072;
constexpr u32 L_W2 = 0;
constexpr u32 L_RP = 74496;
constexpr u32 L_BP = 77568;
constexpr u32 L_GG = 77952, L_PS = 79488, L_RED = 79552;
constexpr u32 L_FL = 80320;
constexpr u32 LDS_BYTES = 163840;

constexpr u32 TOT_ROW = 512, TOT_BIAS = 384;
constexpr u32 WS_EPOCH = 0, WS_TOT = 64, WS_PART = 64 + 8 * TOT_ROW;
#define CODE_LINES 172
constexpr int SPIN_CAP = 1 << 20;

#define SB() __builtin_amdgcn_sched_barrier(0)
#define PIN(v) asm volatile("" : "+v"(v))
template <int V> struct int_c { static constexpr int value = V; };
template <int N, int I = 0, class F> DI void static_for(F&& f) { if constexpr (I < N) { f(int_c<I>{}); static_for<N, I + 1>(f); } }
DI void bar_lds() { asm volatile("s_waitcnt lgkmcnt(0)\n\ts_barrier" ::: "memory"); }
DI void bar_all() { asm volatile("s_waitcnt vmcnt(0) lgkmcnt(0)\n\ts_barrier" ::: "memory"); }
#define LD_AUX 0
#define FE_CODE 1
#define FE_WARM 1
#define LN_LAST 2
#define POLL_SLEEP 8
#define POLL_DEPTH 1
#define G1_PRIO 0x1af
#define G2_PRIO 1
#define W2_EARLY 8
#define G2_PF 3
#define ST_AUX 16
typedef __amdgpu_buffer_rsrc_t rsrc_t;
DI rsrc_t make_rsrc(gcu16* p, u32 bytes) { return __builtin_amdgcn_make_buffer_rsrc((void*)p, 0, bytes, 0x00020000); }
DI u32x4 ld128(rsrc_t r, u32 off) { return __builtin_amdgcn_raw_buffer_load_b128(r, off, 0, LD_AUX); }
DI void dma16(u32x4 rs, u32 voff, u32 soff, u32 lds) {
  u32 keep;
  asm volatile("s_mov_b32 %0, m0\n\ts_mov_b32 m0, %1\n\ts_nop 0\n\tbuffer_load_dwordx4 %2, %3, %4 offen lds\n\ts_mov_b32 m0, %0"
               : "=&s"(keep) : "s"(lds), "v"(voff), "s"(rs), "s"(soff) : "memory");
}
DI u32 pack_bf16(float a, float b) { bf16x2 v = __builtin_convertvector(f32x2{a, b}, bf16x2); return __builtin_bit_cast(u32, v); }
DI float bf_lo(u32 v) { return __uint_as_float(v << 16); }
DI float bf_hi(u32 v) { return __uint_as_float(v & 0xffff0000u); }
DI u32 perm_lo(u32 a, u32 b) { return __builtin_amdgcn_perm(b, a, 0x05040100u); }
DI u32 perm_hi(u32 a, u32 b) { return __builtin_amdgcn_perm(b, a, 0x07060302u); }
DI s16x4 tr_read(LDS_AS char* p) { return __builtin_amdgcn_ds_read_tr16_b64_v4i16((LDS_AS s16x4*)p); }
DI s16x8 tr_frag(LDS_AS char* p, int step) {
  s16x4 a = tr_read(p), b = tr_read(p + step);
  return __builtin_shufflevector(a, b, 0, 1, 2, 3, 4, 5, 6, 7);
}
DI f32x16 mfma32(s16x8 a, s16x8 b, f32x16 c) { return __builtin_amdgcn_mfma_f32_32x32x16_bf16(a, b, c, 0, 0, 0); }
DI f32x4 mfma16(s16x8 a, s16x8 b, f32x4 c) { return __builtin_amdgcn_mfma_f32_16x16x32_bf16(a, b, c, 0, 0, 0); }
DI float sum_xor32(float v) { auto r = __builtin_amdgcn_permlane32_swap(__float_as_uint(v), __float_as_uint(v), false, false); return __uint_as_float(r[0]) + __uint_as_float(r[1]); }
DI float sum_xor16(float v) { auto r = __builtin_amdgcn_permlane16_swap(__float_as_uint(v), __float_as_uint(v), false, false); return __uint_as_float(r[0]) + __uint_as_float(r[1]); }
typedef __attribute__((address_space(1))) char gchar;
DI u64 ws_load(gu64* ws, u32 word) { return __hip_atomic_load((gu64*)((gchar*)ws + word * 8u), RLX_AGENT); }
DI void ws_store(gu64* ws, u32 word, u64 v) { __hip_atomic_store((gu64*)((gchar*)ws + word * 8u), v, RLX_AGENT); }
DI u64 granule(u64 tag, float v) { return (tag << 24) | (u64)((__float_as_uint(v) + 0x80u) >> 8); }
DI float granule_val(u64 g) { return __uint_as_float((u32)g << 8); }
template <int CTRL> DI float dpp_add(float v) { return v + __builtin_bit_cast(float, __builtin_amdgcn_update_dpp(0, __builtin_bit_cast(int, v), CTRL, 0xf, 0xf, false)); }
DI float wave_sum(float v) { return dpp_add<0xb1>(dpp_add<0x4e>(dpp_add<0x124>(dpp_add<0x128>(sum_xor32(sum_xor16(v)))))); }
DI u32 wave_slot(int w) { return w < 6 ? L_Y + w * 6144 : L_W1E + (w - 6) * 6144; }
DI u32 pos_channel(u32 p) { const u32 e = (p * 43) >> 9; return 8 * (p - 12 * e) + e; }

template <class F> DI void layer_norm_unit(LDS_AS char* L, int lane, u32 vcol, u32 dst, u32 dst_step, F&& between) {
  const int g = lane >> 4, q = (lane >> 2) & 3;
  LDS_AS char* p0 = L + L_V + (8 * g + 4 * (g & 1) + q) * V_ROW + vcol;
  LDS_AS char* p1 = L + L_V + (8 * g + 4 * ((g & 1) ^ 1) + q) * V_ROW + vcol;
  s16x8 vf[3];
#pragma unroll
  for (int c = 0; c < 3; c++)
    vf[c] = __builtin_shufflevector(tr_read(p0 + c * 32 * V_ROW), tr_read(p1 + c * 32 * V_ROW), 0, 1, 2, 3, 4, 5, 6, 7);
  between();
  const s16x8 ones = {0x3f80, 0x3f80, 0x3f80, 0x3f80, 0x3f80, 0x3f80, 0x3f80, 0x3f80};
  f32x4 ms = {0.f, 0.f, 0.f, 0.f}, mq = {0.f, 0.f, 0.f, 0.f};
#pragma unroll
  for (int c = 0; c < 3; c++) { mq = mfma16(vf[c], vf[c], mq); ms = mfma16(ones, vf[c], ms); }
  f32x2 f[12];
#pragma unroll
  for (int i = 0; i < 12; i++) { const u32 v = __builtin_bit_cast(u32x4, vf[i >> 2])[i & 3]; f[i] = f32x2{bf_lo(v), bf_hi(v)}; }
  const float dg = (lane & 2) ? ((lane & 1) ? mq[3] : mq[2]) : ((lane & 1) ? mq[1] : mq[0]);
  const float sq = __int_as_float(__builtin_amdgcn_ds_bpermute(((lane & 15) + (lane & 12) * 4) * 4, __float_as_int(dg))), sum = ms[0];
  const float mu = sum * (1.0f / CH);
  const float var = fmaxf(__builtin_fmaf(-mu, mu, sq * (1.0f / CH)), 0.f);
  const float rs = GA * __builtin_amdgcn_rsqf(var + 1e-5f);
  const float off = -mu * rs;
#pragma unroll
  for (int c = 0; c < 3; c++) {
    u32x4 o;
#pragma unroll
    for (int i = 0; i < 4; i++) { f32x2 y = __builtin_elementwise_fma(f[4 * c + i], f32x2{rs, rs}, f32x2{off, off}); o[i] = pack_bf16(y.x, y.y); }
    *(LDS_AS u32x4*)(L + dst + c * dst_step) = o;
  }
}

DI void layer_norm_last(LDS_AS char* L, int lane) {
  const int cq = lane >> 2, q = lane & 3, oq = 4 * (lane >> 4) + (lane & 3);
  const u32x2 a = __builtin_bit_cast(u32x2, tr_read(L + L_V + (4 * oq + ((lane >> 2) & 3)) * V_ROW + 48 * 8));
  const u32x2 b = __builtin_bit_cast(u32x2, tr_read(L + L_V + (64 + 4 * (oq & 7) + ((lane >> 2) & 3)) * V_ROW + 48 * 8));
  f32x2 f[4] = {{bf_lo(a.x), bf_hi(a.x)}, {bf_lo(a.y), bf_hi(a.y)}, {bf_lo(b.x), bf_hi(b.x)}, {bf_lo(b.y), bf_hi(b.y)}};
  const float m = cq < 8 ? 1.f : 0.f;
  const f32x2 s = (f[0] + f[1]) + (f[2] + f[3]) * m;
  const f32x2 ss = __builtin_elementwise_fma(f[0], f[0], f[1] * f[1]) + __builtin_elementwise_fma(f[2], f[2], f[3] * f[3]) * m;
  const float sum = dpp_add<0x124>(dpp_add<0x128>(sum_xor32(sum_xor16(s.x + s.y))));
  const float sq = dpp_add<0x124>(dpp_add<0x128>(sum_xor32(sum_xor16(ss.x + ss.y))));
  const float mu = sum * (1.0f / CH);
  const float var = fmaxf(__builtin_fmaf(-mu, mu, sq * (1.0f / CH)), 0.f);
  const float rs = GA * __builtin_amdgcn_rsqf(var + 1e-5f);
  const float off = -mu * rs;
#pragma unroll
  for (int i = 0; i < 4; i++) f[i] = __builtin_elementwise_fma(f[i], f32x2{rs, rs}, f32x2{off, off});
  LDS_AS char* d = L + L_YR + ((cq >> 1) * 4 + q) * 16 + ((cq ^ (cq >> 1)) & 1) * 8;
  *(LDS_AS u32x2*)d = u32x2{pack_bf16(f[0].x, f[0].y), pack_bf16(f[1].x, f[1].y)};
  if (cq < 8) *(LDS_AS u32x2*)(d + 512) = u32x2{pack_bf16(f[2].x, f[2].y), pack_bf16(f[3].x, f[3].y)};
}

DI float add1(float a, float b) { float r; asm("v_add_f32 %0, %1, %2" : "=v"(r) : "v"(a), "v"(b)); return r; }
DI void bar_only() { asm volatile("s_barrier" ::: "memory"); }

#define STAMP(i) do { if ((STAMP_MASK >> (i)) & 1) { SB(); if ((STAMP_RT >> (i)) & 1) st_r[i] = __builtin_amdgcn_s_memrealtime(); else st_r[i] = st_r[(i) - 1]; st_c[i] = __builtin_amdgcn_s_memtime(); SB(); } else { st_r[i] = st_r[(i) - 1]; st_c[i] = st_c[(i) - 1]; } } while (0)

__global__ void __launch_bounds__(NT, 3)
slak_block_kernel(gcu16*, gcu16*, gcu16*, gcu16*, gcu16*, gcu16*, gu16*, gu64*) {
  u64x8 karg;
  asm volatile("s_load_dwordx16 %0, %1, 0x0" : "=s"(karg) : "s"(__builtin_amdgcn_kernarg_segment_ptr()));
  extern __shared__ __attribute__((aligned(16))) char smem[];
  LDS_AS char* const L = (LDS_AS char*)smem;
  const int t = threadIdx.x, lane = t & 63, bid = blockIdx.x;
  const int w = __builtin_amdgcn_readfirstlane(t >> 6);
  u64 st_r[NSTAMP], st_c[NSTAMP];
  STAMP(0);
  const int xcd = bid & 7, idx = bid >> 3;
  const int y0 = 14 * ((xcd >> 1) * 4 + (idx >> 3)), x0 = 14 * ((xcd & 1) * 8 + (idx & 7));

  const int cg = w >> 1, xh = w & 1;
  const u32 tq = ((u32)t * 2731) >> 16, c4 = t - tq * 24;
  u32 dwo[2], xo[4], xdst[2];
  {
    const u32 r0 = 2 * (tq & 3) - (tq >> 2);
#pragma unroll
    for (int h = 0; h < 2; h++) dwo[h] = r0 + h < 7u ? (r0 + h) * (7 * CH * 2) + c4 * 8 : 0x80000000u;
  }
  auto item_offsets = [&](int it) {
    const u32 item = t + it * NT, P = (item * 5462) >> 16, o = item - P * 12, Y = (P * 6554) >> 16, X = P - Y * 10;
    const u32 gy = y0 - 3 + 2 * Y, gx = (it == 0 || item < 1200) ? x0 - 3 + 2 * X : 1024u;
    u32 base = (u32)(__mul24((int)gy, IMG * CH * 2) + __mul24((int)gx, CH * 2)) + o * 16;
    PIN(base);
    const u32 b0 = gx < (u32)IMG ? base : 0x80000000u, b1 = gx + 1 < (u32)IMG ? base + CH * 2 : 0x80000000u;
    xdst[it] = L_X + ((Y >> 1) * 10 + X) * X_REC + o * 16 + (Y & 1) * 8;
#pragma unroll
    for (int k = 0; k < 4; k++) xo[k] = gy + (k >> 1) < (u32)IMG ? ((k & 1) ? b1 : b0) + (k >> 1) * (IMG * CH * 2) : 0x80000000u;
  };
  item_offsets(0);
  u32 pfo = ((w == 0 ? idx & 3 : idx * 18) + lane * (w == 0 ? 4 : 1)) * 128;
  asm volatile("" : "+v"(dwo[0]), "+v"(dwo[1]), "+v"(xo[0]), "+v"(xo[1]), "+v"(xo[2]), "+v"(xo[3]), "+v"(pfo));
  SB();
  asm volatile("s_waitcnt lgkmcnt(0)" : "+s"(karg));
  gcu16* const x = (gcu16*)karg[0]; gcu16* const dw = (gcu16*)karg[1]; gcu16* const w1 = (gcu16*)karg[2]; gcu16* const w2 = (gcu16*)karg[3];
  gcu16* const grn_g = (gcu16*)karg[4]; gcu16* const grn_b = (gcu16*)karg[5]; gu16* const out = (gu16*)karg[6]; gu64* const ws = (gu64*)karg[7];
  gu64* stamps = ws + (1 << 20);
  const rsrc_t xrs = make_rsrc(x, X_BYTES);
  u32x2 dv[2][7];
  u32 warm = 0;
  if (w < 3) {
    if (FE_CODE && w == 0 && lane < CODE_LINES / 4)
      warm = *(const __attribute__((address_space(1))) u32*)((__builtin_amdgcn_s_getpc() & ~255ull) + pfo);
    const rsrc_t drs = make_rsrc(dw, 49 * CH * 2);
#pragma unroll
    for (int i = 0; i < 14; i++) dv[i / 7][i % 7] = __builtin_amdgcn_raw_buffer_load_b64(drs, dwo[i / 7] + (i % 7) * (CH * 2), 0, LD_AUX);
  }
  u32x4 xin[2][4];
  auto load_item = [&](int it) {
#pragma unroll
    for (int k = 0; k < 4; k++) xin[it][k] = ld128(xrs, xo[k]);
  };
  auto store_item = [&](int it) {
#pragma unroll
    for (int e = 0; e < 8; e++) {
      const u32x4* v = xin[it];
      *(volatile LDS_AS u32x2*)(L + xdst[it] + e * 192) = (e & 1) ? u32x2{perm_hi(v[0][e >> 1], v[1][e >> 1]), perm_hi(v[2][e >> 1], v[3][e >> 1])}
                                                                  : u32x2{perm_lo(v[0][e >> 1], v[1][e >> 1]), perm_lo(v[2][e >> 1], v[3][e >> 1])};
    }
  };
  load_item(0);
  if (FE_WARM && w == 1 && lane < 36) warm = *(const __attribute__((address_space(1))) u32*)((gchar*)(lane < 18 ? w1 : w2 - 18 * 64) + pfo);
  SB();
  if (w < 7) item_offsets(1);
  const u32x4 w1rs = {(u32)(u64)w1, (u32)((u64)w1 >> 32) & 0xffffu, CH * HID * 2, 0x00020000};
  const u32 w1d = (w / 6) * 3 * 6144 + (w % 6) * 1024;
  const u32 k1 = 16 * (w % 6) + (lane >> 2);
  u32 w1o = __umul24(pos_channel(k1 ^ (((k1 >> 3) & 1) << 2)), HID * 2) + (w / 6) * 384 + (lane & 3) * 16;
  u32 tp = L_T + (16 * cg + (cg >= 3 ? 4 : 0) + (lane >> 2) + 100 * (lane & 3)) * T_ROW;
  u32 xb = L_X + (4 * xh + (lane & 3)) * X_REC + (16 * cg + (lane >> 2)) * 16, xb2 = xb + 40 * X_REC;
  u32 vb = 4 * xh + (lane & 3) < 7 ? L_V + (16 * cg + (lane >> 2)) * V_ROW + (4 * xh + (lane & 3)) * 8 : L_HREM + lane * 8;
  asm volatile("" : "+v"(w1o), "+v"(tp), "+v"(xb), "+v"(xb2), "+v"(vb));
  SB();
  if (w < 3) {
#pragma unroll
    for (int e = 0; e < 4; e++) {
      u32 q[2][2][4];
#pragma unroll
      for (int h = 0; h < 2; h++) {
        u32 v[7];
#pragma unroll
        for (int j = 0; j < 7; j++) v[j] = dv[h][j][e >> 1];
#pragma unroll
        for (int sx = 0; sx < 3; sx++) {
          q[0][h][sx] = (e & 1) ? perm_hi(v[2 * sx], v[2 * sx + 1]) : perm_lo(v[2 * sx], v[2 * sx + 1]);
          q[1][h][sx + 1] = (e & 1) ? perm_hi(v[2 * sx + 1], v[2 * sx + 2]) : perm_lo(v[2 * sx + 1], v[2 * sx + 2]);
        }
        q[0][h][3] = (e & 1) ? v[6] >> 16 : v[6] & 0xffffu;
        q[1][h][0] = (e & 1) ? v[0] & 0xffff0000u : v[0] << 16;
      }
      LDS_AS char* d = L + L_T + (52 * (c4 & 1) + 12 * e + (c4 >> 1) + 200 * (tq >> 2)) * T_ROW + (tq & 3) * 32;
#pragma unroll
      for (int ox = 0; ox < 2; ox++) {
        *(LDS_AS u32x4*)(d + ox * 100 * T_ROW) = u32x4{q[ox][0][0], q[ox][1][0], q[ox][0][1], q[ox][1][1]};
        *(LDS_AS u32x4*)(d + ox * 100 * T_ROW + 16) = u32x4{q[ox][0][2], q[ox][1][2], q[ox][0][3], q[ox][1][3]};
      }
    }
  }
  if (t == NT - 1) *(LDS_AS u32x4*)(L + L_CNT) = u32x4{0, 0, 0, 0};
  STAMP(1);
  asm volatile("" : "+v"(xin[0][0]), "+v"(xin[0][1]), "+v"(xin[0][2]), "+v"(xin[0][3]));
  if (w < 7) load_item(1);
  SB();
  store_item(0);
  STAMP(2);
  bar_lds();
  STAMP(3);

  u64 epoch;
  {
    s16x8 wq[8], d[4][5];
    auto load_x = [&](int sx, int yp) {
      d[sx][yp] = yp < 4 ? *(volatile LDS_AS s16x8*)(L + xb + (yp * 10 + sx) * X_REC) : *(volatile LDS_AS s16x8*)(L + xb2 + sx * X_REC);
    };
    auto load_w = [&](int i) { wq[i] = *(volatile LDS_AS s16x8*)(L + tp + i * 16); };
    auto half = [](s16x8 v, int h) { return h ? __builtin_shufflevector(v, v, 4, 5, 6, 7) : __builtin_shufflevector(v, v, 0, 1, 2, 3); };
    auto conv_mfma = [&](f32x4& acc, int sx, int sy, int Yo) {
      acc = __builtin_amdgcn_mfma_f32_4x4x4bf16_1k(half(wq[2 * sy + (sx >> 1)], sx & 1), half(d[sx][(Yo + sy) >> 1], (Yo + sy) & 1), acc, 0, 0, 0);
    };
    auto put_row = [&](const f32x4& r, int Yo) { *(LDS_AS u32x2*)(L + vb + Yo * 56) = u32x2{pack_bf16(r[0], r[1]), pack_bf16(r[2], r[3])}; };
    load_w(0); load_x(0, 0); load_x(0, 1); load_w(2); load_w(4); load_x(0, 2); load_w(6);
    f32x4 acc[7];
#pragma unroll
    for (int i = 0; i < 7; i++) acc[i] = f32x4{0.f, 0.f, 0.f, 0.f};
    SB();
    static_for<16>([&](auto ic) {
      constexpr int i = ic.value, sx = i >> 2, sy = i & 3;
#pragma unroll
      for (int Yo = 0; Yo + sy < 6; Yo++) conv_mfma(acc[Yo], sx, sy, Yo);
      if constexpr (i < 9) load_x(1 + i / 3, i % 3);
      if constexpr (i < 4) load_w(2 * i + 1);
      SB();
    });
    if (t < 432) store_item(1);
    bar_lds();
    STAMP(4);
#pragma unroll
    for (int i = 0; i < 8; i++) load_x(i & 3, 3 + (i >> 2));
    epoch = ws_load(ws, WS_EPOCH);
#pragma unroll
    for (int Yo = 0; Yo < 3; Yo++) put_row(acc[Yo], Yo);
    SB();
    static_for<16>([&](auto ic) {
      constexpr int i = ic.value, sx = i & 3, Y = 6 + (i >> 2);
#pragma unroll
      for (int Yo = Y - 3; Yo < 7; Yo++) conv_mfma(acc[Yo], sx, Y - Yo, Yo);
      if constexpr (i < 3) dma16(w1rs, w1o, i * 64, L_W1E + w1d + i * 6144);
      if constexpr (i == 5 || i == 10 || i == 14) put_row(acc[Y - 4], Y - 4);
      SB();
    });
    put_row(acc[6], 6);
  }
  bar_lds();
  STAMP(5);

  const int T = w >> 1, Hh = w & 1;
  const bool bias_wave = w == 4 && bid < CH;
  u32 bw[6], bb[6];
  if (bias_wave) {
#pragma unroll
    for (int i = 0; i < 6; i++) { bw[i] = w2[(lane + 64 * i) * CH + bid]; bb[i] = grn_b[lane + 64 * i]; }
  }
  SB();
  layer_norm_unit(L, lane, (4 * w + (lane & 3)) * 8, L_Y + T * 6144 + ((lane >> 4) * 32 + 16 * Hh + (lane & 15)) * 16, 2048, [&]() {
#pragma unroll
    for (int u = 0; u < 3; u++) dma16(w1rs, w1o, 192 + u * 64, L_W1L + w1d + u * 6144);
  });
  if (w == LN_LAST) layer_norm_last(L, lane);
  asm volatile("s_waitcnt vmcnt(0)" :: "v"(warm) : "memory");
  epoch = ((u64)(u32)__builtin_amdgcn_readfirstlane((int)(epoch >> 32)) << 32) | (u32)__builtin_amdgcn_readfirstlane((int)epoch);
  const u64 tag = (epoch + 1) & ((1ull << 40) - 1);
  float bias_val = 0.f;
  if (bias_wave) {
    float a = 0.f;
#pragma unroll
    for (int i = 0; i < 6; i++) { asm volatile("" : "+v"(bw[i]), "+v"(bb[i])); a = __builtin_fmaf(bf_lo(bw[i]), bf_lo(bb[i]), a); }
    bias_val = __int_as_float(__builtin_amdgcn_readfirstlane(__float_as_int(wave_sum(a))));
  }
  bar_lds();
  STAMP(6);

  const int g4 = lane >> 4, q4 = (lane >> 2) & 3, p4 = lane & 3;
  s16x8 hk[6], hs[6];
  const int rq = T / 3, otj[3] = {T % 3, (T + 1) % 3, (T + 2) % 3};
  const u32 totw = WS_TOT + (bid & 7) * TOT_ROW;
  u32 ooff, remo;
  u32x4 xr[3], xrem;
  u32 gam;
  u64 bgr;
  const f32x16 zero16 = {0.f, 0.f, 0.f, 0.f, 0.f, 0.f, 0.f, 0.f, 0.f, 0.f, 0.f, 0.f, 0.f, 0.f, 0.f, 0.f};
  {
    const u32 wl = (8 * (g4 >> 1) + q4) * 64 + (g4 & 1) * 32 + p4 * 8 + Hh * 3 * 6144;
    LDS_AS char* wE = L + L_W1E + wl;
    LDS_AS char* wL = L + L_W1L + wl;
    LDS_AS char* sw = L + L_SCR + w * SCR_W + (lane & 31) * 72 + (lane >> 5) * 8;
    LDS_AS char* srk = L + L_SCR + w * SCR_W + (8 * g4 + q4) * 72 + (4 * Hh + p4) * 8;
    LDS_AS char* srs = L + L_SCR + w * SCR_W + (8 * g4 + q4) * 72 + (4 * (Hh ^ 1) + p4) * 8;
    LDS_AS char* wr = L + (T < 3 ? L_W1E : L_W1L) + (Hh * 3 + T % 3) * 6144 + (8 * g4 + q4) * 64 + p4 * 8;
    LDS_AS char* yr = L + L_YR + (g4 * 4 + p4) * 16;
    s16x8 yf[6], wf[6], ry, rw[2];
    f32x4 racc[2] = {{0.f, 0.f, 0.f, 0.f}, {0.f, 0.f, 0.f, 0.f}};
    float ssv[6];
    f32x16 acc = zero16;
    auto arrive = [&](int c) { if (lane == 0) asm volatile("ds_add_u32 %0, %1" :: "v"(L_CNT + 4 * c), "v"(1u) : "memory"); };
    auto await = [&](int c) {
#pragma unroll 1
      for (int spin = 0; spin < SPIN_CAP && __builtin_amdgcn_readfirstlane(*(volatile LDS_AS u32*)(L + L_CNT + 4 * c)) < 12; spin++) {}
    };
    __builtin_amdgcn_s_setprio(3);
#pragma unroll
    for (int k = 0; k < 6; k++) { yf[k] = *(LDS_AS s16x8*)(L + L_Y + T * 6144 + k * 1024 + lane * 16); wf[k] = tr_frag(wE + k * 1024, 256); }
    SB();
#pragma unroll
    for (int k = 0; k < 6; k++) {
      acc = mfma32(yf[k], wf[k], acc);
      wf[k] = tr_frag(wE + 6144 + k * 1024, 256);
      SB();
    }
    const u32 w2raw = wave_slot(w);
    auto fetch_w2 = [&](int c) {
      await(c);
      const u32x4 rs = {(u32)(u64)w2, (u32)((u64)w2 >> 32) & 0xffffu, HID * CH * 2, 0x00020000};
      const u32 vo = (32 * w + 4 * ((lane >> 2) & 7) + (lane >> 5)) * (CH * 2) + (lane & 3) * 16;
#pragma unroll
      for (int j = 0; j < 6; j++) dma16(rs, vo, (j & 1) * 384 + (j >> 1) * 64, w2raw + j * 1024);
    };
    arrive(0);
    static_for<6>([&](auto sc_) {
      constexpr int s = sc_.value, NP = s == 3 ? 10 : 8;
      LDS_AS char* wp2 = (s + 2 < 3 ? wE : wL) + ((s + 2) % 3) * 6144;
      __builtin_amdgcn_s_setprio((G1_PRIO >> (2 * s)) & 3);
      if constexpr (s == 5) {
        const u32 pi = 4 * w + q4, py = (pi * 37) >> 8, r24 = lane >= 24;
        ooff = (u32)__mul24((y0 + 2 * py + (p4 >> 1)) * IMG + x0 + 2 * (pi - 7 * py) + (p4 & 1), CH * 2) + g4 * 16;
        remo = lane < 48 ? (u32)(((y0 + 12) * IMG + x0 + 12) * (CH * 2)) + r24 * (IMG * CH * 2 - 384) + lane * 16 : 0x80000000u;
      }
      if constexpr (s == 0) { if (bias_wave && lane < 8) ws_store(ws, WS_TOT + lane * TOT_ROW + TOT_BIAS + bid, granule(tag, bias_val)); }
      if constexpr (s == 3) arrive(1);
      if constexpr (s == 1) { if (w < 6) fetch_w2(0); }
      if constexpr (s == 4) { if (w >= 6) fetch_w2(1); }
      if constexpr (s < 3) { ry = *(LDS_AS s16x8*)(yr + s * 256); rw[0] = tr_frag(wr + s * 2048, 256); rw[1] = tr_frag(wr + s * 2048 + 32, 256); }
      f32x2 xv[NP], z[NP];
#pragma unroll
      for (int i = 0; i < NP; i++) {
        if (i < 8) xv[i] = f32x2{acc[2 * (i & 7)], acc[2 * (i & 7) + 1]};
        else xv[i] = g4 == 0 ? f32x2{racc[0][2 * (i & 1)], racc[0][2 * (i & 1) + 1]} : f32x2{racc[1][2 * (i & 1)], racc[1][2 * (i & 1) + 1]};
        z[i] = __builtin_elementwise_fma(xv[i], xv[i], f32x2{GK, GK});
      }
#pragma unroll
      for (int i = 0; i < NP; i++) PIN(z[i]);
      SB();
#pragma unroll
      for (int i = 0; i < NP; i++) z[i] = __builtin_elementwise_fma(-z[i], xv[i], f32x2{-GS, -GS});
#pragma unroll
      for (int i = 0; i < NP; i++) PIN(z[i]);
      SB();
      f32x16 nxt = zero16;
      float e[2 * NP];
#pragma unroll
      for (int i = 0; i < 2 * NP; i++) {
        if (s < 5 && i % 3 == 0 && i < 15) { nxt = mfma32(yf[i / 3], wf[i / 3], nxt); PIN(nxt); SB(); }
        if (s < 3 && i == 15) { racc[0] = mfma16(ry, rw[0], racc[0]); PIN(racc[0]); SB(); }
        if (s == 5 && i % 3 == 0 && i < 9) { xr[i / 3] = __builtin_amdgcn_raw_buffer_load_b128(xrs, ooff, otj[i / 3] * 64, LD_AUX); SB(); }
        if (s == 5 && i == 9) { xrem = ld128(xrs, remo); SB(); }
        if (s == 5 && i == 12) { gam = grn_g[t < HID ? t : 0]; bgr = ws_load(ws, totw + TOT_BIAS + (t & 127)); SB(); }
        e[i] = __builtin_amdgcn_fmed3f(__builtin_amdgcn_exp2f(z[i >> 1][i & 1]), 0.f, 1.f);
        PIN(e[i]);
      }
      SB();
      if constexpr (s < 4) {
#pragma unroll
        for (int k = 0; k < 5; k++) wf[k] = tr_frag(wp2 + k * 1024, 256);
      }
#pragma unroll
      for (int i = 0; i < NP; i++) z[i] = f32x2{e[2 * i], e[2 * i + 1]} + GC;
#pragma unroll
      for (int i = 0; i < NP; i++) PIN(z[i]);
      SB();
      f32x2 pp[NP / 2], rr[2];
#pragma unroll
      for (int i = 0; i < NP / 2; i++) pp[i] = z[2 * i] * z[2 * i + 1];
      xv[0] *= z[1]; xv[1] *= z[0];
#pragma unroll
      for (int i = 0; i < NP / 2; i++) PIN(pp[i]);
      PIN(xv[0]); PIN(xv[1]);
      SB();
      rr[0] = pp[0] * pp[1]; rr[1] = pp[2] * pp[3];
      PIN(rr[0]); PIN(rr[1]);
      SB();
#pragma unroll
      for (int i = 2; i < NP; i++) xv[i] *= z[i ^ 1];
#pragma unroll
      for (int i = 2; i < NP; i++) PIN(xv[i]);
      SB();
      if constexpr (s < 5) { nxt = mfma32(yf[5], wf[5], nxt); PIN(nxt); SB(); }
      float rc[6];
#pragma unroll
      for (int i = 0; i < (NP == 10 ? 6 : 4); i++) {
        if (s < 3 && i == 3) { racc[1] = mfma16(ry, rw[1], racc[1]); PIN(racc[1]); SB(); }
        rc[i] = __builtin_amdgcn_rcpf(i < 4 ? rr[(i >> 1) & 1][i & 1] : pp[NP / 2 - 1][i & 1]);
        PIN(rc[i]);
      }
      SB();
      if constexpr (s < 4) wf[5] = tr_frag(wp2 + 5 * 1024, 256);
      f32x2 ip[NP / 2];
#pragma unroll
      for (int i = 0; i < 4; i++) ip[i] = f32x2{rc[i & 2], rc[(i & 2) + 1]} * pp[i ^ 1];
      if constexpr (NP == 10) ip[4] = f32x2{rc[4], rc[5]};
#pragma unroll
      for (int i = 0; i < 4; i++) PIN(ip[i]);
      SB();
#pragma unroll
      for (int i = 0; i < NP; i++) xv[i] *= ip[i >> 1];
#pragma unroll
      for (int i = 0; i < NP; i++) PIN(xv[i]);
      SB();
      f32x2 ssa = xv[0] * xv[0], ssb = xv[1] * xv[1];
#pragma unroll
      for (int i = 2; i < 8; i += 2) { ssa = __builtin_elementwise_fma(xv[i], xv[i], ssa); ssb = __builtin_elementwise_fma(xv[i + 1], xv[i + 1], ssb); }
      ssa += ssb;
      PIN(ssa);
      SB();
#pragma unroll
      for (int Q = 0; Q < 4; Q++)
        *(volatile LDS_AS u32x2*)(sw + Q * 16) = u32x2{pack_bf16(xv[2 * Q].x, xv[2 * Q].y), pack_bf16(xv[2 * Q + 1].x, xv[2 * Q + 1].y)};
      ssv[s] = ssa.x + ssa.y;
      asm volatile("" : "+v"(ssv[s]));
      hk[s] = tr_frag(srk, 4 * 72);
      hs[s] = tr_frag(srs, 4 * 72);
      if constexpr (s == 3) {
        const f32x2 sq = __builtin_elementwise_fma(xv[8], xv[8], xv[9] * xv[9]);
        if (lane < 32) {
          const int hc = Hh * 192 + T * 32 + lane;
          const u32 p01 = pack_bf16(xv[8].x, xv[8].y), p23 = pack_bf16(xv[9].x, xv[9].y);
          LDS_AS unsigned short* hr = (LDS_AS unsigned short*)(L + L_HREM + (hc >> 3) * 64 + (hc & 7) * 2);
          hr[0] = (unsigned short)p01; hr[8] = (unsigned short)(p01 >> 16); hr[16] = (unsigned short)p23; hr[24] = (unsigned short)(p23 >> 16);
          *(LDS_AS float*)(L + L_SS + 12 * 1536 + ((Hh * 32 + lane) * 6 + T) * 4) = sq.x + sq.y;
        }
      }
      acc = nxt;
      SB();
    });
    LDS_AS char* ssw = L + L_SS + (2 * T + (lane >> 5)) * 1536 + (Hh * 32 + (lane & 31)) * 24;
#pragma unroll
    for (int i = 0; i < 3; i++) *(LDS_AS f32x2*)(ssw + i * 8) = f32x2{ssv[2 * i], ssv[2 * i + 1]};
  }
  asm volatile("" : "+v"(xr[0]), "+v"(xr[1]), "+v"(xr[2]), "+v"(xrem), "+v"(gam), "+v"(bgr));
  bar_all();
  STAMP(7);

  if (t < HID) {
    float v[13];
#pragma unroll
    for (int i = 0; i < 13; i++) v[i] = *(LDS_AS float*)(L + L_SS + i * 1536 + (((w / 3) * 32 + (lane & 31)) * 6 + 2 * (w % 3) + (lane >> 5)) * 4);
    const float s = (((v[0] + v[1]) + (v[2] + v[3])) + ((v[4] + v[5]) + (v[6] + v[7]))) + (((v[8] + v[9]) + (v[10] + v[11])) + v[12]);
    ws_store(ws, WS_PART + (((u32)t >> 4) * 256 + bid) * 16 + (t & 15), granule(tag, s));
  }
  u32x2 w2r[12];
  u32 ap[4], ab[2], hp, rw, w2d0, w2d1;
  f32x2 w2f[24];
  auto unpack_w2 = [&](int i) {
    const int ot = i >> 3, r = (i >> 1) & 3, kp = i & 1;
    const u32 a = w2r[4 * ot + 2 * kp][r >> 1], b = w2r[4 * ot + 2 * kp + 1][r >> 1];
    w2f[i] = (r & 1) ? f32x2{bf_hi(a), bf_hi(b)} : f32x2{bf_lo(a), bf_lo(b)};
    PIN(w2f[i]);
  };
  auto swap_out = [&]() {
    SB();
#pragma unroll
    for (int i = 0; i < 12; i++) w2r[i] = *(volatile LDS_AS u32x2*)(L + wave_slot(w) + lane * 8 + i * 512);
#pragma unroll
    for (int i = 0; i < 6; i++) *(volatile LDS_AS s16x8*)(L + wave_slot(w) + lane * 16 + i * 1024) = hs[i];
    if (t == 512) *(LDS_AS u32*)(L + L_FL) = 0;
    SB();
    const u32 w2d = L_W2 + ((t & 1) * 12 + w) * 1024 + (16 * g4 + ((t >> 1) & 3)) * 16 + (t & 8);
    const u32 aslot = L_W2 + (16 * g4 + q4) * 16;
    ab[0] = aslot + p4 * 64; ab[1] = aslot + (p4 ^ 1) * 64 + 12288;
#pragma unroll
    for (int i = 0; i < 4; i++) ap[i] = (((i >> 1) ^ rq) ? ab[1] : ab[0]) + (otj[0] * 24576 + ((i & 1) ^ Hh) * 6144);
    hp = L_HREM + (Hh * 24 + g4) * 64 + p4 * 16;
    rw = L_RP + (Hh * 4 * CH + 32 * otj[0] + 4 * rq) * 4 + p4 * (CH * 4) + g4 * 32;
    w2d0 = w2d + (t & 1) * 64; w2d1 = w2d + ((t & 1) ^ 1) * 64;
    asm volatile("" : "+v"(ap[0]), "+v"(ap[1]), "+v"(ap[2]), "+v"(ap[3]), "+v"(ab[0]), "+v"(ab[1]), "+v"(hp), "+v"(rw), "+v"(w2d0), "+v"(w2d1));
#pragma unroll
    for (int i = 0; i < W2_EARLY; i++) unpack_w2(i);
    SB();
  };
  {
    const bool mine = (u32)(t - HID) < CH;
    if (!__all(!mine || (bgr >> 24) == tag)) {
#pragma unroll 1
      for (int spin = 0; spin < SPIN_CAP; spin++) {
        bgr = ws_load(ws, totw + TOT_BIAS + (t & 127));
        if (__all(!mine || (bgr >> 24) == tag)) break;
      }
    }
    if (mine) *(LDS_AS float*)(L + L_BP + (t - HID) * 4) = granule_val(bgr);
  }
  if (bid < 24) {
    u64 gr[6];
    const u32 partw = WS_PART + bid * 4096 + t;
#pragma unroll
    for (int i = 0; i < 6; i++) gr[i] = ws_load(ws, partw + (i < 5 || t < 256 ? i * NT : 0));
    swap_out();
#pragma unroll 1
    for (int spin = 0; spin < SPIN_CAP; spin++) {
      bool ok = true;
#pragma unroll
      for (int i = 0; i < 6; i++) ok &= (gr[i] >> 24) == tag;
      if (__all(ok)) break;
#pragma unroll
      for (int i = 0; i < 6; i++)
        if ((gr[i] >> 24) != tag) gr[i] = ws_load(ws, partw + (i < 5 || t < 256 ? i * NT : 0));
    }
    float s = ((granule_val(gr[0]) + granule_val(gr[1])) + (granule_val(gr[2]) + granule_val(gr[3]))) + (granule_val(gr[4]) + (t < 256 ? granule_val(gr[5]) : 0.f));
    s = sum_xor32(sum_xor16(s));
    if (lane < 16) *(LDS_AS float*)(L + L_RED + (lane * 12 + w) * 4) = s;
  } else {
    swap_out();
  }
  bar_lds();
  if (bid < 24 && w < 2) {
    LDS_AS char* q = L + L_RED + (t & 15) * 48;
    const f32x4 a = *(LDS_AS f32x4*)(q) + *(LDS_AS f32x4*)(q + 16) + *(LDS_AS f32x4*)(q + 32);
    const float gx = __builtin_amdgcn_sqrtf(__builtin_fmaf((a[0] + a[1]) + (a[2] + a[3]), GA_INV2, 1e-6f));
    ws_store(ws, WS_TOT + (t >> 4) * TOT_ROW + bid * 16 + (t & 15), granule(tag, gx));
  }
  SB();
#pragma unroll
  for (int i = 0; i < 6; i++) hs[i] = *(LDS_AS s16x8*)(L + wave_slot(w ^ 1) + lane * 16 + i * 1024);
  f32x4 acc2[3][2];
#pragma unroll
  for (int i = 0; i < 6; i++) acc2[i >> 1][i & 1] = *(LDS_AS f32x4*)(L + L_BP + g4 * 32 + otj[i >> 1] * 128 + ((i & 1) ^ (i < 2 ? rq : 0)) * 16);
  SB();
#pragma unroll
  for (int i = 0; i < 6; i++) {
    const u32x4 xv = i < 2 && rq ? u32x4{xr[0][2], xr[0][3], xr[0][0], xr[0][1]} : xr[i >> 1];
    acc2[i >> 1][i & 1] += f32x4{bf_lo(xv[2 * (i & 1)]), bf_hi(xv[2 * (i & 1)]), bf_lo(xv[2 * (i & 1) + 1]), bf_hi(xv[2 * (i & 1) + 1])};
    asm volatile("" : "+v"(acc2[i >> 1][i & 1]));
  }
  SB();
  const f32x4 zero4 = {0.f, 0.f, 0.f, 0.f};
#pragma unroll
  for (int i = W2_EARLY; i < 24; i++) unpack_w2(i);
#pragma unroll
  for (int i = 0; i < 6; i++) asm volatile("" : "+v"(hs[i]));
  STAMP(8);

  if (t < HID) {
    u64 g;
    if (POLL_DEPTH == 1) {
#pragma unroll 1
      for (int spin = 0; spin < SPIN_CAP; spin++) {
        g = ws_load(ws, totw + t);
        if (__all((g >> 24) == tag)) break;
      }
    } else {
      u64 ga = ws_load(ws, totw + t);
      __builtin_amdgcn_s_sleep(POLL_SLEEP);
      u64 gb = ws_load(ws, totw + t);
#pragma unroll 1
      for (int spin = 0; spin < SPIN_CAP; spin++) {
        g = ga;
        if (__all((g >> 24) == tag)) break;
        ga = ws_load(ws, totw + t);
        g = gb;
        if (__all((g >> 24) == tag)) break;
        gb = ws_load(ws, totw + t);
      }
    }
    const float gx = granule_val(g);
    *(LDS_AS float*)(L + L_GG + t * 4) = bf_lo(gam) * gx;
    const float s = wave_sum(gx);
    if (lane == 0) *(LDS_AS float*)(L + L_PS + w * 4) = s;
  }
  bar_lds();
  STAMP(9);
  {
    const f32x4 pa = *(LDS_AS f32x4*)(L + L_PS);
    const f32x2 pb = *(LDS_AS f32x2*)(L + L_PS + 16);
    const f32x4 gg = *(LDS_AS f32x4*)(L + L_GG + (t >> 3) * 16);
    if (bid == 0 && t == NT - 1) ws_store(ws, WS_EPOCH, epoch + 1);
    const float mean = (((pa[0] + pa[1]) + (pa[2] + pa[3])) + (pb[0] + pb[1])) * (1.0f / HID);
    const float inv = GA_INV * __builtin_amdgcn_rcpf(mean + 1e-6f);
    const f32x2 s01 = f32x2{gg[0], gg[1]} * inv + GA_INV, s23 = f32x2{gg[2], gg[3]} * inv + GA_INV;
#pragma unroll
    for (int i = 0; i < 12; i++) {
      const f32x2 a = w2f[2 * i] * s01, b = w2f[2 * i + 1] * s23;
      *(volatile LDS_AS u32x2*)(L + (i & 1 ? w2d1 : w2d0) + ((i >> 1) & 1) * 128 + (i >> 2) * 24576) = u32x2{pack_bf16(a.x, a.y), pack_bf16(b.x, b.y)};
    }
  }

  {
    const rsrc_t ors = make_rsrc(out, X_BYTES);
    constexpr int PF = G2_PF;
    s16x8 af[72], rb[6];
    f32x4 ra = zero4;
    u32 rem_cnt = 0;
    auto load_a = [&](int i) {
      if (i % 24 == 0 && i) {
#pragma unroll
        for (int j = 0; j < 4; j++) ap[j] = ab[j >> 1] + (otj[i / 24] * 24576 + ((j & 1) ^ Hh) * 6144);
      }
      af[i] = *(volatile LDS_AS s16x8*)(L + ap[(i / 6) & 3] + (i % 6) * 1024);
    };
    auto store_tile = [&](int j) {
      const u32x2 a = {pack_bf16(acc2[j][0][0], acc2[j][0][1]), pack_bf16(acc2[j][0][2], acc2[j][0][3])};
      const u32x2 b = {pack_bf16(acc2[j][1][0], acc2[j][1][1]), pack_bf16(acc2[j][1][2], acc2[j][1][3])};
      __builtin_amdgcn_raw_buffer_store_b128(j == 0 && rq ? u32x4{b.x, b.y, a.x, a.y} : u32x4{a.x, a.y, b.x, b.y}, ors, ooff, otj[j] * 64, ST_AUX);
    };
    bar_lds();
    STAMP(10);
    if (G2_PRIO) __builtin_amdgcn_s_setprio(3);
#pragma unroll
    for (int i = 0; i < PF; i++) load_a(i);
    SB();
    static_for<72>([&](auto ic) {
      constexpr int i = ic.value, ot = i / 24, rt = (i / 12) & 1, k = i % 12;
      acc2[ot][rt] = mfma16(af[i], k < 6 ? hk[k % 6] : hs[k % 6], acc2[ot][rt]);
      if constexpr (i + PF < 72) load_a(i + PF);
      if constexpr (G2_PRIO && i % 18 == 17 && i < 60) __builtin_amdgcn_s_setprio(2 - i / 18);
      if constexpr (i < 12 && (i & 1)) rb[i >> 1] = *(volatile LDS_AS s16x8*)(L + hp + (i >> 1) * 256);
      if constexpr (i >= 6 && i < 18 && (i & 1) == 0) ra = mfma16(af[(i - 6) >> 1], rb[(i - 6) >> 1], ra);
      if constexpr (i == 19) {
        if (q4 == 0) *(volatile LDS_AS f32x4*)(L + rw) = ra;
        u32 fl = L_FL;
        PIN(fl);
        if (lane == 0) rem_cnt = __hip_atomic_fetch_add((LDS_AS u32*)(L + fl), 1u, __ATOMIC_RELAXED, __HIP_MEMORY_SCOPE_WORKGROUP);
      }
      if constexpr (i == 25 || i == 49) store_tile(ot - 1);
      if constexpr (i == 23) { STAMP(11); }
      if constexpr (i == 47) { STAMP(12); }
      if constexpr (i == 40) {
        if (__builtin_amdgcn_readfirstlane(rem_cnt) == 11 && lane < 48) {
          f32x4 xq[2];
#pragma unroll
          for (int j = 0; j < 2; j++) {
            const f32x4 a = *(volatile LDS_AS f32x4*)(L + L_RP + (lane * 8 + j * 4) * 4), b = *(volatile LDS_AS f32x4*)(L + L_RP + (4 * CH + lane * 8 + j * 4) * 4);
            const f32x4 c = *(volatile LDS_AS f32x4*)(L + L_BP + (lane - 12 * ((lane * 43) >> 9)) * 32 + j * 16);
#pragma unroll
            for (int e = 0; e < 4; e++) xq[j][e] = add1(add1((e & 1) ? bf_hi(xrem[2 * j + (e >> 1)]) : bf_lo(xrem[2 * j + (e >> 1)]), c[e]), add1(a[e], b[e]));
          }
          __builtin_amdgcn_raw_buffer_store_b128(u32x4{pack_bf16(xq[0][0], xq[0][1]), pack_bf16(xq[0][2], xq[0][3]), pack_bf16(xq[1][0], xq[1][1]), pack_bf16(xq[1][2], xq[1][3])}, ors, remo, 0, ST_AUX);
        }
      }
      SB();
    });
    STAMP(13);
    store_tile(2);
  }
  STAMP(14);
  { const u64 d = st_r[0] - st_r[0]; const u64 e = __builtin_amdgcn_s_memrealtime() + d;
    if (w == 0) while (__builtin_amdgcn_s_memrealtime() < e) __builtin_amdgcn_s_sleep(1); (void)stamps; (void)st_c; }
}

extern "C" void kernel_launch(void* const* d_in, const int* in_sizes, int n_in,
                              void* d_out, int out_size, void* d_ws, size_t ws_size,
                              hipStream_t stream) {
  (void)in_sizes; (void)n_in; (void)out_size; (void)ws_size;
  (void)hipFuncSetAttribute((const void*)slak_block_kernel, hipFuncAttributeMaxDynamicSharedMemorySize, LDS_BYTES);
  slak_block_kernel<<<256, NT, LDS_BYTES, stream>>>(
      (gcu16*)d_in[0], (gcu16*)d_in[1], (gcu16*)d_in[2], (gcu16*)d_in[3], (gcu16*)d_in[4], (gcu16*)d_in[5], (gu16*)d_out, (gu64*)d_ws);
}
```

```cpp
#include <hip/hip_runtime.h>
#include <stdint.h>

typedef unsigned long long u64;
typedef unsigned u32;
typedef float f32x2 __attribute__((ext_vector_type(2)));
typedef float f32x4 __attribute__((ext_vector_type(4)));
typedef float f32x16 __attribute__((ext_vector_type(16)));
typedef short s16x4 __attribute__((ext_vector_type(4)));
typedef short s16x8 __attribute__((ext_vector_type(8)));
typedef __bf16 bf16x2 __attribute__((ext_vector_type(2)));
typedef u32 u32x2 __attribute__((ext_vector_type(2)));
typedef u32 u32x4 __attribute__((ext_vector_type(4)));
typedef u64 u64x8 __attribute__((ext_vector_type(8)));
#define LDS_AS __attribute__((address_space(3)))
typedef __attribute__((address_space(1))) u64 gu64;
typedef __attribute__((address_space(1))) unsigned short gu16;
typedef __attribute__((address_space(1))) const unsigned short gcu16;
#define RLX_AGENT __ATOMIC_RELAXED, __HIP_MEMORY_SCOPE_AGENT
#define DI __device__ __forceinline__

constexpr int IMG = 224, CH = 96, HID = 384, NT = 768;
constexpr u32 X_BYTES = IMG * IMG * CH * 2;

constexpr float GA = 0.4686686930532063f, GK = 4.91222953926424f;
constexpr float GC = 0x1p-30f, GS = 30.f;
constexpr float GA_INV = 2.1337034344781243f * 0x1p-30f, GA_INV2 = 4.552690346303743f * 0x1p-60f;

constexpr u32 L_X = 0, X_REC = 1600;
constexpr u32 L_Y = 0, L_YR = 36864;
constexpr u32 L_W1L = 37632;
constexpr u32 L_SCR = 74496, SCR_W = 2304;
constexpr u32 L_V = 83200, V_ROW = 416;
constexpr u32 L_T = 83200, T_ROW = 144;
constexpr u32 L_W1E = 123136;
constexpr u32 L_HREM = 160000;
constexpr u32 L_CNT = 163072;
constexpr u32 L_W2 = 0;
constexpr u32 L_RP = 102144;
constexpr u32 L_BP = 105216;
constexpr u32 L_GG = 105600, L_PS = 107136, L_RED = 107200;
constexpr u32 L_FL = 107968;
constexpr u32 LDS_BYTES = 163840;

constexpr u32 TOT_ROW = 512, TOT_BIAS = 384;
constexpr u32 WS_EPOCH = 0, WS_TOT = 64, WS_PART = 64 + 8 * TOT_ROW;
#define CODE_LINES 172
constexpr int SPIN_CAP = 1 << 20;

#define SB() __builtin_amdgcn_sched_barrier(0)
#define PIN(v) asm volatile("" : "+v"(v))
template <int V> struct int_c { static constexpr int value = V; };
template <int N, int I = 0, class F> DI void static_for(F&& f) { if constexpr (I < N) { f(int_c<I>{}); static_for<N, I + 1>(f); } }
DI void bar_lds() { asm volatile("s_waitcnt lgkmcnt(0)\n\ts_barrier" ::: "memory"); }
DI void bar_all() { asm volatile("s_waitcnt vmcnt(0) lgkmcnt(0)\n\ts_barrier" ::: "memory"); }
#define LD_AUX 0
#define FE_CODE 1
#define FE_WARM 1
#define LN_LAST 2
#define POLL_SLEEP 8
#define POLL_DEPTH 1
#define G1_PRIO 0x1af
#define G2_PRIO 1
#define W2_EARLY 8
#define G2_PF 3
#define ST_AUX 16
typedef __amdgpu_buffer_rsrc_t rsrc_t;
DI rsrc_t make_rsrc(gcu16* p, u32 bytes) { return __builtin_amdgcn_make_buffer_rsrc((void*)p, 0, bytes, 0x00020000); }
DI u32x4 ld128(rsrc_t r, u32 off) { return __builtin_amdgcn_raw_buffer_load_b128(r, off, 0, LD_AUX); }
DI void dma16(u32x4 rs, u32 voff, u32 soff, u32 lds) {
  u32 keep;
  asm volatile("s_mov_b32 %0, m0\n\ts_mov_b32 m0, %1\n\ts_nop 0\n\tbuffer_load_dwordx4 %2, %3, %4 offen lds\n\ts_mov_b32 m0, %0"
               : "=&s"(keep) : "s"(lds), "v"(voff), "s"(rs), "s"(soff) : "memory");
}
DI u32 pack_bf16(float a, float b) { bf16x2 v = __builtin_convertvector(f32x2{a, b}, bf16x2); return __builtin_bit_cast(u32, v); }
DI float bf_lo(u32 v) { return __uint_as_float(v << 16); }
DI float bf_hi(u32 v) { return __uint_as_float(v & 0xffff0000u); }
DI u32 perm_lo(u32 a, u32 b) { return __builtin_amdgcn_perm(b, a, 0x05040100u); }
DI u32 perm_hi(u32 a, u32 b) { return __builtin_amdgcn_perm(b, a, 0x07060302u); }
DI s16x4 tr_read(LDS_AS char* p) { return __builtin_amdgcn_ds_read_tr16_b64_v4i16((LDS_AS s16x4*)p); }
DI s16x8 tr_frag(LDS_AS char* p, int step) {
  s16x4 a = tr_read(p), b = tr_read(p + step);
  return __builtin_shufflevector(a, b, 0, 1, 2, 3, 4, 5, 6, 7);
}
DI f32x16 mfma32(s16x8 a, s16x8 b, f32x16 c) { return __builtin_amdgcn_mfma_f32_32x32x16_bf16(a, b, c, 0, 0, 0); }
DI f32x4 mfma16(s16x8 a, s16x8 b, f32x4 c) { return __builtin_amdgcn_mfma_f32_16x16x32_bf16(a, b, c, 0, 0, 0); }
DI float sum_xor32(float v) { auto r = __builtin_amdgcn_permlane32_swap(__float_as_uint(v), __float_as_uint(v), false, false); return __uint_as_float(r[0]) + __uint_as_float(r[1]); }
DI float sum_xor16(float v) { auto r = __builtin_amdgcn_permlane16_swap(__float_as_uint(v), __float_as_uint(v), false, false); return __uint_as_float(r[0]) + __uint_as_float(r[1]); }
typedef __attribute__((address_space(1))) char gchar;
DI u64 ws_load(gu64* ws, u32 word) { return __hip_atomic_load((gu64*)((gchar*)ws + word * 8u), RLX_AGENT); }
DI void ws_store(gu64* ws, u32 word, u64 v) { __hip_atomic_store((gu64*)((gchar*)ws + word * 8u), v, RLX_AGENT); }
DI u64 granule(u64 tag, float v) { return (tag << 24) | (u64)((__float_as_uint(v) + 0x80u) >> 8); }
DI float granule_val(u64 g) { return __uint_as_float((u32)g << 8); }
template <int CTRL> DI float dpp_add(float v) { return v + __builtin_bit_cast(float, __builtin_amdgcn_update_dpp(0, __builtin_bit_cast(int, v), CTRL, 0xf, 0xf, false)); }
DI float wave_sum(float v) { return dpp_add<0xb1>(dpp_add<0x4e>(dpp_add<0x124>(dpp_add<0x128>(sum_xor32(sum_xor16(v)))))); }
DI void arrive(int c) {
  if (__builtin_amdgcn_inverse_ballot_w64(1ull)) asm volatile("ds_add_u32 %0, %1" :: "v"(L_CNT + 4 * c), "v"(1u) : "memory");
}
DI void await(int c) {
#pragma unroll 1
  for (int spin = 0; spin < SPIN_CAP; spin++) {
    if (__builtin_amdgcn_readfirstlane(*(volatile LDS_AS u32*)(L_CNT + 4 * c)) >= 12) break;
    __builtin_amdgcn_s_sleep(1);
  }
  asm volatile("" ::: "memory");
}
DI u32 wave_slot(int w) { return w < 6 ? L_Y + w * 6144 : L_W1E + (w - 6) * 6144; }
DI u32 pos_channel(u32 p) { const u32 e = (p * 43) >> 9; return 8 * (p - 12 * e) + e; }

template <class F> DI void layer_norm_unit(LDS_AS char* L, int lane, u32 src0, u32 src1, u32 dst, u32 diag, F&& between) {
  LDS_AS char* p0 = L + src0;
  LDS_AS char* p1 = L + src1;
  constexpr u32 dst_step = 2048;
  s16x8 vf[3];
#pragma unroll
  for (int c = 0; c < 3; c++)
    vf[c] = __builtin_shufflevector(tr_read(p0 + c * 32 * V_ROW), tr_read(p1 + c * 32 * V_ROW), 0, 1, 2, 3, 4, 5, 6, 7);
  between();
  const s16x8 ones = {0x3f80, 0x3f80, 0x3f80, 0x3f80, 0x3f80, 0x3f80, 0x3f80, 0x3f80};
  f32x4 ms = {0.f, 0.f, 0.f, 0.f}, mq = {0.f, 0.f, 0.f, 0.f};
#pragma unroll
  for (int c = 0; c < 3; c++) { mq = mfma16(vf[c], vf[c], mq); ms = mfma16(ones, vf[c], ms); }
  f32x2 f[12];
#pragma unroll
  for (int i = 0; i < 12; i++) { const u32 v = __builtin_bit_cast(u32x4, vf[i >> 2])[i & 3]; f[i] = f32x2{bf_lo(v), bf_hi(v)}; }
  const bool b0 = __builtin_amdgcn_inverse_ballot_w64(0xaaaaaaaaaaaaaaaaull), b1 = __builtin_amdgcn_inverse_ballot_w64(0xccccccccccccccccull);
  const float d01 = b0 ? mq[1] : mq[0], d23 = b0 ? mq[3] : mq[2], dg = b1 ? d23 : d01;
  const float sq = __int_as_float(__builtin_amdgcn_ds_bpermute(diag, __float_as_int(dg))), sum = ms[0];
  const float mu = sum * (1.0f / CH);
  const float var = fmaxf(__builtin_fmaf(-mu, mu, sq * (1.0f / CH)), 0.f);
  const float rs = GA * __builtin_amdgcn_rsqf(var + 1e-5f);
  const float off = -mu * rs;
#pragma unroll
  for (int c = 0; c < 3; c++) {
    u32x4 o;
#pragma unroll
    for (int i = 0; i < 4; i++) { f32x2 y = __builtin_elementwise_fma(f[4 * c + i], f32x2{rs, rs}, f32x2{off, off}); o[i] = pack_bf16(y.x, y.y); }
    *(LDS_AS u32x4*)(L + dst + c * dst_step) = o;
  }
}

DI void layer_norm_last(LDS_AS char* L, int lane) {
  const int cq = lane >> 2, q = lane & 3, oq = 4 * (lane >> 4) + (lane & 3);
  const u32x2 a = __builtin_bit_cast(u32x2, tr_read(L + L_V + (4 * oq + ((lane >> 2) & 3)) * V_ROW + 48 * 8));
  const u32x2 b = __builtin_bit_cast(u32x2, tr_read(L + L_V + (64 + 4 * (oq & 7) + ((lane >> 2) & 3)) * V_ROW + 48 * 8));
  f32x2 f[4] = {{bf_lo(a.x), bf_hi(a.x)}, {bf_lo(a.y), bf_hi(a.y)}, {bf_lo(b.x), bf_hi(b.x)}, {bf_lo(b.y), bf_hi(b.y)}};
  const float m = cq < 8 ? 1.f : 0.f;
  const f32x2 s = (f[0] + f[1]) + (f[2] + f[3]) * m;
  const f32x2 ss = __builtin_elementwise_fma(f[0], f[0], f[1] * f[1]) + __builtin_elementwise_fma(f[2], f[2], f[3] * f[3]) * m;
  const float sum = dpp_add<0x124>(dpp_add<0x128>(sum_xor32(sum_xor16(s.x + s.y))));
  const float sq = dpp_add<0x124>(dpp_add<0x128>(sum_xor32(sum_xor16(ss.x + ss.y))));
  const float mu = sum * (1.0f / CH);
  const float var = fmaxf(__builtin_fmaf(-mu, mu, sq * (1.0f / CH)), 0.f);
  const float rs = GA * __builtin_amdgcn_rsqf(var + 1e-5f);
  const float off = -mu * rs;
#pragma unroll
  for (int i = 0; i < 4; i++) f[i] = __builtin_elementwise_fma(f[i], f32x2{rs, rs}, f32x2{off, off});
  LDS_AS char* d = L + L_YR + ((cq >> 1) * 4 + q) * 16 + ((cq ^ (cq >> 1)) & 1) * 8;
  *(LDS_AS u32x2*)d = u32x2{pack_bf16(f[0].x, f[0].y), pack_bf16(f[1].x, f[1].y)};
  if (cq < 8) *(LDS_AS u32x2*)(d + 512) = u32x2{pack_bf16(f[2].x, f[2].y), pack_bf16(f[3].x, f[3].y)};
}

DI float sum_sq8(const u32* h) {
  float r;
  asm volatile("v_dot2_f32_bf16 %0, %1, %1, 0\n\tv_dot2_f32_bf16 %0, %2, %2, %0\n\tv_dot2_f32_bf16 %0, %3, %3, %0\n\tv_dot2_f32_bf16 %0, %4, %4, %0\n\t"
               "v_dot2_f32_bf16 %0, %5, %5, %0\n\tv_dot2_f32_bf16 %0, %6, %6, %0\n\tv_dot2_f32_bf16 %0, %7, %7, %0\n\tv_dot2_f32_bf16 %0, %8, %8, %0\n\ts_nop 2"
               : "=&v"(r) : "v"(h[0]), "v"(h[1]), "v"(h[2]), "v"(h[3]), "v"(h[4]), "v"(h[5]), "v"(h[6]), "v"(h[7]));
  return r;
}
DI float add1(float a, float b) { float r; asm("v_add_f32 %0, %1, %2" : "=v"(r) : "v"(a), "v"(b)); return r; }
DI void bar_only() { asm volatile("s_barrier" ::: "memory"); }

#define STAMP(i)

__global__ void __launch_bounds__(NT, 3)
slak_block_kernel(gcu16*, gcu16*, gcu16*, gcu16*, gcu16*, gcu16*, gu16*, gu64*) {
  u64x8 karg;
  asm volatile("s_load_dwordx16 %0, %1, 0x0" : "=s"(karg) : "s"(__builtin_amdgcn_kernarg_segment_ptr()));
  LDS_AS char* const L = (LDS_AS char*)0u;
  const int t = threadIdx.x, lane = t & 63, bid = blockIdx.x;
  const int w = __builtin_amdgcn_readfirstlane(t >> 6);
  STAMP(0);
  const int xcd = bid & 7, idx = bid >> 3;
  const int y0 = 14 * ((xcd >> 1) * 4 + (idx >> 3)), x0 = 14 * ((xcd & 1) * 8 + (idx & 7));

  const int cg = w >> 1, xh = w & 1;
  const u32 tq = ((u32)t * 2731) >> 16, c4 = t - tq * 24;
  u32 dwo[2], xo[4], xdst[2];
  {
    const u32 r0 = 2 * (tq & 3) - (tq >> 2);
#pragma unroll
    for (int h = 0; h < 2; h++) dwo[h] = r0 + h < 7u ? (r0 + h) * (7 * CH * 2) + c4 * 8 : 0x80000000u;
  }
  auto item_offsets = [&](int it) {
    const u32 item = t + it * NT, P = (item * 5462) >> 16, o = item - P * 12, Y = (P * 6554) >> 16, X = P - Y * 10;
    const u32 gy = y0 - 3 + 2 * Y, gx = (it == 0 || item < 1200) ? x0 - 3 + 2 * X : 1024u;
    u32 base = (u32)(__mul24((int)gy, IMG * CH * 2) + __mul24((int)gx, CH * 2)) + o * 16;
    PIN(base);
    const u32 b0 = gx < (u32)IMG ? base : 0x80000000u, b1 = gx + 1 < (u32)IMG ? base + CH * 2 : 0x80000000u;
    xdst[it] = L_X + ((Y >> 1) * 10 + X) * X_REC + o * 16 + (Y & 1) * 8;
#pragma unroll
    for (int k = 0; k < 4; k++) xo[k] = gy + (k >> 1) < (u32)IMG ? ((k & 1) ? b1 : b0) + (k >> 1) * (IMG * CH * 2) : 0x80000000u;
  };
  item_offsets(0);
  u32 pfo = ((w == 0 ? idx & 3 : idx * 18) + lane * (w == 0 ? 4 : 1)) * 128;
  asm volatile("" : "+v"(dwo[0]), "+v"(dwo[1]), "+v"(xo[0]), "+v"(xo[1]), "+v"(xo[2]), "+v"(xo[3]), "+v"(pfo));
  SB();
  asm volatile("s_waitcnt lgkmcnt(0)" : "+s"(karg));
  gcu16* const x = (gcu16*)karg[0]; gcu16* const dw = (gcu16*)karg[1]; gcu16* const w1 = (gcu16*)karg[2]; gcu16* const w2 = (gcu16*)karg[3];
  gcu16* const grn_g = (gcu16*)karg[4]; gcu16* const grn_b = (gcu16*)karg[5]; gu16* const out = (gu16*)karg[6]; gu64* const ws = (gu64*)karg[7];
  const rsrc_t xrs = make_rsrc(x, X_BYTES);
  u32x2 dv[2][7];
  u32 warm = 0;
  if (w < 3) {
    if (FE_CODE && w == 0 && lane < CODE_LINES / 4)
      warm = *(const __attribute__((address_space(1))) u32*)((__builtin_amdgcn_s_getpc() & ~255ull) + pfo);
    const rsrc_t drs = make_rsrc(dw, 49 * CH * 2);
#pragma unroll
    for (int i = 0; i < 14; i++) dv[i / 7][i % 7] = __builtin_amdgcn_raw_buffer_load_b64(drs, dwo[i / 7] + (i % 7) * (CH * 2), 0, LD_AUX);
  }
  u32x4 xin[2][4];
  auto load_item = [&](int it) {
#pragma unroll
    for (int k = 0; k < 4; k++) xin[it][k] = ld128(xrs, xo[k]);
  };
  auto store_item = [&](int it) {
#pragma unroll
    for (int e = 0; e < 8; e++) {
      const u32x4* v = xin[it];
      *(volatile LDS_AS u32x2*)(L + xdst[it] + e * 192) = (e & 1) ? u32x2{perm_hi(v[0][e >> 1], v[1][e >> 1]), perm_hi(v[2][e >> 1], v[3][e >> 1])}
                                                                  : u32x2{perm_lo(v[0][e >> 1], v[1][e >> 1]), perm_lo(v[2][e >> 1], v[3][e >> 1])};
    }
  };
  load_item(0);
  if (FE_WARM && w == 1 && lane < 36) warm = *(const __attribute__((address_space(1))) u32*)((gchar*)(lane < 18 ? w1 : w2 - 18 * 64) + pfo);
  SB();
  if (w < 7) item_offsets(1);
  const u32x4 w1rs = {(u32)(u64)w1, (u32)((u64)w1 >> 32) & 0xffffu, CH * HID * 2, 0x00020000};
  const u32 w1d = (w / 6) * 3 * 6144 + (w % 6) * 1024;
  const u32 k1 = 16 * (w % 6) + (lane >> 2);
  u32 w1o = __umul24(pos_channel(k1 ^ (((k1 >> 3) & 1) << 2)), HID * 2) + (w / 6) * 384 + (lane & 3) * 16;
  u32 tp = L_T + (16 * cg + (cg >= 3 ? 4 : 0) + (lane >> 2) + 100 * (lane & 3)) * T_ROW;
  u32 xb = L_X + (4 * xh + (lane & 3)) * X_REC + (16 * cg + (lane >> 2)) * 16, xb2 = xb + 40 * X_REC;
  u32 vb = 4 * xh + (lane & 3) < 7 ? L_V + (16 * cg + (lane >> 2)) * V_ROW + (4 * xh + (lane & 3)) * 8 : L_HREM + lane * 8;
  asm volatile("" : "+v"(w1o), "+v"(tp), "+v"(xb), "+v"(xb2), "+v"(vb));
  const int T = w >> 1, Hh = w & 1, g4 = lane >> 4, q4 = (lane >> 2) & 3, p4 = lane & 3;
  enum { A_LN0, A_LN1, A_YD, A_DIAG, A_WB, A_WE, A_Y, A_SW, A_SR, A_WR, A_YR, A_W2V, A_OOFF, A_HR, A_N };
  u32 adr[A_N];
  auto make_adr = [&](int i) {
    u32 v;
    switch (i) {
      case A_LN0: v = L_V + (8 * g4 + 4 * (g4 & 1) + q4) * V_ROW + (4 * w + p4) * 8; break;
      case A_LN1: v = (g4 & 1) ? adr[A_LN0] - 4 * V_ROW : adr[A_LN0] + 4 * V_ROW; break;
      case A_YD: v = L_Y + T * 6144 + (g4 * 32 + 16 * Hh + (lane & 15)) * 16; break;
      case A_DIAG: v = ((lane & 15) + (lane & 12) * 4) * 4; break;
      case A_WB: v = (8 * (g4 >> 1) + q4) * 64 + (g4 & 1) * 32 + p4 * 8 + Hh * 3 * 6144; break;
      case A_WE: v = adr[A_WB] + L_W1E; break;
      case A_Y: v = L_Y + T * 6144 + (lane ^ (16 * Hh)) * 16; break;
      case A_SW: v = L_SCR + w * SCR_W + (lane & 31) * 72 + (lane >> 5) * 8; break;
      case A_SR: v = L_SCR + w * SCR_W + (8 * g4 + q4) * 72 + p4 * 8; break;
      case A_WR: v = (T < 3 ? L_W1E : L_W1L) + (Hh * 3 + T % 3) * 6144 + (8 * g4 + q4) * 64 + p4 * 8; break;
      case A_YR: v = L_YR + (g4 * 4 + p4) * 16; break;
      case A_W2V: v = (32 * w + 4 * ((lane >> 2) & 7) + (lane >> 5)) * (CH * 2) + p4 * 16; break;
      case A_OOFF: {
        const u32 pi = 4 * w + q4, py = (pi * 37) >> 8;
        v = (u32)__mul24((y0 + 2 * py + (p4 >> 1)) * IMG + x0 + 2 * (pi - 7 * py) + (p4 & 1), CH * 2) + g4 * 16;
      } break;
      default: v = L_HREM + (Hh * 24 + T * 4 + (lane >> 3)) * 64 + (lane & 7) * 2; break;
    }
    PIN(v);
    adr[i] = v;
  };
#pragma unroll
  for (int i = 0; i < A_N; i++) make_adr(i);
  SB();
  if (w < 3) {
#pragma unroll
    for (int e = 0; e < 4; e++) {
      u32 q[2][2][4];
#pragma unroll
      for (int h = 0; h < 2; h++) {
        u32 v[7];
#pragma unroll
        for (int j = 0; j < 7; j++) v[j] = dv[h][j][e >> 1];
#pragma unroll
        for (int sx = 0; sx < 3; sx++) {
          q[0][h][sx] = (e & 1) ? perm_hi(v[2 * sx], v[2 * sx + 1]) : perm_lo(v[2 * sx], v[2 * sx + 1]);
          q[1][h][sx + 1] = (e & 1) ? perm_hi(v[2 * sx + 1], v[2 * sx + 2]) : perm_lo(v[2 * sx + 1], v[2 * sx + 2]);
        }
        q[0][h][3] = (e & 1) ? v[6] >> 16 : v[6] & 0xffffu;
        q[1][h][0] = (e & 1) ? v[0] & 0xffff0000u : v[0] << 16;
      }
      LDS_AS char* d = L + L_T + (52 * (c4 & 1) + 12 * e + (c4 >> 1) + 200 * (tq >> 2)) * T_ROW + (tq & 3) * 32;
#pragma unroll
      for (int ox = 0; ox < 2; ox++) {
        *(LDS_AS u32x4*)(d + ox * 100 * T_ROW) = u32x4{q[ox][0][0], q[ox][1][0], q[ox][0][1], q[ox][1][1]};
        *(LDS_AS u32x4*)(d + ox * 100 * T_ROW + 16) = u32x4{q[ox][0][2], q[ox][1][2], q[ox][0][3], q[ox][1][3]};
      }
    }
  }
  if (t == NT - 1) *(LDS_AS u32x4*)(L + L_CNT) = u32x4{0, 0, 0, 0};
  STAMP(1);
  asm volatile("" : "+v"(xin[0][0]), "+v"(xin[0][1]), "+v"(xin[0][2]), "+v"(xin[0][3]));
  if (w < 7) load_item(1);
  SB();
  store_item(0);
  STAMP(2);
  bar_lds();
  STAMP(3);

  u64 epoch;
  {
    s16x8 wq[8], d[4][5];
    auto load_x = [&](int sx, int yp) {
      d[sx][yp] = yp < 4 ? *(volatile LDS_AS s16x8*)(L + xb + (yp * 10 + sx) * X_REC) : *(volatile LDS_AS s16x8*)(L + xb2 + sx * X_REC);
    };
    auto load_w = [&](int i) { wq[i] = *(volatile LDS_AS s16x8*)(L + tp + i * 16); };
    auto half = [](s16x8 v, int h) { return h ? __builtin_shufflevector(v, v, 4, 5, 6, 7) : __builtin_shufflevector(v, v, 0, 1, 2, 3); };
    auto conv_mfma = [&](f32x4& acc, int sx, int sy, int Yo) {
      acc = __builtin_amdgcn_mfma_f32_4x4x4bf16_1k(half(wq[2 * sy + (sx >> 1)], sx & 1), half(d[sx][(Yo + sy) >> 1], (Yo + sy) & 1), acc, 0, 0, 0);
    };
    auto put_row = [&](const f32x4& r, int Yo) { *(LDS_AS u32x2*)(L + vb + Yo * 56) = u32x2{pack_bf16(r[0], r[1]), pack_bf16(r[2], r[3])}; };
    load_w(0); load_x(0, 0); load_x(0, 1); load_w(2); load_w(4); load_x(0, 2); load_w(6);
    f32x4 acc[7];
#pragma unroll
    for (int i = 0; i < 7; i++) acc[i] = f32x4{0.f, 0.f, 0.f, 0.f};
    SB();
    static_for<16>([&](auto ic) {
      constexpr int i = ic.value, sx = i >> 2, sy = i & 3;
#pragma unroll
      for (int Yo = 0; Yo + sy < 6; Yo++) conv_mfma(acc[Yo], sx, sy, Yo);
      if constexpr (i < 9) load_x(1 + i / 3, i % 3);
      if constexpr (i < 4) load_w(2 * i + 1);
      if constexpr (i == 8) { if (w >= 7) arrive(2); }
      SB();
    });
    if (w < 7) {
      if (t < 432) store_item(1);
      arrive(2);
    }
    await(2);
    STAMP(4);
#pragma unroll
    for (int i = 0; i < 8; i++) load_x(i >> 1, 3 + (i & 1));
    epoch = ws_load(ws, WS_EPOCH);
#pragma unroll
    for (int Yo = 0; Yo < 3; Yo++) put_row(acc[Yo], Yo);
    SB();
    static_for<40>([&](auto ic) {
      constexpr int i = ic.value, sx = i / 10, j = i % 10;
      constexpr int yo[10] = {6, 5, 4, 6, 3, 5, 6, 4, 5, 6}, sy[10] = {0, 1, 2, 1, 3, 2, 2, 3, 3, 3};
      conv_mfma(acc[yo[j]], sx, sy[j], yo[j]);
      if constexpr (i == 4 || i == 12 || i == 20) dma16(w1rs, w1o, (i / 8) * 64, L_W1E + w1d + (i / 8) * 6144);
      if constexpr (i == 37) put_row(acc[3], 3);
      SB();
    });
#pragma unroll
    for (int Yo = 4; Yo < 7; Yo++) put_row(acc[Yo], Yo);
  }
  bar_lds();
  STAMP(5);

  const bool bias_wave = w == 4 && bid < CH;
  u32 bw[6], bb[6];
  if (bias_wave) {
#pragma unroll
    for (int i = 0; i < 6; i++) {
      bw[i] = __builtin_amdgcn_raw_buffer_load_b16(make_rsrc(w2, HID * CH * 2), lane * (CH * 2), bid * 2 + i * (64 * CH * 2), 0);
      bb[i] = __builtin_amdgcn_raw_buffer_load_b16(make_rsrc(grn_b, HID * 2), lane * 2 + i * 128, 0, 0);
    }
  }
  SB();
  layer_norm_unit(L, lane, adr[A_LN0], adr[A_LN1], adr[A_YD], adr[A_DIAG], [&]() {
#pragma unroll
    for (int u = 0; u < 3; u++) dma16(w1rs, w1o, 192 + u * 64, L_W1L + w1d + u * 6144);
  });
  if (w == LN_LAST) layer_norm_last(L, lane);
  asm volatile("s_waitcnt vmcnt(0)" :: "v"(warm) : "memory");
  epoch = ((u64)(u32)__builtin_amdgcn_readfirstlane((int)(epoch >> 32)) << 32) | (u32)__builtin_amdgcn_readfirstlane((int)epoch);
  const u64 tag = (epoch + 1) & ((1ull << 40) - 1);
  float bias_val = 0.f;
  if (bias_wave) {
    float a = 0.f;
#pragma unroll
    for (int i = 0; i < 6; i++) { asm volatile("" : "+v"(bw[i]), "+v"(bb[i])); a = __builtin_fmaf(bf_lo(bw[i]), bf_lo(bb[i]), a); }
    bias_val = __int_as_float(__builtin_amdgcn_readfirstlane(__float_as_int(wave_sum(a))));
  }
  bar_lds();
  STAMP(6);

  s16x8 hk[6], hs[6];
  const int rq = T / 3, otj[3] = {T % 3, (T + 1) % 3, (T + 2) % 3};
  const u32 totw = WS_TOT + (bid & 7) * TOT_ROW;
  const u32 ooff = adr[A_OOFF];
  u32x4 xr[3];
  u64 bgr;
  const f32x16 zero16 = {0.f, 0.f, 0.f, 0.f, 0.f, 0.f, 0.f, 0.f, 0.f, 0.f, 0.f, 0.f, 0.f, 0.f, 0.f, 0.f};
  {
    LDS_AS char* wE = L + adr[A_WE];
    LDS_AS char* wL = L + adr[A_WB] + L_W1L;
    LDS_AS char* sw = L + adr[A_SW];
    LDS_AS char* sr = L + adr[A_SR];
    LDS_AS char* wr = L + adr[A_WR];
    LDS_AS char* yr = L + adr[A_YR];
    s16x8 yf[6], wf[6], ry, rw[2];
    f32x4 racc[2] = {{0.f, 0.f, 0.f, 0.f}, {0.f, 0.f, 0.f, 0.f}};
    float ssv[6];
    f32x16 acc = zero16;
    __builtin_amdgcn_s_setprio(3);
#pragma unroll
    for (int k = 0; k < 6; k++) { yf[k] = *(LDS_AS s16x8*)(L + adr[A_Y] + k * 1024); wf[k] = tr_frag(wE + k * 1024, 256); }
    SB();
#pragma unroll
    for (int k = 0; k < 6; k++) {
      acc = mfma32(yf[k], wf[k], acc);
      wf[k] = tr_frag(wE + 6144 + k * 1024, 256);
      SB();
    }
    const u32 w2raw = wave_slot(w);
    auto fetch_w2 = [&](int c) {
      await(c);
      const u32x4 rs = {(u32)(u64)w2, (u32)((u64)w2 >> 32) & 0xffffu, HID * CH * 2, 0x00020000};
#pragma unroll
      for (int j = 0; j < 6; j++) dma16(rs, adr[A_W2V], (j & 1) * 384 + (j >> 1) * 64, w2raw + j * 1024);
    };
    arrive(0);
    static_for<6>([&](auto sc_) {
      constexpr int s = sc_.value, NP = s == 3 ? 10 : 8;
      LDS_AS char* wp2 = (s + 2 < 3 ? wE : wL) + ((s + 2) % 3) * 6144;
      __builtin_amdgcn_s_setprio((G1_PRIO >> (2 * s)) & 3);
      if constexpr (s == 0) { if (bias_wave && lane < 8) ws_store(ws, WS_TOT + lane * TOT_ROW + TOT_BIAS + bid, granule(tag, bias_val)); }
      if constexpr (s == 3) arrive(1);
      if constexpr (s == 1) { if (w < 6) fetch_w2(0); }
      if constexpr (s == 4) { if (w >= 6) fetch_w2(1); }
      if constexpr (s < 3) { ry = *(LDS_AS s16x8*)(yr + s * 256); rw[0] = tr_frag(wr + s * 2048, 256); rw[1] = tr_frag(wr + s * 2048 + 32, 256); }
      f32x2 xv[NP], z[NP];
#pragma unroll
      for (int i = 0; i < NP; i++) {
        if (i < 8) xv[i] = f32x2{acc[2 * (i & 7)], acc[2 * (i & 7) + 1]};
        else xv[i] = __builtin_amdgcn_inverse_ballot_w64(0xffffull) ? f32x2{racc[0][2 * (i & 1)], racc[0][2 * (i & 1) + 1]} : f32x2{racc[1][2 * (i & 1)], racc[1][2 * (i & 1) + 1]};
        z[i] = __builtin_elementwise_fma(xv[i], xv[i], f32x2{GK, GK});
      }
#pragma unroll
      for (int i = 0; i < NP; i++) PIN(z[i]);
      SB();
#pragma unroll
      for (int i = 0; i < NP; i++) z[i] = __builtin_elementwise_fma(-z[i], xv[i], f32x2{-GS, -GS});
#pragma unroll
      for (int i = 0; i < NP; i++) PIN(z[i]);
      SB();
      f32x16 nxt = zero16;
      float e[2 * NP];
#pragma unroll
      for (int i = 0; i < 2 * NP; i++) {
        if (s < 5 && i % 3 == 0 && i < 15) { nxt = mfma32(yf[i / 3], wf[i / 3], nxt); PIN(nxt); SB(); }
        if (s < 3 && i == 15) { racc[0] = mfma16(ry, rw[0], racc[0]); PIN(racc[0]); SB(); }
        if (s == 5 && i % 3 == 0 && i < 9) { xr[i / 3] = __builtin_amdgcn_raw_buffer_load_b128(xrs, ooff, otj[i / 3] * 64, LD_AUX); SB(); }
        if (s == 5 && i == 9) { bgr = ws_load(ws + (totw + TOT_BIAS + (w & 1) * 64), lane); SB(); }
        e[i] = __builtin_amdgcn_fmed3f(__builtin_amdgcn_exp2f(z[i >> 1][i & 1]), 0.f, 1.f);
        PIN(e[i]);
      }
      SB();
      if constexpr (s < 4) {
#pragma unroll
        for (int k = 0; k < 5; k++) wf[k] = tr_frag(wp2 + k * 1024, 256);
      }
#pragma unroll
      for (int i = 0; i < NP; i++) z[i] = f32x2{e[2 * i], e[2 * i + 1]} + GC;
#pragma unroll
      for (int i = 0; i < NP; i++) PIN(z[i]);
      SB();
      f32x2 pp[NP / 2], rr[2];
#pragma unroll
      for (int i = 0; i < NP / 2; i++) pp[i] = z[2 * i] * z[2 * i + 1];
      xv[0] *= z[1]; xv[1] *= z[0];
#pragma unroll
      for (int i = 0; i < NP / 2; i++) PIN(pp[i]);
      PIN(xv[0]); PIN(xv[1]);
      SB();
      rr[0] = pp[0] * pp[1]; rr[1] = pp[2] * pp[3];
      PIN(rr[0]); PIN(rr[1]);
      SB();
#pragma unroll
      for (int i = 2; i < NP; i++) xv[i] *= z[i ^ 1];
#pragma unroll
      for (int i = 2; i < NP; i++) PIN(xv[i]);
      SB();
      if constexpr (s < 5) { nxt = mfma32(yf[5], wf[5], nxt); PIN(nxt); SB(); }
      float rc[6];
#pragma unroll
      for (int i = 0; i < (NP == 10 ? 6 : 4); i++) {
        if (s < 3 && i == 3) { racc[1] = mfma16(ry, rw[1], racc[1]); PIN(racc[1]); SB(); }
        rc[i] = __builtin_amdgcn_rcpf(i < 4 ? rr[(i >> 1) & 1][i & 1] : pp[NP / 2 - 1][i & 1]);
        PIN(rc[i]);
      }
      SB();
      if constexpr (s < 4) wf[5] = tr_frag(wp2 + 5 * 1024, 256);
      f32x2 ip[NP / 2];
#pragma unroll
      for (int i = 0; i < 4; i++) ip[i] = f32x2{rc[i & 2], rc[(i & 2) + 1]} * pp[i ^ 1];
      if constexpr (NP == 10) ip[4] = f32x2{rc[4], rc[5]};
#pragma unroll
      for (int i = 0; i < 4; i++) PIN(ip[i]);
      SB();
#pragma unroll
      for (int i = 0; i < NP; i++) xv[i] *= ip[i >> 1];
#pragma unroll
      for (int i = 0; i < NP; i++) PIN(xv[i]);
      SB();
      u32 hp8[8];
#pragma unroll
      for (int i = 0; i < 8; i++) { hp8[i] = pack_bf16(xv[i].x, xv[i].y); PIN(hp8[i]); }
      SB();
#pragma unroll
      for (int Q = 0; Q < 4; Q++) *(volatile LDS_AS u32x2*)(sw + Q * 16) = u32x2{hp8[2 * Q], hp8[2 * Q + 1]};
      SB();
      ssv[s] = sum_sq8(hp8);
      hk[s] = tr_frag(sr, 4 * 72);
      hs[s] = tr_frag(sr + 32, 4 * 72);
      if constexpr (s == 3) {
        const f32x2 sq = __builtin_elementwise_fma(xv[8], xv[8], xv[9] * xv[9]);
        if (lane < 32) {
          const u32 p01 = pack_bf16(xv[8].x, xv[8].y), p23 = pack_bf16(xv[9].x, xv[9].y);
          LDS_AS unsigned short* hr = (LDS_AS unsigned short*)(L + adr[A_HR]);
          hr[0] = (unsigned short)p01; hr[8] = (unsigned short)(p01 >> 16); hr[16] = (unsigned short)p23; hr[24] = (unsigned short)(p23 >> 16);
          *(LDS_AS float*)(sw + 64) = sq.x + sq.y;
        }
      }
      acc = nxt;
      SB();
    });
#pragma unroll
    for (int i = 0; i < 3; i++) *(volatile LDS_AS f32x2*)(sw + i * 16) = f32x2{ssv[2 * i], ssv[2 * i + 1]};
  }
  asm volatile("" : "+v"(xr[0]), "+v"(xr[1]), "+v"(xr[2]), "+v"(bgr));
  bar_all();
  STAMP(7);

  if (t < HID) {
    float v[13];
    LDS_AS char* q = L + L_SCR + (w / 3) * SCR_W + (lane & 31) * 72;
#pragma unroll
    for (int i = 0; i < 12; i++) v[i] = *(LDS_AS float*)(q + (w % 3) * 16 + (lane >> 5) * 4 + (i >> 1) * 2 * SCR_W + (i & 1) * 8);
    v[12] = *(LDS_AS float*)(q + (2 * (w % 3) + (lane >> 5)) * 2 * SCR_W + 64);
    const float s = (((v[0] + v[1]) + (v[2] + v[3])) + ((v[4] + v[5]) + (v[6] + v[7]))) + (((v[8] + v[9]) + (v[10] + v[11])) + v[12]);
    ws_store(ws, WS_PART + (((u32)t >> 4) * 256 + bid) * 16 + (t & 15), granule(tag, s));
  }
  u32x2 w2r[12];
  u32 ap[4], ab[2], hp, rw, w2d0, w2d1;
  f32x2 w2f[24];
  auto unpack_w2 = [&](int i) {
    const int ot = i >> 3, r = (i >> 1) & 3, kp = i & 1;
    const u32 a = w2r[4 * ot + 2 * kp][r >> 1], b = w2r[4 * ot + 2 * kp + 1][r >> 1];
    w2f[i] = (r & 1) ? f32x2{bf_hi(a), bf_hi(b)} : f32x2{bf_lo(a), bf_lo(b)};
    PIN(w2f[i]);
  };
  auto swap_out = [&]() {
    SB();
#pragma unroll
    for (int i = 0; i < 12; i++) w2r[i] = *(volatile LDS_AS u32x2*)(L + wave_slot(w) + lane * 8 + i * 512);
#pragma unroll
    for (int i = 0; i < 6; i++) *(volatile LDS_AS s16x8*)(L + wave_slot(w) + lane * 16 + i * 1024) = hs[i];
    if (t == 512) *(LDS_AS u32*)(L + L_FL) = 0;
    SB();
    const u32 w2d = L_W2 + ((t & 1) * 12 + w) * 1024 + (16 * g4 + ((t >> 1) & 3)) * 16 + (t & 8);
    const u32 aslot = L_W2 + (16 * g4 + q4) * 16;
    ab[0] = aslot + p4 * 64; ab[1] = aslot + (p4 ^ 1) * 64 + 12288;
#pragma unroll
    for (int i = 0; i < 4; i++) ap[i] = (((i >> 1) ^ rq) ? ab[1] : ab[0]) + (otj[0] * 24576 + ((i & 1) ^ Hh) * 6144);
    hp = L_HREM + (Hh * 24 + g4) * 64 + p4 * 16;
    rw = L_RP + (Hh * 4 * CH + 32 * otj[0] + 4 * rq) * 4 + p4 * (CH * 4) + g4 * 32;
    w2d0 = w2d + (t & 1) * 64; w2d1 = w2d + ((t & 1) ^ 1) * 64;
    asm volatile("" : "+v"(ap[0]), "+v"(ap[1]), "+v"(ap[2]), "+v"(ap[3]), "+v"(ab[0]), "+v"(ab[1]), "+v"(hp), "+v"(rw), "+v"(w2d0), "+v"(w2d1));
#pragma unroll
    for (int i = 0; i < W2_EARLY; i++) unpack_w2(i);
    SB();
  };
  {
    const bool mine = (u32)(t - HID) < CH;
    if (!__all(!mine || (bgr >> 24) == tag)) {
#pragma unroll 1
      for (int spin = 0; spin < SPIN_CAP; spin++) {
        bgr = ws_load(ws + (totw + TOT_BIAS + (w & 1) * 64), lane);
        if (__all(!mine || (bgr >> 24) == tag)) break;
      }
    }
    if (mine) *(LDS_AS float*)(L + L_BP + (t - HID) * 4) = granule_val(bgr);
  }
  if (bid < 24) {
    u64 gr[6];
    const u32 partw = WS_PART + bid * 4096 + t;
#pragma unroll
    for (int i = 0; i < 6; i++) gr[i] = ws_load(ws, partw + (i < 5 || t < 256 ? i * NT : 0));
    swap_out();
#pragma unroll 1
    for (int spin = 0; spin < SPIN_CAP; spin++) {
      bool ok = true;
#pragma unroll
      for (int i = 0; i < 6; i++) ok &= (gr[i] >> 24) == tag;
      if (__all(ok)) break;
#pragma unroll
      for (int i = 0; i < 6; i++)
        if ((gr[i] >> 24) != tag) gr[i] = ws_load(ws, partw + (i < 5 || t < 256 ? i * NT : 0));
    }
    float s = ((granule_val(gr[0]) + granule_val(gr[1])) + (granule_val(gr[2]) + granule_val(gr[3]))) + (granule_val(gr[4]) + (t < 256 ? granule_val(gr[5]) : 0.f));
    s = sum_xor32(sum_xor16(s));
    if (lane < 16) *(LDS_AS float*)(L + L_RED + (lane * 12 + w) * 4) = s;
  } else {
    swap_out();
  }
  bar_lds();
  if (bid < 24 && w < 2) {
    LDS_AS char* q = L + L_RED + (t & 15) * 48;
    const f32x4 a = *(LDS_AS f32x4*)(q) + *(LDS_AS f32x4*)(q + 16) + *(LDS_AS f32x4*)(q + 32);
    const float gx = __builtin_amdgcn_sqrtf(__builtin_fmaf((a[0] + a[1]) + (a[2] + a[3]), GA_INV2, 1e-6f));
    ws_store(ws, WS_TOT + (t >> 4) * TOT_ROW + bid * 16 + (t & 15), granule(tag, gx));
  }
  SB();
#pragma unroll
  for (int i = 0; i < 6; i++) hs[i] = *(LDS_AS s16x8*)(L + wave_slot(w ^ 1) + lane * 16 + i * 1024);
  const u32 remo = lane < 48 ? (u32)(((y0 + 12) * IMG + x0 + 12) * (CH * 2)) + (lane >= 24 ? IMG * CH * 2 - 384 : 0) + lane * 16 : 0x80000000u;
  u32x4 xrem = ld128(xrs, remo);
  u32 gam = grn_g[t < HID ? t : 0];
  f32x4 acc2[3][2];
#pragma unroll
  for (int i = 0; i < 6; i++) acc2[i >> 1][i & 1] = *(LDS_AS f32x4*)(L + L_BP + g4 * 32 + otj[i >> 1] * 128 + ((i & 1) ^ (i < 2 ? rq : 0)) * 16);
  SB();
#pragma unroll
  for (int i = 0; i < 6; i++) {
    const u32x4 xv = i < 2 && rq ? u32x4{xr[0][2], xr[0][3], xr[0][0], xr[0][1]} : xr[i >> 1];
    acc2[i >> 1][i & 1] += f32x4{bf_lo(xv[2 * (i & 1)]), bf_hi(xv[2 * (i & 1)]), bf_lo(xv[2 * (i & 1) + 1]), bf_hi(xv[2 * (i & 1) + 1])};
    asm volatile("" : "+v"(acc2[i >> 1][i & 1]));
  }
  SB();
  const f32x4 zero4 = {0.f, 0.f, 0.f, 0.f};
#pragma unroll
  for (int i = W2_EARLY; i < 24; i++) unpack_w2(i);
#pragma unroll
  for (int i = 0; i < 6; i++) asm volatile("" : "+v"(hs[i]));
  STAMP(8);
  if (t >= HID) PIN(xrem);

  if (t < HID) {
    u64 g;
    if (POLL_DEPTH == 1) {
#pragma unroll 1
      for (int spin = 0; spin < SPIN_CAP; spin++) {
        g = ws_load(ws, totw + t);
        if (__all((g >> 24) == tag)) break;
      }
    } else {
      u64 ga = ws_load(ws, totw + t);
      __builtin_amdgcn_s_sleep(POLL_SLEEP);
      u64 gb = ws_load(ws, totw + t);
#pragma unroll 1
      for (int spin = 0; spin < SPIN_CAP; spin++) {
        g = ga;
        if (__all((g >> 24) == tag)) break;
        ga = ws_load(ws, totw + t);
        g = gb;
        if (__all((g >> 24) == tag)) break;
        gb = ws_load(ws, totw + t);
      }
    }
    PIN(xrem);
    const float gx = granule_val(g);
    *(LDS_AS float*)(L + L_GG + t * 4) = bf_lo(gam) * gx;
    const float s = wave_sum(gx);
    if (lane == 0) *(LDS_AS float*)(L + L_PS + w * 4) = s;
  }
  bar_lds();
  STAMP(9);
  {
    const f32x4 pa = *(LDS_AS f32x4*)(L + L_PS);
    const f32x2 pb = *(LDS_AS f32x2*)(L + L_PS + 16);
    const f32x4 gg = *(LDS_AS f32x4*)(L + L_GG + (t >> 3) * 16);
    if (bid == 0 && t == NT - 1) ws_store(ws, WS_EPOCH, epoch + 1);
    const float mean = (((pa[0] + pa[1]) + (pa[2] + pa[3])) + (pb[0] + pb[1])) * (1.0f / HID);
    const float inv = GA_INV * __builtin_amdgcn_rcpf(mean + 1e-6f);
    const f32x2 s01 = f32x2{gg[0], gg[1]} * inv + GA_INV, s23 = f32x2{gg[2], gg[3]} * inv + GA_INV;
#pragma unroll
    for (int i = 0; i < 12; i++) {
      const f32x2 a = w2f[2 * i] * s01, b = w2f[2 * i + 1] * s23;
      *(volatile LDS_AS u32x2*)(L + (i & 1 ? w2d1 : w2d0) + ((i >> 1) & 1) * 128 + (i >> 2) * 24576) = u32x2{pack_bf16(a.x, a.y), pack_bf16(b.x, b.y)};
    }
  }

  {
    const rsrc_t ors = make_rsrc(out, X_BYTES);
    constexpr int PF = G2_PF;
    s16x8 af[72], rb[6];
    f32x4 ra = zero4;
    u32 rem_cnt = 0;
    auto load_a = [&](int i) {
      if (i % 24 == 0 && i) {
#pragma unroll
        for (int j = 0; j < 4; j++) ap[j] = ab[j >> 1] + (otj[i / 24] * 24576 + ((j & 1) ^ Hh) * 6144);
      }
      af[i] = *(volatile LDS_AS s16x8*)(L + ap[(i / 6) & 3] + (i % 6) * 1024);
    };
    auto store_tile = [&](int j) {
      const u32x2 a = {pack_bf16(acc2[j][0][0], acc2[j][0][1]), pack_bf16(acc2[j][0][2], acc2[j][0][3])};
      const u32x2 b = {pack_bf16(acc2[j][1][0], acc2[j][1][1]), pack_bf16(acc2[j][1][2], acc2[j][1][3])};
      __builtin_amdgcn_raw_buffer_store_b128(j == 0 && rq ? u32x4{b.x, b.y, a.x, a.y} : u32x4{a.x, a.y, b.x, b.y}, ors, ooff, otj[j] * 64, ST_AUX);
    };
    bar_lds();
    STAMP(10);
    if (G2_PRIO) __builtin_amdgcn_s_setprio(3);
#pragma unroll
    for (int i = 0; i < PF; i++) load_a(i);
    SB();
    static_for<72>([&](auto ic) {
      constexpr int i = ic.value, ot = i / 24, rt = (i / 12) & 1, k = i % 12;
      acc2[ot][rt] = mfma16(af[i], k < 6 ? hk[k % 6] : hs[k % 6], acc2[ot][rt]);
      if constexpr (i + PF < 72) load_a(i + PF);
      if constexpr (G2_PRIO && i % 18 == 17 && i < 60) __builtin_amdgcn_s_setprio(2 - i / 18);
      if constexpr (i < 12 && (i & 1)) rb[i >> 1] = *(volatile LDS_AS s16x8*)(L + hp + (i >> 1) * 256);
      if constexpr (i >= 6 && i < 18 && (i & 1) == 0) ra = mfma16(af[(i - 6) >> 1], rb[(i - 6) >> 1], ra);
      if constexpr (i == 19) {
        if (q4 == 0) *(volatile LDS_AS f32x4*)(L + rw) = ra;
        u32 fl = L_FL;
        PIN(fl);
        if (lane == 0) rem_cnt = __hip_atomic_fetch_add((LDS_AS u32*)(L + fl), 1u, __ATOMIC_RELAXED, __HIP_MEMORY_SCOPE_WORKGROUP);
      }
      if constexpr (i == 25 || i == 49) store_tile(ot - 1);
      if constexpr (i == 23) { STAMP(11); }
      if constexpr (i == 47) { STAMP(12); }
      if constexpr (i == 40) {
        if (__builtin_amdgcn_readfirstlane(rem_cnt) == 11 && lane < 48) {
          f32x4 xq[2];
#pragma unroll
          for (int j = 0; j < 2; j++) {
            const f32x4 a = *(volatile LDS_AS f32x4*)(L + L_RP + (lane * 8 + j * 4) * 4), b = *(volatile LDS_AS f32x4*)(L + L_RP + (4 * CH + lane * 8 + j * 4) * 4);
            const f32x4 c = *(volatile LDS_AS f32x4*)(L + L_BP + (lane - 12 * ((lane * 43) >> 9)) * 32 + j * 16);
#pragma unroll
            for (int e = 0; e < 4; e++) xq[j][e] = add1(add1((e & 1) ? bf_hi(xrem[2 * j + (e >> 1)]) : bf_lo(xrem[2 * j + (e >> 1)]), c[e]), add1(a[e], b[e]));
          }
          __builtin_amdgcn_raw_buffer_store_b128(u32x4{pack_bf16(xq[0][0], xq[0][1]), pack_bf16(xq[0][2], xq[0][3]), pack_bf16(xq[1][0], xq[1][1]), pack_bf16(xq[1][2], xq[1][3])}, ors, remo, 0, ST_AUX);
        }
      }
      SB();
    });
    STAMP(13);
    store_tile(2);
  }
  STAMP(14);
}

extern "C" void kernel_launch(void* const* d_in, const int* in_sizes, int n_in,
                              void* d_out, int out_size, void* d_ws, size_t ws_size,
                              hipStream_t stream) {
  (void)in_sizes; (void)n_in; (void)out_size; (void)ws_size;
  (void)hipFuncSetAttribute((const void*)slak_block_kernel, hipFuncAttributeMaxDynamicSharedMemorySize, LDS_BYTES);
  slak_block_kernel<<<256, NT, LDS_BYTES, stream>>>(
      (gcu16*)d_in[0], (gcu16*)d_in[1], (gcu16*)d_in[2], (gcu16*)d_in[3], (gcu16*)d_in[4], (gcu16*)d_in[5], (gu16*)d_out, (gu64*)d_ws);
}
```

```cpp
#include <hip/hip_runtime.h>
#include <stdint.h>

typedef unsigned long long u64;
typedef unsigned u32;
typedef float f32x2 __attribute__((ext_vector_type(2)));
typedef float f32x4 __attribute__((ext_vector_type(4)));
typedef float f32x16 __attribute__((ext_vector_type(16)));
typedef short s16x4 __attribute__((ext_vector_type(4)));
typedef short s16x8 __attribute__((ext_vector_type(8)));
typedef __bf16 bf16x2 __attribute__((ext_vector_type(2)));
typedef u32 u32x2 __attribute__((ext_vector_type(2)));
typedef u32 u32x4 __attribute__((ext_vector_type(4)));
typedef u64 u64x8 __attribute__((ext_vector_type(8)));
#define LDS_AS __attribute__((address_space(3)))
typedef __attribute__((address_space(1))) u64 gu64;
typedef __attribute__((address_space(1))) unsigned short gu16;
typedef __attribute__((address_space(1))) const unsigned short gcu16;
#define RLX_AGENT __ATOMIC_RELAXED, __HIP_MEMORY_SCOPE_AGENT
#define DI __device__ __forceinline__

constexpr int IMG = 224, CH = 96, HID = 384, NT = 768;
constexpr u32 X_BYTES = IMG * IMG * CH * 2;

constexpr float GA = 0.4686686930532063f, GK = 4.91222953926424f;
constexpr float GC = 0x1p-30f, GS = 30.f;
constexpr float GA_INV = 2.1337034344781243f * 0x1p-30f, GA_INV2 = 4.552690346303743f * 0x1p-60f;

constexpr u32 L_X = 0, X_REC = 1600;
constexpr u32 L_Y = 0, L_YR = 36864;
constexpr u32 L_W1L = 37632;
constexpr u32 L_SCR = 74496, SCR_W = 2304;
constexpr u32 L_V = 83200, V_ROW = 416;
constexpr u32 L_T = 83200, T_ROW = 144;
constexpr u32 L_W1E = 123136;
constexpr u32 L_HREM = 160000;
constexpr u32 L_CNT = 163072;
constexpr u32 L_W2 = 0;
constexpr u32 L_RP = 102144;
constexpr u32 L_BP = 105216;
constexpr u32 L_GG = 105600, L_PS = 107136, L_RED = 107200;
constexpr u32 L_FL = 107968;
constexpr u32 LDS_BYTES = 163840;

constexpr u32 TOT_ROW = 512, TOT_BIAS = 384;
constexpr u32 WS_EPOCH = 0, WS_TOT = 64, WS_PART = 64 + 8 * TOT_ROW;
#define CODE_LINES 176
constexpr int SPIN_CAP = 1 << 20;

#define SB() __builtin_amdgcn_sched_barrier(0)
#define PIN(v) asm volatile("" : "+v"(v))
template <int V> struct int_c { static constexpr int value = V; };
template <int N, int I = 0, class F> DI void static_for(F&& f) { if constexpr (I < N) { f(int_c<I>{}); static_for<N, I + 1>(f); } }
DI void bar_lds() { asm volatile("s_waitcnt lgkmcnt(0)\n\ts_barrier" ::: "memory"); }
DI void bar_all() { asm volatile("s_waitcnt vmcnt(0) lgkmcnt(0)\n\ts_barrier" ::: "memory"); }
#define LD_AUX 0
#define FE_WARM 1
#define LN_LAST 2
#define POLL_SLEEP 8
#define POLL_DEPTH 1
#define G1_PRIO 0x1af
#define G2_PRIO 1
#define W2_EARLY 8
#define G2_PF 3
#define ST_AUX 16
typedef __amdgpu_buffer_rsrc_t rsrc_t;
DI rsrc_t make_rsrc(gcu16* p, u32 bytes) { return __builtin_amdgcn_make_buffer_rsrc((void*)p, 0, bytes, 0x00020000); }
DI u32x4 ld128(rsrc_t r, u32 off) { return __builtin_amdgcn_raw_buffer_load_b128(r, off, 0, LD_AUX); }
DI void dma16(u32x4 rs, u32 voff, u32 soff, u32 lds) {
  u32 keep;
  asm volatile("s_mov_b32 %0, m0\n\ts_mov_b32 m0, %1\n\ts_nop 0\n\tbuffer_load_dwordx4 %2, %3, %4 offen lds\n\ts_mov_b32 m0, %0"
               : "=&s"(keep) : "s"(lds), "v"(voff), "s"(rs), "s"(soff) : "memory");
}
DI u32 pack_bf16(float a, float b) { bf16x2 v = __builtin_convertvector(f32x2{a, b}, bf16x2); return __builtin_bit_cast(u32, v); }
DI float bf_lo(u32 v) { return __uint_as_float(v << 16); }
DI float bf_hi(u32 v) { return __uint_as_float(v & 0xffff0000u); }
DI u32 perm_lo(u32 a, u32 b) { return __builtin_amdgcn_perm(b, a, 0x05040100u); }
DI u32 perm_hi(u32 a, u32 b) { return __builtin_amdgcn_perm(b, a, 0x07060302u); }
DI s16x4 tr_read(LDS_AS char* p) { return __builtin_amdgcn_ds_read_tr16_b64_v4i16((LDS_AS s16x4*)p); }
DI s16x8 tr_frag(LDS_AS char* p, int step) {
  s16x4 a = tr_read(p), b = tr_read(p + step);
  return __builtin_shufflevector(a, b, 0, 1, 2, 3, 4, 5, 6, 7);
}
DI f32x16 mfma32(s16x8 a, s16x8 b, f32x16 c) { return __builtin_amdgcn_mfma_f32_32x32x16_bf16(a, b, c, 0, 0, 0); }
DI f32x4 mfma16(s16x8 a, s16x8 b, f32x4 c) { return __builtin_amdgcn_mfma_f32_16x16x32_bf16(a, b, c, 0, 0, 0); }
DI float sum_xor32(float v) { auto r = __builtin_amdgcn_permlane32_swap(__float_as_uint(v), __float_as_uint(v), false, false); return __uint_as_float(r[0]) + __uint_as_float(r[1]); }
DI float sum_xor16(float v) { auto r = __builtin_amdgcn_permlane16_swap(__float_as_uint(v), __float_as_uint(v), false, false); return __uint_as_float(r[0]) + __uint_as_float(r[1]); }
typedef __attribute__((address_space(1))) char gchar;
DI u64 ws_load(gu64* ws, u32 word) { return __hip_atomic_load((gu64*)((gchar*)ws + word * 8u), RLX_AGENT); }
DI void ws_store(gu64* ws, u32 word, u64 v) { __hip_atomic_store((gu64*)((gchar*)ws + word * 8u), v, RLX_AGENT); }
DI u64 granule(u64 tag, float v) { return (tag << 24) | (u64)((__float_as_uint(v) + 0x80u) >> 8); }
DI float granule_val(u64 g) { return __uint_as_float((u32)g << 8); }
template <int CTRL> DI float dpp_add(float v) { return v + __builtin_bit_cast(float, __builtin_amdgcn_update_dpp(0, __builtin_bit_cast(int, v), CTRL, 0xf, 0xf, false)); }
DI float wave_sum(float v) { return dpp_add<0xb1>(dpp_add<0x4e>(dpp_add<0x124>(dpp_add<0x128>(sum_xor32(sum_xor16(v)))))); }
DI void arrive(int c) {
  if (__builtin_amdgcn_inverse_ballot_w64(1ull)) asm volatile("ds_add_u32 %0, %1" :: "v"(L_CNT + 4 * c), "v"(1u) : "memory");
}
DI void await(int c) {
#pragma unroll 1
  for (int spin = 0; spin < SPIN_CAP; spin++) {
    if (__builtin_amdgcn_readfirstlane(*(volatile LDS_AS u32*)(L_CNT + 4 * c)) >= 12) break;
    __builtin_amdgcn_s_sleep(1);
  }
  asm volatile("" ::: "memory");
}
DI u32 wave_slot(int w) { return w < 6 ? L_Y + w * 6144 : L_W1E + (w - 6) * 6144; }
DI u32 pos_channel(u32 p) { const u32 e = (p * 43) >> 9; return 8 * (p - 12 * e) + e; }

DI void layer_norm_unit(LDS_AS char* L, u32 src0, u32 src1, u32 dst, u32 diag) {
  LDS_AS char* p0 = L + src0;
  LDS_AS char* p1 = L + src1;
  constexpr u32 dst_step = 2048;
  s16x8 vf[3];
#pragma unroll
  for (int c = 0; c < 3; c++)
    vf[c] = __builtin_shufflevector(tr_read(p0 + c * 32 * V_ROW), tr_read(p1 + c * 32 * V_ROW), 0, 1, 2, 3, 4, 5, 6, 7);
  const s16x8 ones = {0x3f80, 0x3f80, 0x3f80, 0x3f80, 0x3f80, 0x3f80, 0x3f80, 0x3f80};
  f32x4 ms = {0.f, 0.f, 0.f, 0.f}, mq = {0.f, 0.f, 0.f, 0.f};
#pragma unroll
  for (int c = 0; c < 3; c++) { mq = mfma16(vf[c], vf[c], mq); ms = mfma16(ones, vf[c], ms); }
  f32x2 f[12];
#pragma unroll
  for (int i = 0; i < 12; i++) { const u32 v = __builtin_bit_cast(u32x4, vf[i >> 2])[i & 3]; f[i] = f32x2{bf_lo(v), bf_hi(v)}; }
  const bool b0 = __builtin_amdgcn_inverse_ballot_w64(0xaaaaaaaaaaaaaaaaull), b1 = __builtin_amdgcn_inverse_ballot_w64(0xccccccccccccccccull);
  const float d01 = b0 ? mq[1] : mq[0], d23 = b0 ? mq[3] : mq[2], dg = b1 ? d23 : d01;
  const float sq = __int_as_float(__builtin_amdgcn_ds_bpermute(diag, __float_as_int(dg))), sum = ms[0];
  const float mu = sum * (1.0f / CH);
  const float var = fmaxf(__builtin_fmaf(-mu, mu, sq * (1.0f / CH)), 0.f);
  const float rs = GA * __builtin_amdgcn_rsqf(var + 1e-5f);
  const float off = -mu * rs;
#pragma unroll
  for (int c = 0; c < 3; c++) {
    u32x4 o;
#pragma unroll
    for (int i = 0; i < 4; i++) { f32x2 y = __builtin_elementwise_fma(f[4 * c + i], f32x2{rs, rs}, f32x2{off, off}); o[i] = pack_bf16(y.x, y.y); }
    *(LDS_AS u32x4*)(L + dst + c * dst_step) = o;
  }
}

DI void layer_norm_last(LDS_AS char* L, int lane) {
  const int cq = lane >> 2, q = lane & 3, oq = 4 * (lane >> 4) + (lane & 3);
  const u32x2 a = __builtin_bit_cast(u32x2, tr_read(L + L_V + (4 * oq + ((lane >> 2) & 3)) * V_ROW + 48 * 8));
  const u32x2 b = __builtin_bit_cast(u32x2, tr_read(L + L_V + (64 + 4 * (oq & 7) + ((lane >> 2) & 3)) * V_ROW + 48 * 8));
  f32x2 f[4] = {{bf_lo(a.x), bf_hi(a.x)}, {bf_lo(a.y), bf_hi(a.y)}, {bf_lo(b.x), bf_hi(b.x)}, {bf_lo(b.y), bf_hi(b.y)}};
  const float m = cq < 8 ? 1.f : 0.f;
  const f32x2 s = (f[0] + f[1]) + (f[2] + f[3]) * m;
  const f32x2 ss = __builtin_elementwise_fma(f[0], f[0], f[1] * f[1]) + __builtin_elementwise_fma(f[2], f[2], f[3] * f[3]) * m;
  const float sum = dpp_add<0x124>(dpp_add<0x128>(sum_xor32(sum_xor16(s.x + s.y))));
  const float sq = dpp_add<0x124>(dpp_add<0x128>(sum_xor32(sum_xor16(ss.x + ss.y))));
  const float mu = sum * (1.0f / CH);
  const float var = fmaxf(__builtin_fmaf(-mu, mu, sq * (1.0f / CH)), 0.f);
  const float rs = GA * __builtin_amdgcn_rsqf(var + 1e-5f);
  const float off = -mu * rs;
#pragma unroll
  for (int i = 0; i < 4; i++) f[i] = __builtin_elementwise_fma(f[i], f32x2{rs, rs}, f32x2{off, off});
  LDS_AS char* d = L + L_YR + ((cq >> 1) * 4 + q) * 16 + ((cq ^ (cq >> 1)) & 1) * 8;
  *(LDS_AS u32x2*)d = u32x2{pack_bf16(f[0].x, f[0].y), pack_bf16(f[1].x, f[1].y)};
  if (cq < 8) *(LDS_AS u32x2*)(d + 512) = u32x2{pack_bf16(f[2].x, f[2].y), pack_bf16(f[3].x, f[3].y)};
}

DI float sum_sq8(const u32* h) {
  float r;
  asm volatile("v_dot2_f32_bf16 %0, %1, %1, 0\n\tv_dot2_f32_bf16 %0, %2, %2, %0\n\tv_dot2_f32_bf16 %0, %3, %3, %0\n\tv_dot2_f32_bf16 %0, %4, %4, %0\n\t"
               "v_dot2_f32_bf16 %0, %5, %5, %0\n\tv_dot2_f32_bf16 %0, %6, %6, %0\n\tv_dot2_f32_bf16 %0, %7, %7, %0\n\tv_dot2_f32_bf16 %0, %8, %8, %0\n\ts_nop 2"
               : "=&v"(r) : "v"(h[0]), "v"(h[1]), "v"(h[2]), "v"(h[3]), "v"(h[4]), "v"(h[5]), "v"(h[6]), "v"(h[7]));
  return r;
}
DI float add1(float a, float b) { float r; asm("v_add_f32 %0, %1, %2" : "=v"(r) : "v"(a), "v"(b)); return r; }
DI void bar_only() { asm volatile("s_barrier" ::: "memory"); }

#define STAMP(i)

__global__ void __launch_bounds__(NT, 3)
slak_block_kernel(gcu16*, gcu16*, gcu16*, gcu16*, gcu16*, gcu16*, gu16*, gu64*) {
  u64x8 karg;
  asm volatile("s_load_dwordx16 %0, %1, 0x0" : "=s"(karg) : "s"(__builtin_amdgcn_kernarg_segment_ptr()));
  LDS_AS char* const L = (LDS_AS char*)0u;
  const int t = threadIdx.x, lane = t & 63, bid = blockIdx.x;
  const int w = __builtin_amdgcn_readfirstlane(t >> 6);
  STAMP(0);
  u32 warm = 0;
  if (t < CODE_LINES / 4)
    warm = *(const __attribute__((address_space(1))) u32*)((__builtin_amdgcn_s_getpc() & ~255ull) + (((bid >> 3) & 3) + 4 * t) * 128);
  SB();
  const int xcd = bid & 7, idx = bid >> 3;
  const int y0 = 14 * ((xcd >> 1) * 4 + (idx >> 3)), x0 = 14 * ((xcd & 1) * 8 + (idx & 7));

  const int cg = w >> 1, xh = w & 1;
  const u32 tq = ((u32)t * 2731) >> 16, c4 = t - tq * 24;
  u32 dwo[2], xo[4], xdst[2];
  {
    const u32 r0 = 2 * (tq & 3) - (tq >> 2);
#pragma unroll
    for (int h = 0; h < 2; h++) dwo[h] = r0 + h < 7u ? (r0 + h) * (7 * CH * 2) + c4 * 8 : 0x80000000u;
  }
  auto item_offsets = [&](int it) {
    const u32 item = t + it * NT, P = (item * 5462) >> 16, o = item - P * 12, Y = (P * 6554) >> 16, X = P - Y * 10;
    const u32 gy = y0 - 3 + 2 * Y, gx = (it == 0 || item < 1200) ? x0 - 3 + 2 * X : 1024u;
    u32 base = (u32)(__mul24((int)gy, IMG * CH * 2) + __mul24((int)gx, CH * 2)) + o * 16;
    PIN(base);
    const u32 b0 = gx < (u32)IMG ? base : 0x80000000u, b1 = gx + 1 < (u32)IMG ? base + CH * 2 : 0x80000000u;
    xdst[it] = L_X + ((Y >> 1) * 10 + X) * X_REC + o * 16 + (Y & 1) * 8;
#pragma unroll
    for (int k = 0; k < 4; k++) xo[k] = gy + (k >> 1) < (u32)IMG ? ((k & 1) ? b1 : b0) + (k >> 1) * (IMG * CH * 2) : 0x80000000u;
  };
  item_offsets(0);
  u32 pfo = (idx * 18 + lane) * 128;
  asm volatile("" : "+v"(dwo[0]), "+v"(dwo[1]), "+v"(xo[0]), "+v"(xo[1]), "+v"(xo[2]), "+v"(xo[3]), "+v"(pfo));
  SB();
  asm volatile("s_waitcnt lgkmcnt(0)" : "+s"(karg));
  gcu16* const x = (gcu16*)karg[0]; gcu16* const dw = (gcu16*)karg[1]; gcu16* const w1 = (gcu16*)karg[2]; gcu16* const w2 = (gcu16*)karg[3];
  gcu16* const grn_g = (gcu16*)karg[4]; gcu16* const grn_b = (gcu16*)karg[5]; gu16* const out = (gu16*)karg[6]; gu64* const ws = (gu64*)karg[7];
  const rsrc_t xrs = make_rsrc(x, X_BYTES);
  u32x2 dv[2][7];
  if (w < 3) {
    const rsrc_t drs = make_rsrc(dw, 49 * CH * 2);
#pragma unroll
    for (int i = 0; i < 14; i++) dv[i / 7][i % 7] = __builtin_amdgcn_raw_buffer_load_b64(drs, dwo[i / 7] + (i % 7) * (CH * 2), 0, LD_AUX);
  }
  u32x4 xin[2][4];
  auto load_item = [&](int it) {
#pragma unroll
    for (int k = 0; k < 4; k++) xin[it][k] = ld128(xrs, xo[k]);
  };
  auto store_item = [&](int it) {
#pragma unroll
    for (int e = 0; e < 8; e++) {
      const u32x4* v = xin[it];
      *(volatile LDS_AS u32x2*)(L + xdst[it] + e * 192) = (e & 1) ? u32x2{perm_hi(v[0][e >> 1], v[1][e >> 1]), perm_hi(v[2][e >> 1], v[3][e >> 1])}
                                                                  : u32x2{perm_lo(v[0][e >> 1], v[1][e >> 1]), perm_lo(v[2][e >> 1], v[3][e >> 1])};
    }
  };
  load_item(0);
  if (FE_WARM && w == 1 && lane < 36) warm = *(const __attribute__((address_space(1))) u32*)((gchar*)(lane < 18 ? w1 : w2 - 18 * 64) + pfo);
  SB();
  if (w < 7) item_offsets(1);
  const u32x4 w1rs = {(u32)(u64)w1, (u32)((u64)w1 >> 32) & 0xffffu, CH * HID * 2, 0x00020000};
  const u32 w1d = (w / 6) * 3 * 6144 + (w % 6) * 1024;
  const u32 k1 = 16 * (w % 6) + (lane >> 2);
  u32 w1o = __umul24(pos_channel(k1 ^ (((k1 >> 3) & 1) << 2)), HID * 2) + (w / 6) * 384 + (lane & 3) * 16;
  u32 tp = L_T + (16 * cg + (cg >= 3 ? 4 : 0) + (lane >> 2) + 100 * (lane & 3)) * T_ROW;
  u32 xb = L_X + (4 * xh + (lane & 3)) * X_REC + (16 * cg + (lane >> 2)) * 16, xb2 = xb + 40 * X_REC;
  u32 vb = 4 * xh + (lane & 3) < 7 ? L_V + (16 * cg + (lane >> 2)) * V_ROW + (4 * xh + (lane & 3)) * 8 : L_HREM + lane * 8;
  asm volatile("" : "+v"(w1o), "+v"(tp), "+v"(xb), "+v"(xb2), "+v"(vb));
  const int T = w >> 1, Hh = w & 1, g4 = lane >> 4, q4 = (lane >> 2) & 3, p4 = lane & 3;
  enum { A_LN0, A_LN1, A_YD, A_DIAG, A_WB, A_WE, A_Y, A_SW, A_SR, A_WR, A_YR, A_W2V, A_OOFF, A_HR, A_N };
  u32 adr[A_N];
  auto make_adr = [&](int i) {
    u32 v;
    switch (i) {
      case A_LN0: v = L_V + (8 * g4 + 4 * (g4 & 1) + q4) * V_ROW + (4 * w + p4) * 8; break;
      case A_LN1: v = (g4 & 1) ? adr[A_LN0] - 4 * V_ROW : adr[A_LN0] + 4 * V_ROW; break;
      case A_YD: v = L_Y + T * 6144 + (g4 * 32 + 16 * Hh + (lane & 15)) * 16; break;
      case A_DIAG: v = ((lane & 15) + (lane & 12) * 4) * 4; break;
      case A_WB: v = (8 * (g4 >> 1) + q4) * 64 + (g4 & 1) * 32 + p4 * 8 + Hh * 3 * 6144; break;
      case A_WE: v = adr[A_WB] + L_W1E; break;
      case A_Y: v = L_Y + T * 6144 + (lane ^ (16 * Hh)) * 16; break;
      case A_SW: v = L_SCR + w * SCR_W + (lane & 31) * 72 + (lane >> 5) * 8; break;
      case A_SR: v = L_SCR + w * SCR_W + (8 * g4 + q4) * 72 + p4 * 8; break;
      case A_WR: v = (T < 3 ? L_W1E : L_W1L) + (Hh * 3 + T % 3) * 6144 + (8 * g4 + q4) * 64 + p4 * 8; break;
      case A_YR: v = L_YR + (g4 * 4 + p4) * 16; break;
      case A_W2V: v = (32 * w + 4 * ((lane >> 2) & 7) + (lane >> 5)) * (CH * 2) + p4 * 16; break;
      case A_OOFF: {
        const u32 pi = 4 * w + q4, py = (pi * 37) >> 8;
        v = (u32)__mul24((y0 + 2 * py + (p4 >> 1)) * IMG + x0 + 2 * (pi - 7 * py) + (p4 & 1), CH * 2) + g4 * 16;
      } break;
      default: v = L_HREM + (Hh * 24 + T * 4 + (lane >> 3)) * 64 + (lane & 7) * 2; break;
    }
    PIN(v);
    adr[i] = v;
  };
#pragma unroll
  for (int i = 0; i < A_N; i++) make_adr(i);
  SB();
  if (w < 3) {
#pragma unroll
    for (int e = 0; e < 4; e++) {
      u32 q[2][2][4];
#pragma unroll
      for (int h = 0; h < 2; h++) {
        u32 v[7];
#pragma unroll
        for (int j = 0; j < 7; j++) v[j] = dv[h][j][e >> 1];
#pragma unroll
        for (int sx = 0; sx < 3; sx++) {
          q[0][h][sx] = (e & 1) ? perm_hi(v[2 * sx], v[2 * sx + 1]) : perm_lo(v[2 * sx], v[2 * sx + 1]);
          q[1][h][sx + 1] = (e & 1) ? perm_hi(v[2 * sx + 1], v[2 * sx + 2]) : perm_lo(v[2 * sx + 1], v[2 * sx + 2]);
        }
        q[0][h][3] = (e & 1) ? v[6] >> 16 : v[6] & 0xffffu;
        q[1][h][0] = (e & 1) ? v[0] & 0xffff0000u : v[0] << 16;
      }
      LDS_AS char* d = L + L_T + (52 * (c4 & 1) + 12 * e + (c4 >> 1) + 200 * (tq >> 2)) * T_ROW + (tq & 3) * 32;
#pragma unroll
      for (int ox = 0; ox < 2; ox++) {
        *(LDS_AS u32x4*)(d + ox * 100 * T_ROW) = u32x4{q[ox][0][0], q[ox][1][0], q[ox][0][1], q[ox][1][1]};
        *(LDS_AS u32x4*)(d + ox * 100 * T_ROW + 16) = u32x4{q[ox][0][2], q[ox][1][2], q[ox][0][3], q[ox][1][3]};
      }
    }
  }
  if (t == NT - 1) *(LDS_AS u32x4*)(L + L_CNT) = u32x4{0, 0, 0, 0};
  STAMP(1);
  asm volatile("" : "+v"(xin[0][0]), "+v"(xin[0][1]), "+v"(xin[0][2]), "+v"(xin[0][3]));
  if (w < 7) load_item(1);
  SB();
  store_item(0);
  STAMP(2);
  bar_lds();
  STAMP(3);

  u64 epoch;
  {
    s16x8 wq[8], d[4][5];
    auto load_x = [&](int sx, int yp) {
      d[sx][yp] = yp < 4 ? *(volatile LDS_AS s16x8*)(L + xb + (yp * 10 + sx) * X_REC) : *(volatile LDS_AS s16x8*)(L + xb2 + sx * X_REC);
    };
    auto load_w = [&](int i) { wq[i] = *(volatile LDS_AS s16x8*)(L + tp + i * 16); };
    auto half = [](s16x8 v, int h) { return h ? __builtin_shufflevector(v, v, 4, 5, 6, 7) : __builtin_shufflevector(v, v, 0, 1, 2, 3); };
    auto conv_mfma = [&](f32x4& acc, int sx, int sy, int Yo) {
      acc = __builtin_amdgcn_mfma_f32_4x4x4bf16_1k(half(wq[2 * sy + (sx >> 1)], sx & 1), half(d[sx][(Yo + sy) >> 1], (Yo + sy) & 1), acc, 0, 0, 0);
    };
    auto put_row = [&](const f32x4& r, int Yo) { *(LDS_AS u32x2*)(L + vb + Yo * 56) = u32x2{pack_bf16(r[0], r[1]), pack_bf16(r[2], r[3])}; };
    load_w(0); load_x(0, 0); load_x(0, 1); load_w(2); load_w(4); load_x(0, 2); load_w(6);
    f32x4 acc[7];
#pragma unroll
    for (int i = 0; i < 7; i++) acc[i] = f32x4{0.f, 0.f, 0.f, 0.f};
    SB();
    static_for<16>([&](auto ic) {
      constexpr int i = ic.value, sx = i >> 2, sy = i & 3;
#pragma unroll
      for (int Yo = 0; Yo + sy < 6; Yo++) conv_mfma(acc[Yo], sx, sy, Yo);
      if constexpr (i < 9) load_x(1 + i / 3, i % 3);
      if constexpr (i < 4) load_w(2 * i + 1);
      if constexpr (i == 8) { if (w >= 7) arrive(2); }
      SB();
    });
    if (w < 7) {
      if (t < 432) store_item(1);
      arrive(2);
    }
    await(2);
    STAMP(4);
#pragma unroll
    for (int i = 0; i < 8; i++) load_x(i >> 1, 3 + (i & 1));
    epoch = ws_load(ws, WS_EPOCH);
#pragma unroll
    for (int Yo = 0; Yo < 3; Yo++) put_row(acc[Yo], Yo);
    SB();
    static_for<40>([&](auto ic) {
      constexpr int i = ic.value, sx = i / 10, j = i % 10;
      constexpr int yo[10] = {6, 5, 4, 6, 3, 5, 6, 4, 5, 6}, sy[10] = {0, 1, 2, 1, 3, 2, 2, 3, 3, 3};
      conv_mfma(acc[yo[j]], sx, sy[j], yo[j]);
      if constexpr (i == 4 || i == 12 || i == 20) dma16(w1rs, w1o, (i / 8) * 64, L_W1E + w1d + (i / 8) * 6144);
      if constexpr (i == 37) put_row(acc[3], 3);
      SB();
    });
#pragma unroll
    for (int Yo = 4; Yo < 7; Yo++) put_row(acc[Yo], Yo);
  }
  bar_lds();
  STAMP(5);

  const bool bias_wave = w == 4 && bid < CH;
  u32 bw[6], bb[6];
  if (bias_wave) {
#pragma unroll
    for (int i = 0; i < 6; i++) {
      bw[i] = __builtin_amdgcn_raw_buffer_load_b16(make_rsrc(w2, HID * CH * 2), lane * (CH * 2), bid * 2 + i * (64 * CH * 2), 0);
      bb[i] = __builtin_amdgcn_raw_buffer_load_b16(make_rsrc(grn_b, HID * 2), lane * 2 + i * 128, 0, 0);
    }
  }
  SB();
  layer_norm_unit(L, adr[A_LN0], adr[A_LN1], adr[A_YD], adr[A_DIAG]);
  if (w == LN_LAST) layer_norm_last(L, lane);
  asm volatile("s_waitcnt vmcnt(0)" :: "v"(warm) : "memory");
  epoch = ((u64)(u32)__builtin_amdgcn_readfirstlane((int)(epoch >> 32)) << 32) | (u32)__builtin_amdgcn_readfirstlane((int)epoch);
  const u64 tag = (epoch + 1) & ((1ull << 40) - 1);
  float bias_val = 0.f;
  if (bias_wave) {
    float a = 0.f;
#pragma unroll
    for (int i = 0; i < 6; i++) { asm volatile("" : "+v"(bw[i]), "+v"(bb[i])); a = __builtin_fmaf(bf_lo(bw[i]), bf_lo(bb[i]), a); }
    bias_val = __int_as_float(__builtin_amdgcn_readfirstlane(__float_as_int(wave_sum(a))));
  }
  bar_lds();
  STAMP(6);

  s16x8 hk[6], hs[6];
  const int rq = T / 3, otj[3] = {T % 3, (T + 1) % 3, (T + 2) % 3};
  const u32 totw = WS_TOT + (bid & 7) * TOT_ROW;
  const u32 ooff = adr[A_OOFF];
  u32x4 xr[3];
  u64 bgr;
  const f32x16 zero16 = {0.f, 0.f, 0.f, 0.f, 0.f, 0.f, 0.f, 0.f, 0.f, 0.f, 0.f, 0.f, 0.f, 0.f, 0.f, 0.f};
  {
    LDS_AS char* wE = L + adr[A_WE];
    LDS_AS char* wL = L + adr[A_WB] + L_W1L;
    LDS_AS char* sw = L + adr[A_SW];
    LDS_AS char* sr = L + adr[A_SR];
    LDS_AS char* wr = L + adr[A_WR];
    LDS_AS char* yr = L + adr[A_YR];
    s16x8 yf[6], wf[6], ry[3], rw[3];
    f32x4 racc[2] = {{0.f, 0.f, 0.f, 0.f}, {0.f, 0.f, 0.f, 0.f}};
    float ssv[6];
    f32x16 acc = zero16;
    __builtin_amdgcn_s_setprio(3);
#pragma unroll
    for (int k = 0; k < 6; k++) { yf[k] = *(LDS_AS s16x8*)(L + adr[A_Y] + k * 1024); wf[k] = tr_frag(wE + k * 1024, 256); }
    SB();
#pragma unroll
    for (int u = 0; u < 3; u++) dma16(w1rs, w1o, 192 + u * 64, L_W1L + w1d + u * 6144);
    SB();
#pragma unroll
    for (int k = 0; k < 6; k++) {
      acc = mfma32(yf[k], wf[k], acc);
      wf[k] = tr_frag(wE + 6144 + k * 1024, 256);
      SB();
    }
    const u32 w2raw = wave_slot(w);
    auto fetch_w2 = [&](int c) {
      await(c);
      const u32x4 rs = {(u32)(u64)w2, (u32)((u64)w2 >> 32) & 0xffffu, HID * CH * 2, 0x00020000};
#pragma unroll
      for (int j = 0; j < 6; j++) dma16(rs, adr[A_W2V], (j & 1) * 384 + (j >> 1) * 64, w2raw + j * 1024);
    };
    asm volatile("s_waitcnt vmcnt(0)" ::: "memory");
    arrive(0);
    static_for<6>([&](auto sc_) {
      constexpr int s = sc_.value, NP = s == 3 ? 10 : 8;
      LDS_AS char* wp2 = (s + 2 < 3 ? wE : wL) + ((s + 2) % 3) * 6144;
      __builtin_amdgcn_s_setprio((G1_PRIO >> (2 * s)) & 3);
      if constexpr (s == 0) { if (bias_wave && lane < 8) ws_store(ws, WS_TOT + lane * TOT_ROW + TOT_BIAS + bid, granule(tag, bias_val)); }
      if constexpr (s == 3) arrive(1);
      if constexpr (s == 1) { if (w < 6) fetch_w2(0); else await(0); }
      if constexpr (s == 4) { if (w >= 6) fetch_w2(1); }
      if constexpr (s == 1 || s == 2) {
#pragma unroll
        for (int j = 0; j < 3; j++) {
          if (s == 1) ry[j] = *(LDS_AS s16x8*)(yr + j * 256);
          rw[j] = tr_frag(wr + j * 2048 + (s - 1) * 32, 256);
        }
      }
      f32x2 xv[NP], z[NP];
#pragma unroll
      for (int i = 0; i < NP; i++) {
        if (i < 8) xv[i] = f32x2{acc[2 * (i & 7)], acc[2 * (i & 7) + 1]};
        else xv[i] = __builtin_amdgcn_inverse_ballot_w64(0xffffull) ? f32x2{racc[0][2 * (i & 1)], racc[0][2 * (i & 1) + 1]} : f32x2{racc[1][2 * (i & 1)], racc[1][2 * (i & 1) + 1]};
        z[i] = __builtin_elementwise_fma(xv[i], xv[i], f32x2{GK, GK});
      }
#pragma unroll
      for (int i = 0; i < NP; i++) PIN(z[i]);
      SB();
#pragma unroll
      for (int i = 0; i < NP; i++) z[i] = __builtin_elementwise_fma(-z[i], xv[i], f32x2{-GS, -GS});
#pragma unroll
      for (int i = 0; i < NP; i++) PIN(z[i]);
      SB();
      f32x16 nxt = zero16;
      float e[2 * NP];
#pragma unroll
      for (int i = 0; i < 2 * NP; i++) {
        constexpr bool rem = s == 1 || s == 2;
        if (s < 5 && ((i % 3 == 0 && i < 12) || i == (rem ? 14 : 12))) { nxt = mfma32(yf[i / 3], wf[i / 3], nxt); PIN(nxt); SB(); }
        if (rem && (i == 12 || i == 13)) { racc[(s - 1) & 1] = mfma16(ry[i - 12], rw[i - 12], racc[(s - 1) & 1]); PIN(racc[(s - 1) & 1]); SB(); }
        if (s == 5 && i % 3 == 0 && i < 9) { xr[i / 3] = __builtin_amdgcn_raw_buffer_load_b128(xrs, ooff, otj[i / 3] * 64, LD_AUX); SB(); }
        if (s == 5 && i == 9) { bgr = ws_load(ws + (totw + TOT_BIAS + (w & 1) * 64), lane); SB(); }
        e[i] = __builtin_amdgcn_fmed3f(__builtin_amdgcn_exp2f(z[i >> 1][i & 1]), 0.f, 1.f);
        PIN(e[i]);
      }
      SB();
      if constexpr (s < 4) {
#pragma unroll
        for (int k = 0; k < 5; k++) wf[k] = tr_frag(wp2 + k * 1024, 256);
      }
#pragma unroll
      for (int i = 0; i < NP; i++) z[i] = f32x2{e[2 * i], e[2 * i + 1]} + GC;
#pragma unroll
      for (int i = 0; i < NP; i++) PIN(z[i]);
      SB();
      f32x2 pp[NP / 2], rr[2];
#pragma unroll
      for (int i = 0; i < NP / 2; i++) pp[i] = z[2 * i] * z[2 * i + 1];
      xv[0] *= z[1]; xv[1] *= z[0];
#pragma unroll
      for (int i = 0; i < NP / 2; i++) PIN(pp[i]);
      PIN(xv[0]); PIN(xv[1]);
      SB();
      rr[0] = pp[0] * pp[1]; rr[1] = pp[2] * pp[3];
      PIN(rr[0]); PIN(rr[1]);
      SB();
#pragma unroll
      for (int i = 2; i < NP; i++) xv[i] *= z[i ^ 1];
#pragma unroll
      for (int i = 2; i < NP; i++) PIN(xv[i]);
      SB();
      if constexpr (s < 5) { nxt = mfma32(yf[5], wf[5], nxt); PIN(nxt); SB(); }
      float rc[6];
#pragma unroll
      for (int i = 0; i < (NP == 10 ? 6 : 4); i++) {
        if ((s == 1 || s == 2) && i == 3) { racc[(s - 1) & 1] = mfma16(ry[2], rw[2], racc[(s - 1) & 1]); PIN(racc[(s - 1) & 1]); SB(); }
        rc[i] = __builtin_amdgcn_rcpf(i < 4 ? rr[(i >> 1) & 1][i & 1] : pp[NP / 2 - 1][i & 1]);
        PIN(rc[i]);
      }
      SB();
      if constexpr (s < 4) wf[5] = tr_frag(wp2 + 5 * 1024, 256);
      f32x2 ip[NP / 2];
#pragma unroll
      for (int i = 0; i < 4; i++) ip[i] = f32x2{rc[i & 2], rc[(i & 2) + 1]} * pp[i ^ 1];
      if constexpr (NP == 10) ip[4] = f32x2{rc[4], rc[5]};
#pragma unroll
      for (int i = 0; i < 4; i++) PIN(ip[i]);
      SB();
#pragma unroll
      for (int i = 0; i < NP; i++) xv[i] *= ip[i >> 1];
#pragma unroll
      for (int i = 0; i < NP; i++) PIN(xv[i]);
      SB();
      u32 hp8[8];
#pragma unroll
      for (int i = 0; i < 8; i++) { hp8[i] = pack_bf16(xv[i].x, xv[i].y); PIN(hp8[i]); }
      SB();
#pragma unroll
      for (int Q = 0; Q < 4; Q++) *(volatile LDS_AS u32x2*)(sw + Q * 16) = u32x2{hp8[2 * Q], hp8[2 * Q + 1]};
      SB();
      ssv[s] = sum_sq8(hp8);
      hk[s] = tr_frag(sr, 4 * 72);
      hs[s] = tr_frag(sr + 32, 4 * 72);
      if constexpr (s == 3) {
        const f32x2 sq = __builtin_elementwise_fma(xv[8], xv[8], xv[9] * xv[9]);
        if (lane < 32) {
          const u32 p01 = pack_bf16(xv[8].x, xv[8].y), p23 = pack_bf16(xv[9].x, xv[9].y);
          LDS_AS unsigned short* hr = (LDS_AS unsigned short*)(L + adr[A_HR]);
          hr[0] = (unsigned short)p01; hr[8] = (unsigned short)(p01 >> 16); hr[16] = (unsigned short)p23; hr[24] = (unsigned short)(p23 >> 16);
          *(LDS_AS float*)(sw + 64) = sq.x + sq.y;
        }
      }
      acc = nxt;
      SB();
    });
#pragma unroll
    for (int i = 0; i < 3; i++) *(volatile LDS_AS f32x2*)(sw + i * 16) = f32x2{ssv[2 * i], ssv[2 * i + 1]};
  }
  asm volatile("" : "+v"(xr[0]), "+v"(xr[1]), "+v"(xr[2]), "+v"(bgr));
  bar_all();
  STAMP(7);

  if (t < HID) {
    float v[13];
    LDS_AS char* q = L + L_SCR + (w / 3) * SCR_W + (lane & 31) * 72;
#pragma unroll
    for (int i = 0; i < 12; i++) v[i] = *(LDS_AS float*)(q + (w % 3) * 16 + (lane >> 5) * 4 + (i >> 1) * 2 * SCR_W + (i & 1) * 8);
    v[12] = *(LDS_AS float*)(q + (2 * (w % 3) + (lane >> 5)) * 2 * SCR_W + 64);
    const float s = (((v[0] + v[1]) + (v[2] + v[3])) + ((v[4] + v[5]) + (v[6] + v[7]))) + (((v[8] + v[9]) + (v[10] + v[11])) + v[12]);
    ws_store(ws, WS_PART + (((u32)t >> 4) * 256 + bid) * 16 + (t & 15), granule(tag, s));
  }
  u32x2 w2r[12];
  u32 ap[4], ab[2], hp, rw, w2d0, w2d1;
  f32x2 w2f[24];
  auto unpack_w2 = [&](int i) {
    const int ot = i >> 3, r = (i >> 1) & 3, kp = i & 1;
    const u32 a = w2r[4 * ot + 2 * kp][r >> 1], b = w2r[4 * ot + 2 * kp + 1][r >> 1];
    w2f[i] = (r & 1) ? f32x2{bf_hi(a), bf_hi(b)} : f32x2{bf_lo(a), bf_lo(b)};
    PIN(w2f[i]);
  };
  auto swap_out = [&]() {
    SB();
#pragma unroll
    for (int i = 0; i < 12; i++) w2r[i] = *(volatile LDS_AS u32x2*)(L + wave_slot(w) + lane * 8 + i * 512);
#pragma unroll
    for (int i = 0; i < 6; i++) *(volatile LDS_AS s16x8*)(L + wave_slot(w) + lane * 16 + i * 1024) = hs[i];
    if (t == 512) *(LDS_AS u32*)(L + L_FL) = 0;
    SB();
    const u32 w2d = L_W2 + ((t & 1) * 12 + w) * 1024 + (16 * g4 + ((t >> 1) & 3)) * 16 + (t & 8);
    const u32 aslot = L_W2 + (16 * g4 + q4) * 16;
    ab[0] = aslot + p4 * 64; ab[1] = aslot + (p4 ^ 1) * 64 + 12288;
#pragma unroll
    for (int i = 0; i < 4; i++) ap[i] = (((i >> 1) ^ rq) ? ab[1] : ab[0]) + (otj[0] * 24576 + ((i & 1) ^ Hh) * 6144);
    hp = L_HREM + (Hh * 24 + g4) * 64 + p4 * 16;
    rw = L_RP + (Hh * 4 * CH + 32 * otj[0] + 4 * rq) * 4 + p4 * (CH * 4) + g4 * 32;
    w2d0 = w2d + (t & 1) * 64; w2d1 = w2d + ((t & 1) ^ 1) * 64;
    asm volatile("" : "+v"(ap[0]), "+v"(ap[1]), "+v"(ap[2]), "+v"(ap[3]), "+v"(ab[0]), "+v"(ab[1]), "+v"(hp), "+v"(rw), "+v"(w2d0), "+v"(w2d1));
#pragma unroll
    for (int i = 0; i < W2_EARLY; i++) unpack_w2(i);
    SB();
  };
  {
    const bool mine = (u32)(t - HID) < CH;
    if (!__all(!mine || (bgr >> 24) == tag)) {
#pragma unroll 1
      for (int spin = 0; spin < SPIN_CAP; spin++) {
        bgr = ws_load(ws + (totw + TOT_BIAS + (w & 1) * 64), lane);
        if (__all(!mine || (bgr >> 24) == tag)) break;
      }
    }
    if (mine) *(LDS_AS float*)(L + L_BP + (t - HID) * 4) = granule_val(bgr);
  }
  if (bid < 24) {
    u64 gr[6];
    const u32 partw = WS_PART + bid * 4096 + t;
#pragma unroll
    for (int i = 0; i < 6; i++) gr[i] = ws_load(ws, partw + (i < 5 || t < 256 ? i * NT : 0));
    swap_out();
#pragma unroll 1
    for (int spin = 0; spin < SPIN_CAP; spin++) {
      bool ok = true;
#pragma unroll
      for (int i = 0; i < 6; i++) ok &= (gr[i] >> 24) == tag;
      if (__all(ok)) break;
#pragma unroll
      for (int i = 0; i < 6; i++)
        if ((gr[i] >> 24) != tag) gr[i] = ws_load(ws, partw + (i < 5 || t < 256 ? i * NT : 0));
    }
    float s = ((granule_val(gr[0]) + granule_val(gr[1])) + (granule_val(gr[2]) + granule_val(gr[3]))) + (granule_val(gr[4]) + (t < 256 ? granule_val(gr[5]) : 0.f));
    s = sum_xor32(sum_xor16(s));
    if (lane < 16) *(LDS_AS float*)(L + L_RED + (lane * 12 + w) * 4) = s;
  } else {
    swap_out();
  }
  bar_lds();
  if (bid < 24 && w < 2) {
    LDS_AS char* q = L + L_RED + (t & 15) * 48;
    const f32x4 a = *(LDS_AS f32x4*)(q) + *(LDS_AS f32x4*)(q + 16) + *(LDS_AS f32x4*)(q + 32);
    const float gx = __builtin_amdgcn_sqrtf(__builtin_fmaf((a[0] + a[1]) + (a[2] + a[3]), GA_INV2, 1e-6f));
    ws_store(ws, WS_TOT + (t >> 4) * TOT_ROW + bid * 16 + (t & 15), granule(tag, gx));
  }
  SB();
#pragma unroll
  for (int i = 0; i < 6; i++) hs[i] = *(LDS_AS s16x8*)(L + wave_slot(w ^ 1) + lane * 16 + i * 1024);
  const u32 remo = lane < 48 ? (u32)(((y0 + 12) * IMG + x0 + 12) * (CH * 2)) + (lane >= 24 ? IMG * CH * 2 - 384 : 0) + lane * 16 : 0x80000000u;
  u32x4 xrem = ld128(xrs, remo);
  u32 gam = grn_g[t < HID ? t : 0];
  f32x4 acc2[3][2];
#pragma unroll
  for (int i = 0; i < 6; i++) acc2[i >> 1][i & 1] = *(LDS_AS f32x4*)(L + L_BP + g4 * 32 + otj[i >> 1] * 128 + ((i & 1) ^ (i < 2 ? rq : 0)) * 16);
  SB();
#pragma unroll
  for (int i = 0; i < 6; i++) {
    const u32x4 xv = i < 2 && rq ? u32x4{xr[0][2], xr[0][3], xr[0][0], xr[0][1]} : xr[i >> 1];
    acc2[i >> 1][i & 1] += f32x4{bf_lo(xv[2 * (i & 1)]), bf_hi(xv[2 * (i & 1)]), bf_lo(xv[2 * (i & 1) + 1]), bf_hi(xv[2 * (i & 1) + 1])};
    asm volatile("" : "+v"(acc2[i >> 1][i & 1]));
  }
  SB();
  const f32x4 zero4 = {0.f, 0.f, 0.f, 0.f};
#pragma unroll
  for (int i = W2_EARLY; i < 24; i++) unpack_w2(i);
#pragma unroll
  for (int i = 0; i < 6; i++) asm volatile("" : "+v"(hs[i]));
  STAMP(8);
  if (t >= HID) PIN(xrem);

  if (t < HID) {
    u64 g;
    if (POLL_DEPTH == 1) {
#pragma unroll 1
      for (int spin = 0; spin < SPIN_CAP; spin++) {
        g = ws_load(ws, totw + t);
        if (__all((g >> 24) == tag)) break;
      }
    } else {
      u64 ga = ws_load(ws, totw + t);
      __builtin_amdgcn_s_sleep(POLL_SLEEP);
      u64 gb = ws_load(ws, totw + t);
#pragma unroll 1
      for (int spin = 0; spin < SPIN_CAP; spin++) {
        g = ga;
        if (__all((g >> 24) == tag)) break;
        ga = ws_load(ws, totw + t);
        g = gb;
        if (__all((g >> 24) == tag)) break;
        gb = ws_load(ws, totw + t);
      }
    }
    PIN(xrem);
    const float gx = granule_val(g);
    *(LDS_AS float*)(L + L_GG + t * 4) = bf_lo(gam) * gx;
    const float s = wave_sum(gx);
    if (lane == 0) *(LDS_AS float*)(L + L_PS + w * 4) = s;
  }
  bar_lds();
  STAMP(9);
  {
    const f32x4 pa = *(LDS_AS f32x4*)(L + L_PS);
    const f32x2 pb = *(LDS_AS f32x2*)(L + L_PS + 16);
    const f32x4 gg = *(LDS_AS f32x4*)(L + L_GG + (t >> 3) * 16);
    if (bid == 0 && t == NT - 1) ws_store(ws, WS_EPOCH, epoch + 1);
    const float mean = (((pa[0] + pa[1]) + (pa[2] + pa[3])) + (pb[0] + pb[1])) * (1.0f / HID);
    const float inv = GA_INV * __builtin_amdgcn_rcpf(mean + 1e-6f);
    const f32x2 s01 = f32x2{gg[0], gg[1]} * inv + GA_INV, s23 = f32x2{gg[2], gg[3]} * inv + GA_INV;
#pragma unroll
    for (int i = 0; i < 12; i++) {
      const f32x2 a = w2f[2 * i] * s01, b = w2f[2 * i + 1] * s23;
      *(volatile LDS_AS u32x2*)(L + (i & 1 ? w2d1 : w2d0) + ((i >> 1) & 1) * 128 + (i >> 2) * 24576) = u32x2{pack_bf16(a.x, a.y), pack_bf16(b.x, b.y)};
    }
  }

  {
    const rsrc_t ors = make_rsrc(out, X_BYTES);
    constexpr int PF = G2_PF;
    s16x8 af[72], rb[6];
    f32x4 ra = zero4;
    u32 rem_cnt = 0;
    auto load_a = [&](int i) {
      if (i % 24 == 0 && i) {
#pragma unroll
        for (int j = 0; j < 4; j++) ap[j] = ab[j >> 1] + (otj[i / 24] * 24576 + ((j & 1) ^ Hh) * 6144);
      }
      af[i] = *(volatile LDS_AS s16x8*)(L + ap[(i / 6) & 3] + (i % 6) * 1024);
    };
    auto store_tile = [&](int j) {
      const u32x2 a = {pack_bf16(acc2[j][0][0], acc2[j][0][1]), pack_bf16(acc2[j][0][2], acc2[j][0][3])};
      const u32x2 b = {pack_bf16(acc2[j][1][0], acc2[j][1][1]), pack_bf16(acc2[j][1][2], acc2[j][1][3])};
      __builtin_amdgcn_raw_buffer_store_b128(j == 0 && rq ? u32x4{b.x, b.y, a.x, a.y} : u32x4{a.x, a.y, b.x, b.y}, ors, ooff, otj[j] * 64, ST_AUX);
    };
    bar_lds();
    STAMP(10);
    if (G2_PRIO) __builtin_amdgcn_s_setprio(3);
#pragma unroll
    for (int i = 0; i < PF; i++) load_a(i);
    SB();
    static_for<72>([&](auto ic) {
      constexpr int i = ic.value, ot = i / 24, rt = (i / 12) & 1, k = i % 12;
      acc2[ot][rt] = mfma16(af[i], k < 6 ? hk[k % 6] : hs[k % 6], acc2[ot][rt]);
      if constexpr (i + PF < 72) load_a(i + PF);
      if constexpr (G2_PRIO && i % 18 == 17 && i < 60) __builtin_amdgcn_s_setprio(2 - i / 18);
      if constexpr (i < 12 && (i & 1)) rb[i >> 1] = *(volatile LDS_AS s16x8*)(L + hp + (i >> 1) * 256);
      if constexpr (i >= 6 && i < 18 && (i & 1) == 0) ra = mfma16(af[(i - 6) >> 1], rb[(i - 6) >> 1], ra);
      if constexpr (i == 19) {
        if (q4 == 0) *(volatile LDS_AS f32x4*)(L + rw) = ra;
        u32 fl = L_FL;
        PIN(fl);
        if (lane == 0) rem_cnt = __hip_atomic_fetch_add((LDS_AS u32*)(L + fl), 1u, __ATOMIC_RELAXED, __HIP_MEMORY_SCOPE_WORKGROUP);
      }
      if constexpr (i == 25 || i == 49) store_tile(ot - 1);
      if constexpr (i == 23) { STAMP(11); }
      if constexpr (i == 47) { STAMP(12); }
      if constexpr (i == 40) {
        if (__builtin_amdgcn_readfirstlane(rem_cnt) == 11 && lane < 48) {
          f32x4 xq[2];
#pragma unroll
          for (int j = 0; j < 2; j++) {
            const f32x4 a = *(volatile LDS_AS f32x4*)(L + L_RP + (lane * 8 + j * 4) * 4), b = *(volatile LDS_AS f32x4*)(L + L_RP + (4 * CH + lane * 8 + j * 4) * 4);
            const f32x4 c = *(volatile LDS_AS f32x4*)(L + L_BP + (lane - 12 * ((lane * 43) >> 9)) * 32 + j * 16);
#pragma unroll
            for (int e = 0; e < 4; e++) xq[j][e] = add1(add1((e & 1) ? bf_hi(xrem[2 * j + (e >> 1)]) : bf_lo(xrem[2 * j + (e >> 1)]), c[e]), add1(a[e], b[e]));
          }
          __builtin_amdgcn_raw_buffer_store_b128(u32x4{pack_bf16(xq[0][0], xq[0][1]), pack_bf16(xq[0][2], xq[0][3]), pack_bf16(xq[1][0], xq[1][1]), pack_bf16(xq[1][2], xq[1][3])}, ors, remo, 0, ST_AUX);
        }
      }
      SB();
    });
    STAMP(13);
    store_tile(2);
  }
  STAMP(14);
}

extern "C" void kernel_launch(void* const* d_in, const int* in_sizes, int n_in,
                              void* d_out, int out_size, void* d_ws, size_t ws_size,
                              hipStream_t stream) {
  (void)in_sizes; (void)n_in; (void)out_size; (void)ws_size;
  (void)hipFuncSetAttribute((const void*)slak_block_kernel, hipFuncAttributeMaxDynamicSharedMemorySize, LDS_BYTES);
  slak_block_kernel<<<256, NT, LDS_BYTES, stream>>>(
      (gcu16*)d_in[0], (gcu16*)d_in[1], (gcu16*)d_in[2], (gcu16*)d_in[3], (gcu16*)d_in[4], (gcu16*)d_in[5], (gu16*)d_out, (gu64*)d_ws);
}
```

```cpp
#include <hip/hip_runtime.h>
#include <stdint.h>

typedef unsigned long long u64;
typedef unsigned u32;
typedef float f32x2 __attribute__((ext_vector_type(2)));
typedef float f32x4 __attribute__((ext_vector_type(4)));
typedef float f32x16 __attribute__((ext_vector_type(16)));
typedef short s16x4 __attribute__((ext_vector_type(4)));
typedef short s16x8 __attribute__((ext_vector_type(8)));
typedef __bf16 bf16x2 __attribute__((ext_vector_type(2)));
typedef u32 u32x2 __attribute__((ext_vector_type(2)));
typedef u32 u32x4 __attribute__((ext_vector_type(4)));
typedef u64 u64x8 __attribute__((ext_vector_type(8)));
#define LDS_AS __attribute__((address_space(3)))
typedef __attribute__((address_space(1))) u64 gu64;
typedef __attribute__((address_space(1))) unsigned short gu16;
typedef __attribute__((address_space(1))) const unsigned short gcu16;
#define RLX_AGENT __ATOMIC_RELAXED, __HIP_MEMORY_SCOPE_AGENT
#define DI __device__ __forceinline__

constexpr int IMG = 224, CH = 96, HID = 384, NT = 768;
constexpr u32 X_BYTES = IMG * IMG * CH * 2;

constexpr float GA = 0.4686686930532063f, GK = 4.91222953926424f;
constexpr float GA_INV = 2.1337034344781243f, GA_INV2 = 4.552690346303743f;

constexpr u32 L_X = 0, X_REC = 1600;
constexpr u32 L_Y = 0, L_YR = 36864;
constexpr u32 L_W1L = 37632;
constexpr u32 L_SCR = 74496, SCR_W = 2304;
constexpr u32 L_V = 83200, V_ROW = 416;
constexpr u32 L_T = 83200, T_ROW = 144;
constexpr u32 L_W1E = 123136;
constexpr u32 L_HREM = 160000;
constexpr u32 L_CNT = 163072;
constexpr u32 L_W2 = 0;
constexpr u32 L_RP = 102144;
constexpr u32 L_BP = 105216;
constexpr u32 L_GG = 105600, L_PS = 107136, L_RED = 107200;
constexpr u32 L_FL = 107968;
constexpr u32 LDS_BYTES = 163840;

constexpr u32 TOT_ROW = 512, TOT_BIAS = 384;
constexpr u32 WS_EPOCH = 0, WS_TOT = 64, WS_PART = 64 + 8 * TOT_ROW;
constexpr int SPIN_CAP = 1 << 20;
constexpr int CODE_LINES = 172;
constexpr int ST_AUX = 16;
constexpr int LN_LAST = 2;
constexpr int G1_PRIO = 0x1bf;
constexpr int W2_EARLY = 8;

#define SB() __builtin_amdgcn_sched_barrier(0)
#define PIN(v) asm volatile("" : "+v"(v))
template <int V> struct int_c { static constexpr int value = V; };
template <int N, int I = 0, class F> DI void static_for(F&& f) { if constexpr (I < N) { f(int_c<I>{}); static_for<N, I + 1>(f); } }
DI void bar_lds() { asm volatile("s_waitcnt lgkmcnt(0)\n\ts_barrier" ::: "memory"); }
DI void bar_all() { asm volatile("s_waitcnt vmcnt(0) lgkmcnt(0)\n\ts_barrier" ::: "memory"); }
typedef __amdgpu_buffer_rsrc_t rsrc_t;
DI rsrc_t make_rsrc(gcu16* p, u32 bytes) { return __builtin_amdgcn_make_buffer_rsrc((void*)p, 0, bytes, 0x00020000); }
DI u32x4 ld128(rsrc_t r, u32 off) { return __builtin_amdgcn_raw_buffer_load_b128(r, off, 0, 0); }
DI void dma16(u32x4 rs, u32 voff, u32 soff, u32 lds) {
  u32 keep;
  asm volatile("s_mov_b32 %0, m0\n\ts_mov_b32 m0, %1\n\ts_nop 0\n\tbuffer_load_dwordx4 %2, %3, %4 offen lds\n\ts_mov_b32 m0, %0"
               : "=&s"(keep) : "s"(lds), "v"(voff), "s"(rs), "s"(soff) : "memory");
}
DI u32 pack_bf16(float a, float b) { bf16x2 v = __builtin_convertvector(f32x2{a, b}, bf16x2); return __builtin_bit_cast(u32, v); }
DI float bf_lo(u32 v) { return __uint_as_float(v << 16); }
DI float bf_hi(u32 v) { return __uint_as_float(v & 0xffff0000u); }
DI u32 perm_lo(u32 a, u32 b) { return __builtin_amdgcn_perm(b, a, 0x05040100u); }
DI u32 perm_hi(u32 a, u32 b) { return __builtin_amdgcn_perm(b, a, 0x07060302u); }
DI s16x4 tr_read(LDS_AS char* p) { return __builtin_amdgcn_ds_read_tr16_b64_v4i16((LDS_AS s16x4*)p); }
DI s16x8 tr_frag(LDS_AS char* p, int step) {
  s16x4 a = tr_read(p), b = tr_read(p + step);
  return __builtin_shufflevector(a, b, 0, 1, 2, 3, 4, 5, 6, 7);
}
DI f32x16 mfma32(s16x8 a, s16x8 b, f32x16 c) { return __builtin_amdgcn_mfma_f32_32x32x16_bf16(a, b, c, 0, 0, 0); }
DI f32x4 mfma16(s16x8 a, s16x8 b, f32x4 c) { return __builtin_amdgcn_mfma_f32_16x16x32_bf16(a, b, c, 0, 0, 0); }
DI float sum_xor32(float v) { auto r = __builtin_amdgcn_permlane32_swap(__float_as_uint(v), __float_as_uint(v), false, false); return __uint_as_float(r[0]) + __uint_as_float(r[1]); }
DI float sum_xor16(float v) { auto r = __builtin_amdgcn_permlane16_swap(__float_as_uint(v), __float_as_uint(v), false, false); return __uint_as_float(r[0]) + __uint_as_float(r[1]); }
typedef __attribute__((address_space(1))) char gchar;
DI u64 ws_load(gu64* ws, u32 word) { return __hip_atomic_load((gu64*)((gchar*)ws + word * 8u), RLX_AGENT); }
DI void ws_store(gu64* ws, u32 word, u64 v) { __hip_atomic_store((gu64*)((gchar*)ws + word * 8u), v, RLX_AGENT); }
DI u64 granule(u64 tag, float v) { return (tag << 24) | (u64)((__float_as_uint(v) + 0x80u) >> 8); }
DI float granule_val(u64 g) { return __uint_as_float((u32)g << 8); }
template <int CTRL> DI float dpp_add(float v) { return v + __builtin_bit_cast(float, __builtin_amdgcn_update_dpp(0, __builtin_bit_cast(int, v), CTRL, 0xf, 0xf, false)); }
DI float wave_sum(float v) { return dpp_add<0xb1>(dpp_add<0x4e>(dpp_add<0x124>(dpp_add<0x128>(sum_xor32(sum_xor16(v)))))); }
enum { C_YFREE, C_W1EFREE, C_STAGED };
DI void arrive(int c) {
  if (__builtin_amdgcn_inverse_ballot_w64(1ull)) asm volatile("ds_add_u32 %0, %1" :: "v"(L_CNT + 4 * c), "v"(1u) : "memory");
}
DI void await(int c) {
#pragma unroll 1
  for (int spin = 0; spin < SPIN_CAP; spin++) {
    if (__builtin_amdgcn_readfirstlane(*(volatile LDS_AS u32*)((LDS_AS char*)0u + L_CNT + 4 * c)) >= 12) break;
    __builtin_amdgcn_s_sleep(1);
  }
  asm volatile("" ::: "memory");
}
DI u32 wave_slot(int w) { return w < 6 ? L_Y + w * 6144 : L_W1E + (w - 6) * 6144; }
DI u32 pos_channel(u32 p) { const u32 e = (p * 43) >> 9; return 8 * (p - 12 * e) + e; }

DI void layer_norm_unit(LDS_AS char* L, u32 src0, u32 src1, u32 dst, u32 diag) {
  LDS_AS char* p0 = L + src0;
  LDS_AS char* p1 = L + src1;
  constexpr u32 dst_step = 2048;
  s16x8 vf[3];
#pragma unroll
  for (int c = 0; c < 3; c++)
    vf[c] = __builtin_shufflevector(tr_read(p0 + c * 32 * V_ROW), tr_read(p1 + c * 32 * V_ROW), 0, 1, 2, 3, 4, 5, 6, 7);
  const s16x8 ones = {0x3f80, 0x3f80, 0x3f80, 0x3f80, 0x3f80, 0x3f80, 0x3f80, 0x3f80};
  f32x4 ms = {0.f, 0.f, 0.f, 0.f}, mq = {0.f, 0.f, 0.f, 0.f};
#pragma unroll
  for (int c = 0; c < 3; c++) { mq = mfma16(vf[c], vf[c], mq); ms = mfma16(ones, vf[c], ms); }
  f32x2 f[12];
#pragma unroll
  for (int i = 0; i < 12; i++) { const u32 v = __builtin_bit_cast(u32x4, vf[i >> 2])[i & 3]; f[i] = f32x2{bf_lo(v), bf_hi(v)}; }
  const bool b0 = __builtin_amdgcn_inverse_ballot_w64(0xaaaaaaaaaaaaaaaaull), b1 = __builtin_amdgcn_inverse_ballot_w64(0xccccccccccccccccull);
  const float d01 = b0 ? mq[1] : mq[0], d23 = b0 ? mq[3] : mq[2], dg = b1 ? d23 : d01;
  const float sq = __int_as_float(__builtin_amdgcn_ds_bpermute(diag, __float_as_int(dg))), sum = ms[0];
  const float mu = sum * (1.0f / CH);
  const float var = fmaxf(__builtin_fmaf(-mu, mu, sq * (1.0f / CH)), 0.f);
  const float rs = GA * __builtin_amdgcn_rsqf(var + 1e-5f);
  const float off = -mu * rs;
#pragma unroll
  for (int c = 0; c < 3; c++) {
    u32x4 o;
#pragma unroll
    for (int i = 0; i < 4; i++) { f32x2 y = __builtin_elementwise_fma(f[4 * c + i], f32x2{rs, rs}, f32x2{off, off}); o[i] = pack_bf16(y.x, y.y); }
    *(LDS_AS u32x4*)(L + dst + c * dst_step) = o;
  }
}

DI void layer_norm_last(LDS_AS char* L, int lane) {
  const int cq = lane >> 2, q = lane & 3, oq = 4 * (lane >> 4) + (lane & 3);
  const u32x2 a = __builtin_bit_cast(u32x2, tr_read(L + L_V + (4 * oq + ((lane >> 2) & 3)) * V_ROW + 48 * 8));
  const u32x2 b = __builtin_bit_cast(u32x2, tr_read(L + L_V + (64 + 4 * (oq & 7) + ((lane >> 2) & 3)) * V_ROW + 48 * 8));
  f32x2 f[4] = {{bf_lo(a.x), bf_hi(a.x)}, {bf_lo(a.y), bf_hi(a.y)}, {bf_lo(b.x), bf_hi(b.x)}, {bf_lo(b.y), bf_hi(b.y)}};
  const float m = cq < 8 ? 1.f : 0.f;
  const f32x2 s = (f[0] + f[1]) + (f[2] + f[3]) * m;
  const f32x2 ss = __builtin_elementwise_fma(f[0], f[0], f[1] * f[1]) + __builtin_elementwise_fma(f[2], f[2], f[3] * f[3]) * m;
  const float sum = dpp_add<0x124>(dpp_add<0x128>(sum_xor32(sum_xor16(s.x + s.y))));
  const float sq = dpp_add<0x124>(dpp_add<0x128>(sum_xor32(sum_xor16(ss.x + ss.y))));
  const float mu = sum * (1.0f / CH);
  const float var = fmaxf(__builtin_fmaf(-mu, mu, sq * (1.0f / CH)), 0.f);
  const float rs = GA * __builtin_amdgcn_rsqf(var + 1e-5f);
  const float off = -mu * rs;
#pragma unroll
  for (int i = 0; i < 4; i++) f[i] = __builtin_elementwise_fma(f[i], f32x2{rs, rs}, f32x2{off, off});
  LDS_AS char* d = L + L_YR + ((cq >> 1) * 4 + q) * 16 + ((cq ^ (cq >> 1)) & 1) * 8;
  *(LDS_AS u32x2*)d = u32x2{pack_bf16(f[0].x, f[0].y), pack_bf16(f[1].x, f[1].y)};
  if (cq < 8) *(LDS_AS u32x2*)(d + 512) = u32x2{pack_bf16(f[2].x, f[2].y), pack_bf16(f[3].x, f[3].y)};
}

DI float sum_sq8(const u32* h) {
  float r;
  asm volatile("v_dot2_f32_bf16 %0, %1, %1, 0\n\tv_dot2_f32_bf16 %0, %2, %2, %0\n\tv_dot2_f32_bf16 %0, %3, %3, %0\n\tv_dot2_f32_bf16 %0, %4, %4, %0\n\t"
               "v_dot2_f32_bf16 %0, %5, %5, %0\n\tv_dot2_f32_bf16 %0, %6, %6, %0\n\tv_dot2_f32_bf16 %0, %7, %7, %0\n\tv_dot2_f32_bf16 %0, %8, %8, %0\n\ts_nop 2"
               : "=&v"(r) : "v"(h[0]), "v"(h[1]), "v"(h[2]), "v"(h[3]), "v"(h[4]), "v"(h[5]), "v"(h[6]), "v"(h[7]));
  return r;
}
DI float add1(float a, float b) { float r; asm("v_add_f32 %0, %1, %2" : "=v"(r) : "v"(a), "v"(b)); return r; }

__global__ void __launch_bounds__(NT, 3)
slak_block_kernel(gcu16*, gcu16*, gcu16*, gcu16*, gcu16*, gcu16*, gu16*, gu64*) {
  u64x8 karg;
  asm volatile("s_load_dwordx16 %0, %1, 0x0" : "=s"(karg) : "s"(__builtin_amdgcn_kernarg_segment_ptr()));
  LDS_AS char* const L = (LDS_AS char*)0u;
  const int t = threadIdx.x, lane = t & 63, bid = blockIdx.x;
  const int w = __builtin_amdgcn_readfirstlane(t >> 6);
  u32 warm = 0;
  if (t < CODE_LINES / 4)
    warm = *(const __attribute__((address_space(1))) u32*)((__builtin_amdgcn_s_getpc() & ~255ull) + (((bid >> 3) & 3) + 4 * t) * 128);
  SB();
  const int xcd = bid & 7, idx = bid >> 3;
  const int y0 = 14 * ((xcd >> 1) * 4 + (idx >> 3)), x0 = 14 * ((xcd & 1) * 8 + (idx & 7));

  const int cg = w >> 1, xh = w & 1;
  const u32 tq = ((u32)t * 2731) >> 16, c4 = t - tq * 24;
  u32 dwo[2], xo[4], xdst[2];
  {
    const u32 r0 = 2 * (tq & 3) - (tq >> 2);
#pragma unroll
    for (int h = 0; h < 2; h++) dwo[h] = r0 + h < 7u ? (r0 + h) * (7 * CH * 2) + c4 * 8 : 0x80000000u;
  }
  auto item_offsets = [&](int it) {
    const u32 item = t + it * NT, P = (item * 5462) >> 16, o = item - P * 12, Y = (P * 6554) >> 16, X = P - Y * 10;
    const u32 gy = y0 - 3 + 2 * Y, gx = (it == 0 || item < 1200) ? x0 - 3 + 2 * X : 1024u;
    u32 base = (u32)(__mul24((int)gy, IMG * CH * 2) + __mul24((int)gx, CH * 2)) + o * 16;
    PIN(base);
    const u32 b0 = gx < (u32)IMG ? base : 0x80000000u, b1 = gx + 1 < (u32)IMG ? base + CH * 2 : 0x80000000u;
    xdst[it] = L_X + ((Y >> 1) * 10 + X) * X_REC + o * 16 + (Y & 1) * 8;
#pragma unroll
    for (int k = 0; k < 4; k++) xo[k] = gy + (k >> 1) < (u32)IMG ? ((k & 1) ? b1 : b0) + (k >> 1) * (IMG * CH * 2) : 0x80000000u;
  };
  item_offsets(0);
  u32 pfo = (idx * 18 + lane) * 128;
  asm volatile("" : "+v"(dwo[0]), "+v"(dwo[1]), "+v"(xo[0]), "+v"(xo[1]), "+v"(xo[2]), "+v"(xo[3]), "+v"(pfo));
  SB();
  asm volatile("s_waitcnt lgkmcnt(0)" : "+s"(karg));
  gcu16* const x = (gcu16*)karg[0]; gcu16* const dw = (gcu16*)karg[1]; gcu16* const w1 = (gcu16*)karg[2]; gcu16* const w2 = (gcu16*)karg[3];
  gcu16* const grn_g = (gcu16*)karg[4]; gcu16* const grn_b = (gcu16*)karg[5]; gu16* const out = (gu16*)karg[6]; gu64* const ws = (gu64*)karg[7];
  const rsrc_t xrs = make_rsrc(x, X_BYTES);
  u32x2 dv[2][7];
  if (w < 3) {
    const rsrc_t drs = make_rsrc(dw, 49 * CH * 2);
#pragma unroll
    for (int i = 0; i < 14; i++) dv[i / 7][i % 7] = __builtin_amdgcn_raw_buffer_load_b64(drs, dwo[i / 7] + (i % 7) * (CH * 2), 0, 0);
  }
  u32x4 xin[2][4];
  auto load_item = [&](int it) {
#pragma unroll
    for (int k = 0; k < 4; k++) xin[it][k] = ld128(xrs, xo[k]);
  };
  auto store_item = [&](int it) {
#pragma unroll
    for (int e = 0; e < 8; e++) {
      const u32x4* v = xin[it];
      *(volatile LDS_AS u32x2*)(L + xdst[it] + e * 192) = (e & 1) ? u32x2{perm_hi(v[0][e >> 1], v[1][e >> 1]), perm_hi(v[2][e >> 1], v[3][e >> 1])}
                                                                  : u32x2{perm_lo(v[0][e >> 1], v[1][e >> 1]), perm_lo(v[2][e >> 1], v[3][e >> 1])};
    }
  };
  load_item(0);
  if (w == 1 && lane < 36) warm = *(const __attribute__((address_space(1))) u32*)((gchar*)(lane < 18 ? w1 : w2 - 18 * 64) + pfo);
  SB();
  if (w < 7) item_offsets(1);
  const u32x4 w1rs = {(u32)(u64)w1, (u32)((u64)w1 >> 32) & 0xffffu, CH * HID * 2, 0x00020000};
  const u32 w1d = (w / 6) * 3 * 6144 + (w % 6) * 1024;
  const u32 k1 = 16 * (w % 6) + (lane >> 2);
  u32 w1o = __umul24(pos_channel(k1 ^ (((k1 >> 3) & 1) << 2)), HID * 2) + (w / 6) * 384 + (lane & 3) * 16;
  u32 tp = L_T + (16 * cg + (cg >= 3 ? 4 : 0) + (lane >> 2) + 100 * (lane & 3)) * T_ROW;
  u32 xb = L_X + (4 * xh + (lane & 3)) * X_REC + (16 * cg + (lane >> 2)) * 16, xb2 = xb + 40 * X_REC;
  u32 vb = 4 * xh + (lane & 3) < 7 ? L_V + (16 * cg + (lane >> 2)) * V_ROW + (4 * xh + (lane & 3)) * 8 : L_HREM + lane * 8;
  asm volatile("" : "+v"(w1o), "+v"(tp), "+v"(xb), "+v"(xb2), "+v"(vb));
  const int T = w >> 1, Hh = w & 1, g4 = lane >> 4, q4 = (lane >> 2) & 3, p4 = lane & 3;
  enum { A_LN0, A_LN1, A_YD, A_DIAG, A_WB, A_WE, A_Y, A_SW, A_SR, A_WR, A_YR, A_W2V, A_OOFF, A_HR, A_N };
  u32 adr[A_N];
  auto make_adr = [&](int i) {
    u32 v;
    switch (i) {
      case A_LN0: v = L_V + (8 * g4 + 4 * (g4 & 1) + q4) * V_ROW + (4 * w + p4) * 8; break;
      case A_LN1: v = (g4 & 1) ? adr[A_LN0] - 4 * V_ROW : adr[A_LN0] + 4 * V_ROW; break;
      case A_YD: v = L_Y + T * 6144 + (g4 * 32 + 16 * Hh + (lane & 15)) * 16; break;
      case A_DIAG: v = ((lane & 15) + (lane & 12) * 4) * 4; break;
      case A_WB: v = (8 * (g4 >> 1) + q4) * 64 + (g4 & 1) * 32 + p4 * 8 + Hh * 3 * 6144; break;
      case A_WE: v = adr[A_WB] + L_W1E; break;
      case A_Y: v = L_Y + T * 6144 + (lane ^ (16 * Hh)) * 16; break;
      case A_SW: v = L_SCR + w * SCR_W + (lane & 31) * 72 + (lane >> 5) * 8; break;
      case A_SR: v = L_SCR + w * SCR_W + (8 * g4 + q4) * 72 + p4 * 8; break;
      case A_WR: v = (T < 3 ? L_W1E : L_W1L) + (Hh * 3 + T % 3) * 6144 + (8 * g4 + q4) * 64 + p4 * 8; break;
      case A_YR: v = L_YR + (g4 * 4 + p4) * 16; break;
      case A_W2V: v = (32 * w + 4 * ((lane >> 2) & 7) + (lane >> 5)) * (CH * 2) + p4 * 16; break;
      case A_OOFF: {
        const u32 pi = 4 * w + q4, py = (pi * 37) >> 8;
        v = (u32)__mul24((y0 + 2 * py + (p4 >> 1)) * IMG + x0 + 2 * (pi - 7 * py) + (p4 & 1), CH * 2) + g4 * 16;
      } break;
      default: v = L_HREM + (Hh * 24 + T * 4 + (lane >> 3)) * 64 + (lane & 7) * 2; break;
    }
    PIN(v);
    adr[i] = v;
  };
#pragma unroll
  for (int i = 0; i < A_N; i++) make_adr(i);
  SB();
  if (w < 3) {
#pragma unroll
    for (int e = 0; e < 4; e++) {
      u32 q[2][2][4];
#pragma unroll
      for (int h = 0; h < 2; h++) {
        u32 v[7];
#pragma unroll
        for (int j = 0; j < 7; j++) v[j] = dv[h][j][e >> 1];
#pragma unroll
        for (int sx = 0; sx < 3; sx++) {
          q[0][h][sx] = (e & 1) ? perm_hi(v[2 * sx], v[2 * sx + 1]) : perm_lo(v[2 * sx], v[2 * sx + 1]);
          q[1][h][sx + 1] = (e & 1) ? perm_hi(v[2 * sx + 1], v[2 * sx + 2]) : perm_lo(v[2 * sx + 1], v[2 * sx + 2]);
        }
        q[0][h][3] = (e & 1) ? v[6] >> 16 : v[6] & 0xffffu;
        q[1][h][0] = (e & 1) ? v[0] & 0xffff0000u : v[0] << 16;
      }
      LDS_AS char* d = L + L_T + (52 * (c4 & 1) + 12 * e + (c4 >> 1) + 200 * (tq >> 2)) * T_ROW + (tq & 3) * 32;
#pragma unroll
      for (int ox = 0; ox < 2; ox++) {
        *(LDS_AS u32x4*)(d + ox * 100 * T_ROW) = u32x4{q[ox][0][0], q[ox][1][0], q[ox][0][1], q[ox][1][1]};
        *(LDS_AS u32x4*)(d + ox * 100 * T_ROW + 16) = u32x4{q[ox][0][2], q[ox][1][2], q[ox][0][3], q[ox][1][3]};
      }
    }
  }
  if (t == NT - 1) *(LDS_AS u32x4*)(L + L_CNT) = u32x4{0, 0, 0, 0};
  asm volatile("" : "+v"(xin[0][0]), "+v"(xin[0][1]), "+v"(xin[0][2]), "+v"(xin[0][3]));
  if (w < 7) load_item(1);
  SB();
  store_item(0);
  bar_lds();

  u64 epoch;
  {
    s16x8 wq[8], d[4][5];
    auto load_x = [&](int sx, int yp) {
      d[sx][yp] = yp < 4 ? *(volatile LDS_AS s16x8*)(L + xb + (yp * 10 + sx) * X_REC) : *(volatile LDS_AS s16x8*)(L + xb2 + sx * X_REC);
    };
    auto load_w = [&](int i) { wq[i] = *(volatile LDS_AS s16x8*)(L + tp + i * 16); };
    auto half = [](s16x8 v, int h) { return h ? __builtin_shufflevector(v, v, 4, 5, 6, 7) : __builtin_shufflevector(v, v, 0, 1, 2, 3); };
    auto conv_mfma = [&](f32x4& acc, int sx, int sy, int Yo) {
      acc = __builtin_amdgcn_mfma_f32_4x4x4bf16_1k(half(wq[2 * sy + (sx >> 1)], sx & 1), half(d[sx][(Yo + sy) >> 1], (Yo + sy) & 1), acc, 0, 0, 0);
    };
    auto put_row = [&](const f32x4& r, int Yo) { *(LDS_AS u32x2*)(L + vb + Yo * 56) = u32x2{pack_bf16(r[0], r[1]), pack_bf16(r[2], r[3])}; };
    load_w(0); load_x(0, 0); load_x(0, 1); load_w(2); load_w(4); load_x(0, 2); load_w(6);
    f32x4 acc[7];
#pragma unroll
    for (int i = 0; i < 7; i++) acc[i] = f32x4{0.f, 0.f, 0.f, 0.f};
    SB();
    static_for<16>([&](auto ic) {
      constexpr int i = ic.value, sx = i >> 2, sy = i & 3;
#pragma unroll
      for (int Yo = 0; Yo + sy < 6; Yo++) conv_mfma(acc[Yo], sx, sy, Yo);
      if constexpr (i < 9) load_x(1 + i / 3, i % 3);
      if constexpr (i < 4) load_w(2 * i + 1);
      if constexpr (i == 8) { if (w >= 7) arrive(C_STAGED); }
      SB();
    });
    if (w < 7) {
      if (t < 432) store_item(1);
      arrive(C_STAGED);
    }
    await(C_STAGED);
#pragma unroll
    for (int i = 0; i < 8; i++) load_x(i >> 1, 3 + (i & 1));
    epoch = ws_load(ws, WS_EPOCH);
#pragma unroll
    for (int Yo = 0; Yo < 3; Yo++) put_row(acc[Yo], Yo);
    SB();
    static_for<40>([&](auto ic) {
      constexpr int i = ic.value, sx = i / 10, j = i % 10;
      constexpr int yo[10] = {6, 5, 4, 6, 3, 5, 6, 4, 5, 6}, sy[10] = {0, 1, 2, 1, 3, 2, 2, 3, 3, 3};
      conv_mfma(acc[yo[j]], sx, sy[j], yo[j]);
      if constexpr (i == 4 || i == 12 || i == 20) dma16(w1rs, w1o, (i / 8) * 64, L_W1E + w1d + (i / 8) * 6144);
      if constexpr (i == 37) put_row(acc[3], 3);
      SB();
    });
#pragma unroll
    for (int Yo = 4; Yo < 7; Yo++) put_row(acc[Yo], Yo);
  }
  bar_lds();

  const bool bias_wave = w == 4 && bid < CH;
  u32 bw[6], bb[6];
  if (bias_wave) {
#pragma unroll
    for (int i = 0; i < 6; i++) {
      bw[i] = __builtin_amdgcn_raw_buffer_load_b16(make_rsrc(w2, HID * CH * 2), lane * (CH * 2), bid * 2 + i * (64 * CH * 2), 0);
      bb[i] = __builtin_amdgcn_raw_buffer_load_b16(make_rsrc(grn_b, HID * 2), lane * 2 + i * 128, 0, 0);
    }
  }
  SB();
  layer_norm_unit(L, adr[A_LN0], adr[A_LN1], adr[A_YD], adr[A_DIAG]);
  if (w == LN_LAST) layer_norm_last(L, lane);
  asm volatile("s_waitcnt vmcnt(0)" :: "v"(warm) : "memory");
  epoch = ((u64)(u32)__builtin_amdgcn_readfirstlane((int)(epoch >> 32)) << 32) | (u32)__builtin_amdgcn_readfirstlane((int)epoch);
  const u64 tag = (epoch + 1) & ((1ull << 40) - 1);
  float bias_val = 0.f;
  if (bias_wave) {
    float a = 0.f;
#pragma unroll
    for (int i = 0; i < 6; i++) { asm volatile("" : "+v"(bw[i]), "+v"(bb[i])); a = __builtin_fmaf(bf_lo(bw[i]), bf_lo(bb[i]), a); }
    bias_val = __int_as_float(__builtin_amdgcn_readfirstlane(__float_as_int(wave_sum(a))));
  }
  bar_lds();

  s16x8 hk[6], hs[6];
  const int rq = T / 3, otj[3] = {T % 3, (T + 1) % 3, (T + 2) % 3};
  const u32 totw = WS_TOT + (bid & 7) * TOT_ROW;
  const u32 ooff = adr[A_OOFF];
  u32x4 xr[3];
  u64 bgr;
  const f32x16 zero16 = {0.f, 0.f, 0.f, 0.f, 0.f, 0.f, 0.f, 0.f, 0.f, 0.f, 0.f, 0.f, 0.f, 0.f, 0.f, 0.f};
  {
    LDS_AS char* wE = L + adr[A_WE];
    LDS_AS char* wL = L + adr[A_WB] + L_W1L;
    LDS_AS char* sw = L + adr[A_SW];
    LDS_AS char* sr = L + adr[A_SR];
    LDS_AS char* wr = L + adr[A_WR];
    LDS_AS char* yr = L + adr[A_YR];
    s16x8 yf[6], wf[6], ry[3], rw[3];
    f32x4 racc[2] = {{0.f, 0.f, 0.f, 0.f}, {0.f, 0.f, 0.f, 0.f}};
    float ssv[6];
    f32x16 acc = zero16;
    __builtin_amdgcn_s_setprio(3);
#pragma unroll
    for (int k = 0; k < 6; k++) { yf[k] = *(LDS_AS s16x8*)(L + adr[A_Y] + k * 1024); wf[k] = tr_frag(wE + k * 1024, 256); }
    SB();
#pragma unroll
    for (int u = 0; u < 3; u++) dma16(w1rs, w1o, 192 + u * 64, L_W1L + w1d + u * 6144);
    SB();
#pragma unroll
    for (int k = 0; k < 6; k++) {
      acc = mfma32(yf[k], wf[k], acc);
      wf[k] = tr_frag(wE + 6144 + k * 1024, 256);
      SB();
    }
    const u32 w2raw = wave_slot(w);
    auto fetch_w2 = [&](int c) {
      await(c);
      const u32x4 rs = {(u32)(u64)w2, (u32)((u64)w2 >> 32) & 0xffffu, HID * CH * 2, 0x00020000};
#pragma unroll
      for (int j = 0; j < 6; j++) dma16(rs, adr[A_W2V], (j & 1) * 384 + (j >> 1) * 64, w2raw + j * 1024);
    };
    asm volatile("s_waitcnt vmcnt(0)" ::: "memory");
    arrive(C_YFREE);
    static_for<6>([&](auto sc_) {
      constexpr int s = sc_.value, NP = s == 3 ? 10 : 8;
      LDS_AS char* wp2 = (s + 2 < 3 ? wE : wL) + ((s + 2) % 3) * 6144;
      __builtin_amdgcn_s_setprio((G1_PRIO >> (2 * s)) & 3);
      if constexpr (s == 0) { if (bias_wave && lane < 8) ws_store(ws, WS_TOT + lane * TOT_ROW + TOT_BIAS + bid, granule(tag, bias_val)); }
      if constexpr (s == 3) arrive(C_W1EFREE);
      if constexpr (s == 1) { if (w < 6) fetch_w2(C_YFREE); else await(C_YFREE); }
      if constexpr (s == 4) { if (w >= 6) fetch_w2(C_W1EFREE); }
      if constexpr (s == 1 || s == 2) {
#pragma unroll
        for (int j = 0; j < 3; j++) {
          if (s == 1) ry[j] = *(LDS_AS s16x8*)(yr + j * 256);
          rw[j] = tr_frag(wr + j * 2048 + (s - 1) * 32, 256);
        }
      }
      f32x2 xv[NP], z[NP];
#pragma unroll
      for (int i = 0; i < NP; i++) {
        if (i < 8) xv[i] = f32x2{acc[2 * (i & 7)], acc[2 * (i & 7) + 1]};
        else xv[i] = __builtin_amdgcn_inverse_ballot_w64(0xffffull) ? f32x2{racc[0][2 * (i & 1)], racc[0][2 * (i & 1) + 1]} : f32x2{racc[1][2 * (i & 1)], racc[1][2 * (i & 1) + 1]};
        z[i] = __builtin_elementwise_fma(xv[i], xv[i], f32x2{GK, GK});
      }
#pragma unroll
      for (int i = 0; i < NP; i++) PIN(z[i]);
      SB();
#pragma unroll
      for (int i = 0; i < NP; i++) z[i] *= xv[i];
#pragma unroll
      for (int i = 0; i < NP; i++) PIN(z[i]);
      SB();
      f32x16 nxt = zero16;
      float e[2 * NP];
#pragma unroll
      for (int i = 0; i < 2 * NP; i++) {
        constexpr bool rem = s == 1 || s == 2;
        if (s < 5 && i % 3 == 0 && i < 9) { nxt = mfma32(yf[i / 3], wf[i / 3], nxt); PIN(nxt); SB(); }
        if (rem && (i == 12 || i == 13)) { racc[(s - 1) & 1] = mfma16(ry[i - 12], rw[i - 12], racc[(s - 1) & 1]); PIN(racc[(s - 1) & 1]); SB(); }
        if (s == 5 && i % 3 == 0 && i < 9) { xr[i / 3] = __builtin_amdgcn_raw_buffer_load_b128(xrs, ooff, otj[i / 3] * 64, 0); SB(); }
        if (s == 5 && i == 9) { bgr = ws_load(ws + (totw + TOT_BIAS + (w & 1) * 64), lane); SB(); }
        e[i] = __builtin_amdgcn_exp2f(-z[i >> 1][i & 1]);
        PIN(e[i]);
      }
      SB();
      if constexpr (s < 4) {
#pragma unroll
        for (int k = 0; k < 3; k++) wf[k] = tr_frag(wp2 + k * 1024, 256);
      }
#pragma unroll
      for (int i = 0; i < NP; i++) z[i] = f32x2{e[2 * i], e[2 * i + 1]} + 1.0f;
#pragma unroll
      for (int i = 0; i < NP; i++) PIN(z[i]);
      SB();
      float rc[2 * NP];
#pragma unroll
      for (int i = 0; i < 2 * NP; i++) {
        if (s < 5 && i % 3 == 0 && i < 9) { nxt = mfma32(yf[3 + i / 3], wf[3 + i / 3], nxt); PIN(nxt); SB(); }
        if ((s == 1 || s == 2) && i == 9) { racc[(s - 1) & 1] = mfma16(ry[2], rw[2], racc[(s - 1) & 1]); PIN(racc[(s - 1) & 1]); SB(); }
        rc[i] = __builtin_amdgcn_rcpf(z[i >> 1][i & 1]);
        PIN(rc[i]);
      }
      SB();
      if constexpr (s < 4) {
#pragma unroll
        for (int k = 3; k < 6; k++) wf[k] = tr_frag(wp2 + k * 1024, 256);
      }
#pragma unroll
      for (int i = 0; i < NP; i++) xv[i] *= f32x2{rc[2 * i], rc[2 * i + 1]};
#pragma unroll
      for (int i = 0; i < NP; i++) PIN(xv[i]);
      SB();
      u32 hp8[8];
#pragma unroll
      for (int i = 0; i < 8; i++) { hp8[i] = pack_bf16(xv[i].x, xv[i].y); PIN(hp8[i]); }
      SB();
#pragma unroll
      for (int Q = 0; Q < 4; Q++) *(volatile LDS_AS u32x2*)(sw + Q * 16) = u32x2{hp8[2 * Q], hp8[2 * Q + 1]};
      SB();
      ssv[s] = sum_sq8(hp8);
      hk[s] = tr_frag(sr, 4 * 72);
      hs[s] = tr_frag(sr + 32, 4 * 72);
      if constexpr (s == 3) {
        const f32x2 sq = __builtin_elementwise_fma(xv[8], xv[8], xv[9] * xv[9]);
        if (lane < 32) {
          const u32 p01 = pack_bf16(xv[8].x, xv[8].y), p23 = pack_bf16(xv[9].x, xv[9].y);
          LDS_AS unsigned short* hr = (LDS_AS unsigned short*)(L + adr[A_HR]);
          hr[0] = (unsigned short)p01; hr[8] = (unsigned short)(p01 >> 16); hr[16] = (unsigned short)p23; hr[24] = (unsigned short)(p23 >> 16);
          *(LDS_AS float*)(sw + 64) = sq.x + sq.y;
        }
      }
      acc = nxt;
      SB();
    });
#pragma unroll
    for (int i = 0; i < 3; i++) *(volatile LDS_AS f32x2*)(sw + i * 16) = f32x2{ssv[2 * i], ssv[2 * i + 1]};
  }
  asm volatile("" : "+v"(xr[0]), "+v"(xr[1]), "+v"(xr[2]), "+v"(bgr));
  bar_all();

  if (t < HID) {
    float v[13];
    LDS_AS char* q = L + L_SCR + (w / 3) * SCR_W + (lane & 31) * 72;
#pragma unroll
    for (int i = 0; i < 12; i++) v[i] = *(LDS_AS float*)(q + (w % 3) * 16 + (lane >> 5) * 4 + (i >> 1) * 2 * SCR_W + (i & 1) * 8);
    v[12] = *(LDS_AS float*)(q + (2 * (w % 3) + (lane >> 5)) * 2 * SCR_W + 64);
    const float s = (((v[0] + v[1]) + (v[2] + v[3])) + ((v[4] + v[5]) + (v[6] + v[7]))) + (((v[8] + v[9]) + (v[10] + v[11])) + v[12]);
    ws_store(ws, WS_PART + (((u32)t >> 4) * 256 + bid) * 16 + (t & 15), granule(tag, s));
  }
  u32x2 w2r[12];
  u32 ap[4], ab[2], hp, rw, w2d0, w2d1;
  f32x2 w2f[24];
  auto unpack_w2 = [&](int i) {
    const int ot = i >> 3, r = (i >> 1) & 3, kp = i & 1;
    const u32 a = w2r[4 * ot + 2 * kp][r >> 1], b = w2r[4 * ot + 2 * kp + 1][r >> 1];
    w2f[i] = (r & 1) ? f32x2{bf_hi(a), bf_hi(b)} : f32x2{bf_lo(a), bf_lo(b)};
    PIN(w2f[i]);
  };
  auto swap_out = [&]() {
    SB();
#pragma unroll
    for (int i = 0; i < 12; i++) w2r[i] = *(volatile LDS_AS u32x2*)(L + wave_slot(w) + lane * 8 + i * 512);
#pragma unroll
    for (int i = 0; i < 6; i++) *(volatile LDS_AS s16x8*)(L + wave_slot(w) + lane * 16 + i * 1024) = hs[i];
    if (t == 512) *(LDS_AS u32*)(L + L_FL) = 0;
    SB();
    const u32 w2d = L_W2 + ((t & 1) * 12 + w) * 1024 + (16 * g4 + ((t >> 1) & 3)) * 16 + (t & 8);
    const u32 aslot = L_W2 + (16 * g4 + q4) * 16;
    ab[0] = aslot + p4 * 64; ab[1] = aslot + (p4 ^ 1) * 64 + 12288;
#pragma unroll
    for (int i = 0; i < 4; i++) ap[i] = (((i >> 1) ^ rq) ? ab[1] : ab[0]) + (otj[0] * 24576 + ((i & 1) ^ Hh) * 6144);
    hp = L_HREM + (Hh * 24 + g4) * 64 + p4 * 16;
    rw = L_RP + (Hh * 4 * CH + 32 * otj[0] + 4 * rq) * 4 + p4 * (CH * 4) + g4 * 32;
    w2d0 = w2d + (t & 1) * 64; w2d1 = w2d + ((t & 1) ^ 1) * 64;
    asm volatile("" : "+v"(ap[0]), "+v"(ap[1]), "+v"(ap[2]), "+v"(ap[3]), "+v"(ab[0]), "+v"(ab[1]), "+v"(hp), "+v"(rw), "+v"(w2d0), "+v"(w2d1));
#pragma unroll
    for (int i = 0; i < W2_EARLY; i++) unpack_w2(i);
    SB();
  };
  {
    const bool mine = (u32)(t - HID) < CH;
    if (!__all(!mine || (bgr >> 24) == tag)) {
#pragma unroll 1
      for (int spin = 0; spin < SPIN_CAP; spin++) {
        bgr = ws_load(ws + (totw + TOT_BIAS + (w & 1) * 64), lane);
        if (__all(!mine || (bgr >> 24) == tag)) break;
      }
    }
    if (mine) *(LDS_AS float*)(L + L_BP + (t - HID) * 4) = granule_val(bgr);
  }
  if (bid < 24) {
    u64 gr[6];
    const u32 partw = WS_PART + bid * 4096 + t;
#pragma unroll
    for (int i = 0; i < 6; i++) gr[i] = ws_load(ws, partw + (i < 5 || t < 256 ? i * NT : 0));
    swap_out();
#pragma unroll 1
    for (int spin = 0; spin < SPIN_CAP; spin++) {
      bool ok = true;
#pragma unroll
      for (int i = 0; i < 6; i++) ok &= (gr[i] >> 24) == tag;
      if (__all(ok)) break;
#pragma unroll
      for (int i = 0; i < 6; i++)
        if ((gr[i] >> 24) != tag) gr[i] = ws_load(ws, partw + (i < 5 || t < 256 ? i * NT : 0));
    }
    float s = ((granule_val(gr[0]) + granule_val(gr[1])) + (granule_val(gr[2]) + granule_val(gr[3]))) + (granule_val(gr[4]) + (t < 256 ? granule_val(gr[5]) : 0.f));
    s = sum_xor32(sum_xor16(s));
    if (lane < 16) *(LDS_AS float*)(L + L_RED + (lane * 12 + w) * 4) = s;
  } else {
    swap_out();
  }
  bar_lds();
  if (bid < 24 && w < 2) {
    LDS_AS char* q = L + L_RED + (t & 15) * 48;
    const f32x4 a = *(LDS_AS f32x4*)(q) + *(LDS_AS f32x4*)(q + 16) + *(LDS_AS f32x4*)(q + 32);
    const float gx = __builtin_amdgcn_sqrtf(__builtin_fmaf((a[0] + a[1]) + (a[2] + a[3]), GA_INV2, 1e-6f));
    ws_store(ws, WS_TOT + (t >> 4) * TOT_ROW + bid * 16 + (t & 15), granule(tag, gx));
  }
  SB();
#pragma unroll
  for (int i = 0; i < 6; i++) hs[i] = *(LDS_AS s16x8*)(L + wave_slot(w ^ 1) + lane * 16 + i * 1024);
  const u32 remo = lane < 48 ? (u32)(((y0 + 12) * IMG + x0 + 12) * (CH * 2)) + (lane >= 24 ? IMG * CH * 2 - 384 : 0) + lane * 16 : 0x80000000u;
  u32x4 xrem = ld128(xrs, remo);
  u32 gam = grn_g[t < HID ? t : 0];
  f32x4 acc2[3][2];
#pragma unroll
  for (int i = 0; i < 6; i++) acc2[i >> 1][i & 1] = *(LDS_AS f32x4*)(L + L_BP + g4 * 32 + otj[i >> 1] * 128 + ((i & 1) ^ (i < 2 ? rq : 0)) * 16);
  SB();
#pragma unroll
  for (int i = 0; i < 6; i++) {
    const u32x4 xv = i < 2 && rq ? u32x4{xr[0][2], xr[0][3], xr[0][0], xr[0][1]} : xr[i >> 1];
    acc2[i >> 1][i & 1] += f32x4{bf_lo(xv[2 * (i & 1)]), bf_hi(xv[2 * (i & 1)]), bf_lo(xv[2 * (i & 1) + 1]), bf_hi(xv[2 * (i & 1) + 1])};
    asm volatile("" : "+v"(acc2[i >> 1][i & 1]));
  }
  SB();
  const f32x4 zero4 = {0.f, 0.f, 0.f, 0.f};
#pragma unroll
  for (int i = W2_EARLY; i < 24; i++) unpack_w2(i);
#pragma unroll
  for (int i = 0; i < 6; i++) asm volatile("" : "+v"(hs[i]));
  if (t >= HID) PIN(xrem);

  if (t < HID) {
    u64 g;
#pragma unroll 1
    for (int spin = 0; spin < SPIN_CAP; spin++) {
      g = ws_load(ws, totw + t);
      if (__all((g >> 24) == tag)) break;
    }
    PIN(xrem);
    const float gx = granule_val(g);
    *(LDS_AS float*)(L + L_GG + t * 4) = bf_lo(gam) * gx;
    const float s = wave_sum(gx);
    if (lane == 0) *(LDS_AS float*)(L + L_PS + w * 4) = s;
  }
  bar_lds();
  {
    const f32x4 pa = *(LDS_AS f32x4*)(L + L_PS);
    const f32x2 pb = *(LDS_AS f32x2*)(L + L_PS + 16);
    const f32x4 gg = *(LDS_AS f32x4*)(L + L_GG + (t >> 3) * 16);
    if (bid == 0 && t == NT - 1) ws_store(ws, WS_EPOCH, epoch + 1);
    const float mean = (((pa[0] + pa[1]) + (pa[2] + pa[3])) + (pb[0] + pb[1])) * (1.0f / HID);
    const float inv = GA_INV * __builtin_amdgcn_rcpf(mean + 1e-6f);
    const f32x2 s01 = f32x2{gg[0], gg[1]} * inv + GA_INV, s23 = f32x2{gg[2], gg[3]} * inv + GA_INV;
#pragma unroll
    for (int i = 0; i < 12; i++) {
      const f32x2 a = w2f[2 * i] * s01, b = w2f[2 * i + 1] * s23;
      *(volatile LDS_AS u32x2*)(L + (i & 1 ? w2d1 : w2d0) + ((i >> 1) & 1) * 128 + (i >> 2) * 24576) = u32x2{pack_bf16(a.x, a.y), pack_bf16(b.x, b.y)};
    }
  }

  {
    const rsrc_t ors = make_rsrc(out, X_BYTES);
    constexpr int PF = 3;
    s16x8 af[72], rb[6];
    f32x4 ra = zero4;
    u32 rem_cnt = 0;
    auto load_a = [&](int i) {
      if (i % 24 == 0 && i) {
#pragma unroll
        for (int j = 0; j < 4; j++) ap[j] = ab[j >> 1] + (otj[i / 24] * 24576 + ((j & 1) ^ Hh) * 6144);
      }
      af[i] = *(volatile LDS_AS s16x8*)(L + ap[(i / 6) & 3] + (i % 6) * 1024);
    };
    auto store_tile = [&](int j) {
      const u32x2 a = {pack_bf16(acc2[j][0][0], acc2[j][0][1]), pack_bf16(acc2[j][0][2], acc2[j][0][3])};
      const u32x2 b = {pack_bf16(acc2[j][1][0], acc2[j][1][1]), pack_bf16(acc2[j][1][2], acc2[j][1][3])};
      __builtin_amdgcn_raw_buffer_store_b128(j == 0 && rq ? u32x4{b.x, b.y, a.x, a.y} : u32x4{a.x, a.y, b.x, b.y}, ors, ooff, otj[j] * 64, ST_AUX);
    };
    bar_lds();
    __builtin_amdgcn_s_setprio(3);
#pragma unroll
    for (int i = 0; i < PF; i++) load_a(i);
    SB();
    static_for<72>([&](auto ic) {
      constexpr int i = ic.value, ot = i / 24, rt = (i / 12) & 1, k = i % 12;
      acc2[ot][rt] = mfma16(af[i], k < 6 ? hk[k % 6] : hs[k % 6], acc2[ot][rt]);
      if constexpr (i + PF < 72) load_a(i + PF);
      if constexpr (i % 18 == 17 && i < 60) __builtin_amdgcn_s_setprio(2 - i / 18);
      if constexpr (i < 12 && (i & 1)) rb[i >> 1] = *(volatile LDS_AS s16x8*)(L + hp + (i >> 1) * 256);
      if constexpr (i >= 6 && i < 18 && (i & 1) == 0) ra = mfma16(af[(i - 6) >> 1], rb[(i - 6) >> 1], ra);
      if constexpr (i == 19) {
        if (q4 == 0) *(volatile LDS_AS f32x4*)(L + rw) = ra;
        u32 fl = L_FL;
        PIN(fl);
        if (lane == 0) rem_cnt = __hip_atomic_fetch_add((LDS_AS u32*)(L + fl), 1u, __ATOMIC_RELAXED, __HIP_MEMORY_SCOPE_WORKGROUP);
      }
      if constexpr (i == 25 || i == 49) store_tile(ot - 1);
      if constexpr (i == 40) {
        if (__builtin_amdgcn_readfirstlane(rem_cnt) == 11 && lane < 48) {
          f32x4 xq[2];
#pragma unroll
          for (int j = 0; j < 2; j++) {
            const f32x4 a = *(volatile LDS_AS f32x4*)(L + L_RP + (lane * 8 + j * 4) * 4), b = *(volatile LDS_AS f32x4*)(L + L_RP + (4 * CH + lane * 8 + j * 4) * 4);
            const f32x4 c = *(volatile LDS_AS f32x4*)(L + L_BP + (lane - 12 * ((lane * 43) >> 9)) * 32 + j * 16);
#pragma unroll
            for (int e = 0; e < 4; e++) xq[j][e] = add1(add1((e & 1) ? bf_hi(xrem[2 * j + (e >> 1)]) : bf_lo(xrem[2 * j + (e >> 1)]), c[e]), add1(a[e], b[e]));
          }
          __builtin_amdgcn_raw_buffer_store_b128(u32x4{pack_bf16(xq[0][0], xq[0][1]), pack_bf16(xq[0][2], xq[0][3]), pack_bf16(xq[1][0], xq[1][1]), pack_bf16(xq[1][2], xq[1][3])}, ors, remo, 0, ST_AUX);
        }
      }
      SB();
    });
    store_tile(2);
  }
}

extern "C" void kernel_launch(void* const* d_in, const int* in_sizes, int n_in,
                              void* d_out, int out_size, void* d_ws, size_t ws_size,
                              hipStream_t stream) {
  (void)in_sizes; (void)n_in; (void)out_size; (void)ws_size;
  (void)hipFuncSetAttribute((const void*)slak_block_kernel, hipFuncAttributeMaxDynamicSharedMemorySize, LDS_BYTES);
  slak_block_kernel<<<256, NT, LDS_BYTES, stream>>>(
      (gcu16*)d_in[0], (gcu16*)d_in[1], (gcu16*)d_in[2], (gcu16*)d_in[3], (gcu16*)d_in[4], (gcu16*)d_in[5], (gu16*)d_out, (gu64*)d_ws);
}
```

```cpp
#include <hip/hip_runtime.h>
#include <stdint.h>
#define DIAG
#define DIAG_SYNC
#define NSTAMP 16
#define STAMP_MASK 0x4001
#define STAMP_RT 0x4001

typedef unsigned long long u64;
typedef unsigned u32;
typedef float f32x2 __attribute__((ext_vector_type(2)));
typedef float f32x4 __attribute__((ext_vector_type(4)));
typedef float f32x16 __attribute__((ext_vector_type(16)));
typedef short s16x4 __attribute__((ext_vector_type(4)));
typedef short s16x8 __attribute__((ext_vector_type(8)));
typedef __bf16 bf16x2 __attribute__((ext_vector_type(2)));
typedef u32 u32x2 __attribute__((ext_vector_type(2)));
typedef u32 u32x4 __attribute__((ext_vector_type(4)));
typedef u64 u64x8 __attribute__((ext_vector_type(8)));
#define LDS_AS __attribute__((address_space(3)))
typedef __attribute__((address_space(1))) u64 gu64;
typedef __attribute__((address_space(1))) unsigned short gu16;
typedef __attribute__((address_space(1))) const unsigned short gcu16;
#define RLX_AGENT __ATOMIC_RELAXED, __HIP_MEMORY_SCOPE_AGENT
#define DI __device__ __forceinline__

constexpr int IMG = 224, CH = 96, HID = 384, NT = 768;
constexpr u32 X_BYTES = IMG * IMG * CH * 2;

constexpr float GA = 0.4686686930532063f, GK = 4.91222953926424f;
constexpr float GA_INV = 2.1337034344781243f, GA_INV2 = 4.552690346303743f;

constexpr u32 L_X = 0, X_REC = 1600;
constexpr u32 L_Y = 0, L_YR = 36864;
constexpr u32 L_W1L = 37632;
constexpr u32 L_SCR = 74496, SCR_W = 2304;
constexpr u32 L_V = 83200, V_ROW = 416;
constexpr u32 L_T = 83200, T_ROW = 144;
constexpr u32 L_W1E = 123136;
constexpr u32 L_HREM = 160000;
constexpr u32 L_CNT = 163072;
constexpr u32 L_W2 = 0;
constexpr u32 L_RP = 102144;
constexpr u32 L_BP = 105216;
constexpr u32 L_GG = 105600, L_PS = 107136, L_RED = 107200;
constexpr u32 L_FL = 107968;
constexpr u32 LDS_BYTES = 163840;

constexpr u32 TOT_ROW = 512, TOT_BIAS = 384;
constexpr u32 WS_EPOCH = 0, WS_TOT = 64, WS_PART = 64 + 8 * TOT_ROW;
#define CODE_LINES 172
constexpr int SPIN_CAP = 1 << 20;

#define SB() __builtin_amdgcn_sched_barrier(0)
#define PIN(v) asm volatile("" : "+v"(v))
template <int V> struct int_c { static constexpr int value = V; };
template <int N, int I = 0, class F> DI void static_for(F&& f) { if constexpr (I < N) { f(int_c<I>{}); static_for<N, I + 1>(f); } }
DI void bar_lds() { asm volatile("s_waitcnt lgkmcnt(0)\n\ts_barrier" ::: "memory"); }
DI void bar_all() { asm volatile("s_waitcnt vmcnt(0) lgkmcnt(0)\n\ts_barrier" ::: "memory"); }
#define LD_AUX 0
#define FE_WARM 1
#define LN_LAST 2
#define POLL_SLEEP 8
#define POLL_DEPTH 1
#define G1_PRIO 0x1bf
#define G2_PRIO 1
#define W2_EARLY 8
#define G2_PF 3
#define ST_AUX 16
typedef __amdgpu_buffer_rsrc_t rsrc_t;
DI rsrc_t make_rsrc(gcu16* p, u32 bytes) { return __builtin_amdgcn_make_buffer_rsrc((void*)p, 0, bytes, 0x00020000); }
DI u32x4 ld128(rsrc_t r, u32 off) { return __builtin_amdgcn_raw_buffer_load_b128(r, off, 0, LD_AUX); }
DI void dma16(u32x4 rs, u32 voff, u32 soff, u32 lds) {
  u32 keep;
  asm volatile("s_mov_b32 %0, m0\n\ts_mov_b32 m0, %1\n\ts_nop 0\n\tbuffer_load_dwordx4 %2, %3, %4 offen lds\n\ts_mov_b32 m0, %0"
               : "=&s"(keep) : "s"(lds), "v"(voff), "s"(rs), "s"(soff) : "memory");
}
DI u32 pack_bf16(float a, float b) { bf16x2 v = __builtin_convertvector(f32x2{a, b}, bf16x2); return __builtin_bit_cast(u32, v); }
DI float bf_lo(u32 v) { return __uint_as_float(v << 16); }
DI float bf_hi(u32 v) { return __uint_as_float(v & 0xffff0000u); }
DI u32 perm_lo(u32 a, u32 b) { return __builtin_amdgcn_perm(b, a, 0x05040100u); }
DI u32 perm_hi(u32 a, u32 b) { return __builtin_amdgcn_perm(b, a, 0x07060302u); }
DI s16x4 tr_read(LDS_AS char* p) { return __builtin_amdgcn_ds_read_tr16_b64_v4i16((LDS_AS s16x4*)p); }
DI s16x8 tr_frag(LDS_AS char* p, int step) {
  s16x4 a = tr_read(p), b = tr_read(p + step);
  return __builtin_shufflevector(a, b, 0, 1, 2, 3, 4, 5, 6, 7);
}
DI f32x16 mfma32(s16x8 a, s16x8 b, f32x16 c) { return __builtin_amdgcn_mfma_f32_32x32x16_bf16(a, b, c, 0, 0, 0); }
DI f32x4 mfma16(s16x8 a, s16x8 b, f32x4 c) { return __builtin_amdgcn_mfma_f32_16x16x32_bf16(a, b, c, 0, 0, 0); }
DI float sum_xor32(float v) { auto r = __builtin_amdgcn_permlane32_swap(__float_as_uint(v), __float_as_uint(v), false, false); return __uint_as_float(r[0]) + __uint_as_float(r[1]); }
DI float sum_xor16(float v) { auto r = __builtin_amdgcn_permlane16_swap(__float_as_uint(v), __float_as_uint(v), false, false); return __uint_as_float(r[0]) + __uint_as_float(r[1]); }
typedef __attribute__((address_space(1))) char gchar;
DI u64 ws_load(gu64* ws, u32 word) { return __hip_atomic_load((gu64*)((gchar*)ws + word * 8u), RLX_AGENT); }
DI void ws_store(gu64* ws, u32 word, u64 v) { __hip_atomic_store((gu64*)((gchar*)ws + word * 8u), v, RLX_AGENT); }
DI u64 granule(u64 tag, float v) { return (tag << 24) | (u64)((__float_as_uint(v) + 0x80u) >> 8); }
DI float granule_val(u64 g) { return __uint_as_float((u32)g << 8); }
template <int CTRL> DI float dpp_add(float v) { return v + __builtin_bit_cast(float, __builtin_amdgcn_update_dpp(0, __builtin_bit_cast(int, v), CTRL, 0xf, 0xf, false)); }
DI float wave_sum(float v) { return dpp_add<0xb1>(dpp_add<0x4e>(dpp_add<0x124>(dpp_add<0x128>(sum_xor32(sum_xor16(v)))))); }
DI void arrive(int c) {
  if (__builtin_amdgcn_inverse_ballot_w64(1ull)) asm volatile("ds_add_u32 %0, %1" :: "v"(L_CNT + 4 * c), "v"(1u) : "memory");
}
DI void await(int c) {
#pragma unroll 1
  for (int spin = 0; spin < SPIN_CAP; spin++) {
    if (__builtin_amdgcn_readfirstlane(*(volatile LDS_AS u32*)((LDS_AS char*)0u + L_CNT + 4 * c)) >= 12) break;
    __builtin_amdgcn_s_sleep(1);
  }
  asm volatile("" ::: "memory");
}
DI u32 wave_slot(int w) { return w < 6 ? L_Y + w * 6144 : L_W1E + (w - 6) * 6144; }
DI u32 pos_channel(u32 p) { const u32 e = (p * 43) >> 9; return 8 * (p - 12 * e) + e; }

DI void layer_norm_unit(LDS_AS char* L, u32 src0, u32 src1, u32 dst, u32 diag) {
  LDS_AS char* p0 = L + src0;
  LDS_AS char* p1 = L + src1;
  constexpr u32 dst_step = 2048;
  s16x8 vf[3];
#pragma unroll
  for (int c = 0; c < 3; c++)
    vf[c] = __builtin_shufflevector(tr_read(p0 + c * 32 * V_ROW), tr_read(p1 + c * 32 * V_ROW), 0, 1, 2, 3, 4, 5, 6, 7);
  const s16x8 ones = {0x3f80, 0x3f80, 0x3f80, 0x3f80, 0x3f80, 0x3f80, 0x3f80, 0x3f80};
  f32x4 ms = {0.f, 0.f, 0.f, 0.f}, mq = {0.f, 0.f, 0.f, 0.f};
#pragma unroll
  for (int c = 0; c < 3; c++) { mq = mfma16(vf[c], vf[c], mq); ms = mfma16(ones, vf[c], ms); }
  f32x2 f[12];
#pragma unroll
  for (int i = 0; i < 12; i++) { const u32 v = __builtin_bit_cast(u32x4, vf[i >> 2])[i & 3]; f[i] = f32x2{bf_lo(v), bf_hi(v)}; }
  const bool b0 = __builtin_amdgcn_inverse_ballot_w64(0xaaaaaaaaaaaaaaaaull), b1 = __builtin_amdgcn_inverse_ballot_w64(0xccccccccccccccccull);
  const float d01 = b0 ? mq[1] : mq[0], d23 = b0 ? mq[3] : mq[2], dg = b1 ? d23 : d01;
  const float sq = __int_as_float(__builtin_amdgcn_ds_bpermute(diag, __float_as_int(dg))), sum = ms[0];
  const float mu = sum * (1.0f / CH);
  const float var = fmaxf(__builtin_fmaf(-mu, mu, sq * (1.0f / CH)), 0.f);
  const float rs = GA * __builtin_amdgcn_rsqf(var + 1e-5f);
  const float off = -mu * rs;
#pragma unroll
  for (int c = 0; c < 3; c++) {
    u32x4 o;
#pragma unroll
    for (int i = 0; i < 4; i++) { f32x2 y = __builtin_elementwise_fma(f[4 * c + i], f32x2{rs, rs}, f32x2{off, off}); o[i] = pack_bf16(y.x, y.y); }
    *(LDS_AS u32x4*)(L + dst + c * dst_step) = o;
  }
}

DI void layer_norm_last(LDS_AS char* L, int lane) {
  const int cq = lane >> 2, q = lane & 3, oq = 4 * (lane >> 4) + (lane & 3);
  const u32x2 a = __builtin_bit_cast(u32x2, tr_read(L + L_V + (4 * oq + ((lane >> 2) & 3)) * V_ROW + 48 * 8));
  const u32x2 b = __builtin_bit_cast(u32x2, tr_read(L + L_V + (64 + 4 * (oq & 7) + ((lane >> 2) & 3)) * V_ROW + 48 * 8));
  f32x2 f[4] = {{bf_lo(a.x), bf_hi(a.x)}, {bf_lo(a.y), bf_hi(a.y)}, {bf_lo(b.x), bf_hi(b.x)}, {bf_lo(b.y), bf_hi(b.y)}};
  const float m = cq < 8 ? 1.f : 0.f;
  const f32x2 s = (f[0] + f[1]) + (f[2] + f[3]) * m;
  const f32x2 ss = __builtin_elementwise_fma(f[0], f[0], f[1] * f[1]) + __builtin_elementwise_fma(f[2], f[2], f[3] * f[3]) * m;
  const float sum = dpp_add<0x124>(dpp_add<0x128>(sum_xor32(sum_xor16(s.x + s.y))));
  const float sq = dpp_add<0x124>(dpp_add<0x128>(sum_xor32(sum_xor16(ss.x + ss.y))));
  const float mu = sum * (1.0f / CH);
  const float var = fmaxf(__builtin_fmaf(-mu, mu, sq * (1.0f / CH)), 0.f);
  const float rs = GA * __builtin_amdgcn_rsqf(var + 1e-5f);
  const float off = -mu * rs;
#pragma unroll
  for (int i = 0; i < 4; i++) f[i] = __builtin_elementwise_fma(f[i], f32x2{rs, rs}, f32x2{off, off});
  LDS_AS char* d = L + L_YR + ((cq >> 1) * 4 + q) * 16 + ((cq ^ (cq >> 1)) & 1) * 8;
  *(LDS_AS u32x2*)d = u32x2{pack_bf16(f[0].x, f[0].y), pack_bf16(f[1].x, f[1].y)};
  if (cq < 8) *(LDS_AS u32x2*)(d + 512) = u32x2{pack_bf16(f[2].x, f[2].y), pack_bf16(f[3].x, f[3].y)};
}

DI float sum_sq8(const u32* h) {
  float r;
  asm volatile("v_dot2_f32_bf16 %0, %1, %1, 0\n\tv_dot2_f32_bf16 %0, %2, %2, %0\n\tv_dot2_f32_bf16 %0, %3, %3, %0\n\tv_dot2_f32_bf16 %0, %4, %4, %0\n\t"
               "v_dot2_f32_bf16 %0, %5, %5, %0\n\tv_dot2_f32_bf16 %0, %6, %6, %0\n\tv_dot2_f32_bf16 %0, %7, %7, %0\n\tv_dot2_f32_bf16 %0, %8, %8, %0\n\ts_nop 2"
               : "=&v"(r) : "v"(h[0]), "v"(h[1]), "v"(h[2]), "v"(h[3]), "v"(h[4]), "v"(h[5]), "v"(h[6]), "v"(h[7]));
  return r;
}
DI float add1(float a, float b) { float r; asm("v_add_f32 %0, %1, %2" : "=v"(r) : "v"(a), "v"(b)); return r; }
DI void bar_only() { asm volatile("s_barrier" ::: "memory"); }

#define STAMP(i) do { if ((STAMP_MASK >> (i)) & 1) { SB(); if ((STAMP_RT >> (i)) & 1) st_r[i] = __builtin_amdgcn_s_memrealtime(); else st_r[i] = st_r[(i) - 1]; st_c[i] = __builtin_amdgcn_s_memtime(); SB(); } else { st_r[i] = st_r[(i) - 1]; st_c[i] = st_c[(i) - 1]; } } while (0)

__global__ void __launch_bounds__(NT, 3)
slak_block_kernel(gcu16*, gcu16*, gcu16*, gcu16*, gcu16*, gcu16*, gu16*, gu64*) {
  u64x8 karg;
  asm volatile("s_load_dwordx16 %0, %1, 0x0" : "=s"(karg) : "s"(__builtin_amdgcn_kernarg_segment_ptr()));
  LDS_AS char* const L = (LDS_AS char*)0u;
  const int t = threadIdx.x, lane = t & 63, bid = blockIdx.x;
  const int w = __builtin_amdgcn_readfirstlane(t >> 6);
  u64 st_r[NSTAMP], st_c[NSTAMP];
  STAMP(0);
  u32 warm = 0;
  if (t < CODE_LINES / 4)
    warm = *(const __attribute__((address_space(1))) u32*)((__builtin_amdgcn_s_getpc() & ~255ull) + (((bid >> 3) & 3) + 4 * t) * 128);
  SB();
  const int xcd = bid & 7, idx = bid >> 3;
  const int y0 = 14 * ((xcd >> 1) * 4 + (idx >> 3)), x0 = 14 * ((xcd & 1) * 8 + (idx & 7));

  const int cg = w >> 1, xh = w & 1;
  const u32 tq = ((u32)t * 2731) >> 16, c4 = t - tq * 24;
  u32 dwo[2], xo[4], xdst[2];
  {
    const u32 r0 = 2 * (tq & 3) - (tq >> 2);
#pragma unroll
    for (int h = 0; h < 2; h++) dwo[h] = r0 + h < 7u ? (r0 + h) * (7 * CH * 2) + c4 * 8 : 0x80000000u;
  }
  auto item_offsets = [&](int it) {
    const u32 item = t + it * NT, P = (item * 5462) >> 16, o = item - P * 12, Y = (P * 6554) >> 16, X = P - Y * 10;
    const u32 gy = y0 - 3 + 2 * Y, gx = (it == 0 || item < 1200) ? x0 - 3 + 2 * X : 1024u;
    u32 base = (u32)(__mul24((int)gy, IMG * CH * 2) + __mul24((int)gx, CH * 2)) + o * 16;
    PIN(base);
    const u32 b0 = gx < (u32)IMG ? base : 0x80000000u, b1 = gx + 1 < (u32)IMG ? base + CH * 2 : 0x80000000u;
    xdst[it] = L_X + ((Y >> 1) * 10 + X) * X_REC + o * 16 + (Y & 1) * 8;
#pragma unroll
    for (int k = 0; k < 4; k++) xo[k] = gy + (k >> 1) < (u32)IMG ? ((k & 1) ? b1 : b0) + (k >> 1) * (IMG * CH * 2) : 0x80000000u;
  };
  item_offsets(0);
  u32 pfo = (idx * 18 + lane) * 128;
  asm volatile("" : "+v"(dwo[0]), "+v"(dwo[1]), "+v"(xo[0]), "+v"(xo[1]), "+v"(xo[2]), "+v"(xo[3]), "+v"(pfo));
  SB();
  asm volatile("s_waitcnt lgkmcnt(0)" : "+s"(karg));
  STAMP(14);
  gcu16* const x = (gcu16*)karg[0]; gcu16* const dw = (gcu16*)karg[1]; gcu16* const w1 = (gcu16*)karg[2]; gcu16* const w2 = (gcu16*)karg[3];
  gcu16* const grn_g = (gcu16*)karg[4]; gcu16* const grn_b = (gcu16*)karg[5]; gu16* const out = (gu16*)karg[6]; gu64* const ws = (gu64*)karg[7];
  gu64* stamps = ws + (1 << 20);
  const rsrc_t xrs = make_rsrc(x, X_BYTES);
  u32x2 dv[2][7];
  if (w < 3) {
    const rsrc_t drs = make_rsrc(dw, 49 * CH * 2);
#pragma unroll
    for (int i = 0; i < 14; i++) dv[i / 7][i % 7] = __builtin_amdgcn_raw_buffer_load_b64(drs, dwo[i / 7] + (i % 7) * (CH * 2), 0, LD_AUX);
  }
  u32x4 xin[2][4];
  auto load_item = [&](int it) {
#pragma unroll
    for (int k = 0; k < 4; k++) xin[it][k] = ld128(xrs, xo[k]);
  };
  auto store_item = [&](int it) {
#pragma unroll
    for (int e = 0; e < 8; e++) {
      const u32x4* v = xin[it];
      *(volatile LDS_AS u32x2*)(L + xdst[it] + e * 192) = (e & 1) ? u32x2{perm_hi(v[0][e >> 1], v[1][e >> 1]), perm_hi(v[2][e >> 1], v[3][e >> 1])}
                                                                  : u32x2{perm_lo(v[0][e >> 1], v[1][e >> 1]), perm_lo(v[2][e >> 1], v[3][e >> 1])};
    }
  };
  load_item(0);
  if (FE_WARM && w == 1 && lane < 36) warm = *(const __attribute__((address_space(1))) u32*)((gchar*)(lane < 18 ? w1 : w2 - 18 * 64) + pfo);
  SB();
  if (w < 7) item_offsets(1);
  const u32x4 w1rs = {(u32)(u64)w1, (u32)((u64)w1 >> 32) & 0xffffu, CH * HID * 2, 0x00020000};
  const u32 w1d = (w / 6) * 3 * 6144 + (w % 6) * 1024;
  const u32 k1 = 16 * (w % 6) + (lane >> 2);
  u32 w1o = __umul24(pos_channel(k1 ^ (((k1 >> 3) & 1) << 2)), HID * 2) + (w / 6) * 384 + (lane & 3) * 16;
  u32 tp = L_T + (16 * cg + (cg >= 3 ? 4 : 0) + (lane >> 2) + 100 * (lane & 3)) * T_ROW;
  u32 xb = L_X + (4 * xh + (lane & 3)) * X_REC + (16 * cg + (lane >> 2)) * 16, xb2 = xb + 40 * X_REC;
  u32 vb = 4 * xh + (lane & 3) < 7 ? L_V + (16 * cg + (lane >> 2)) * V_ROW + (4 * xh + (lane & 3)) * 8 : L_HREM + lane * 8;
  asm volatile("" : "+v"(w1o), "+v"(tp), "+v"(xb), "+v"(xb2), "+v"(vb));
  const int T = w >> 1, Hh = w & 1, g4 = lane >> 4, q4 = (lane >> 2) & 3, p4 = lane & 3;
  enum { A_LN0, A_LN1, A_YD, A_DIAG, A_WB, A_WE, A_Y, A_SW, A_SR, A_WR, A_YR, A_W2V, A_OOFF, A_HR, A_N };
  u32 adr[A_N];
  auto make_adr = [&](int i) {
    u32 v;
    switch (i) {
      case A_LN0: v = L_V + (8 * g4 + 4 * (g4 & 1) + q4) * V_ROW + (4 * w + p4) * 8; break;
      case A_LN1: v = (g4 & 1) ? adr[A_LN0] - 4 * V_ROW : adr[A_LN0] + 4 * V_ROW; break;
      case A_YD: v = L_Y + T * 6144 + (g4 * 32 + 16 * Hh + (lane & 15)) * 16; break;
      case A_DIAG: v = ((lane & 15) + (lane & 12) * 4) * 4; break;
      case A_WB: v = (8 * (g4 >> 1) + q4) * 64 + (g4 & 1) * 32 + p4 * 8 + Hh * 3 * 6144; break;
      case A_WE: v = adr[A_WB] + L_W1E; break;
      case A_Y: v = L_Y + T * 6144 + (lane ^ (16 * Hh)) * 16; break;
      case A_SW: v = L_SCR + w * SCR_W + (lane & 31) * 72 + (lane >> 5) * 8; break;
      case A_SR: v = L_SCR + w * SCR_W + (8 * g4 + q4) * 72 + p4 * 8; break;
      case A_WR: v = (T < 3 ? L_W1E : L_W1L) + (Hh * 3 + T % 3) * 6144 + (8 * g4 + q4) * 64 + p4 * 8; break;
      case A_YR: v = L_YR + (g4 * 4 + p4) * 16; break;
      case A_W2V: v = (32 * w + 4 * ((lane >> 2) & 7) + (lane >> 5)) * (CH * 2) + p4 * 16; break;
      case A_OOFF: {
        const u32 pi = 4 * w + q4, py = (pi * 37) >> 8;
        v = (u32)__mul24((y0 + 2 * py + (p4 >> 1)) * IMG + x0 + 2 * (pi - 7 * py) + (p4 & 1), CH * 2) + g4 * 16;
      } break;
      default: v = L_HREM + (Hh * 24 + T * 4 + (lane >> 3)) * 64 + (lane & 7) * 2; break;
    }
    PIN(v);
    adr[i] = v;
  };
#pragma unroll
  for (int i = 0; i < A_N; i++) make_adr(i);
  SB();
  if (w < 3) {
#pragma unroll
    for (int e = 0; e < 4; e++) {
      u32 q[2][2][4];
#pragma unroll
      for (int h = 0; h < 2; h++) {
        u32 v[7];
#pragma unroll
        for (int j = 0; j < 7; j++) v[j] = dv[h][j][e >> 1];
#pragma unroll
        for (int sx = 0; sx < 3; sx++) {
          q[0][h][sx] = (e & 1) ? perm_hi(v[2 * sx], v[2 * sx + 1]) : perm_lo(v[2 * sx], v[2 * sx + 1]);
          q[1][h][sx + 1] = (e & 1) ? perm_hi(v[2 * sx + 1], v[2 * sx + 2]) : perm_lo(v[2 * sx + 1], v[2 * sx + 2]);
        }
        q[0][h][3] = (e & 1) ? v[6] >> 16 : v[6] & 0xffffu;
        q[1][h][0] = (e & 1) ? v[0] & 0xffff0000u : v[0] << 16;
      }
      LDS_AS char* d = L + L_T + (52 * (c4 & 1) + 12 * e + (c4 >> 1) + 200 * (tq >> 2)) * T_ROW + (tq & 3) * 32;
#pragma unroll
      for (int ox = 0; ox < 2; ox++) {
        *(LDS_AS u32x4*)(d + ox * 100 * T_ROW) = u32x4{q[ox][0][0], q[ox][1][0], q[ox][0][1], q[ox][1][1]};
        *(LDS_AS u32x4*)(d + ox * 100 * T_ROW + 16) = u32x4{q[ox][0][2], q[ox][1][2], q[ox][0][3], q[ox][1][3]};
      }
    }
  }
  if (t == NT - 1) *(LDS_AS u32x4*)(L + L_CNT) = u32x4{0, 0, 0, 0};
  STAMP(1);
  asm volatile("" : "+v"(xin[0][0]), "+v"(xin[0][1]), "+v"(xin[0][2]), "+v"(xin[0][3]));
  if (w < 7) load_item(1);
  SB();
  store_item(0);
  STAMP(2);
  bar_lds();
  STAMP(3);

  u64 epoch;
  {
    s16x8 wq[8], d[4][5];
    auto load_x = [&](int sx, int yp) {
      d[sx][yp] = yp < 4 ? *(volatile LDS_AS s16x8*)(L + xb + (yp * 10 + sx) * X_REC) : *(volatile LDS_AS s16x8*)(L + xb2 + sx * X_REC);
    };
    auto load_w = [&](int i) { wq[i] = *(volatile LDS_AS s16x8*)(L + tp + i * 16); };
    auto half = [](s16x8 v, int h) { return h ? __builtin_shufflevector(v, v, 4, 5, 6, 7) : __builtin_shufflevector(v, v, 0, 1, 2, 3); };
    auto conv_mfma = [&](f32x4& acc, int sx, int sy, int Yo) {
      acc = __builtin_amdgcn_mfma_f32_4x4x4bf16_1k(half(wq[2 * sy + (sx >> 1)], sx & 1), half(d[sx][(Yo + sy) >> 1], (Yo + sy) & 1), acc, 0, 0, 0);
    };
    auto put_row = [&](const f32x4& r, int Yo) { *(LDS_AS u32x2*)(L + vb + Yo * 56) = u32x2{pack_bf16(r[0], r[1]), pack_bf16(r[2], r[3])}; };
    load_w(0); load_x(0, 0); load_x(0, 1); load_w(2); load_w(4); load_x(0, 2); load_w(6);
    f32x4 acc[7];
#pragma unroll
    for (int i = 0; i < 7; i++) acc[i] = f32x4{0.f, 0.f, 0.f, 0.f};
    SB();
    static_for<16>([&](auto ic) {
      constexpr int i = ic.value, sx = i >> 2, sy = i & 3;
#pragma unroll
      for (int Yo = 0; Yo + sy < 6; Yo++) conv_mfma(acc[Yo], sx, sy, Yo);
      if constexpr (i < 9) load_x(1 + i / 3, i % 3);
      if constexpr (i < 4) load_w(2 * i + 1);
      if constexpr (i == 8) { if (w >= 7) arrive(2); }
      SB();
    });
    if (w < 7) {
      if (t < 432) store_item(1);
      arrive(2);
    }
    await(2);
    STAMP(4);
#pragma unroll
    for (int i = 0; i < 8; i++) load_x(i >> 1, 3 + (i & 1));
    epoch = ws_load(ws, WS_EPOCH);
#pragma unroll
    for (int Yo = 0; Yo < 3; Yo++) put_row(acc[Yo], Yo);
    SB();
    static_for<40>([&](auto ic) {
      constexpr int i = ic.value, sx = i / 10, j = i % 10;
      constexpr int yo[10] = {6, 5, 4, 6, 3, 5, 6, 4, 5, 6}, sy[10] = {0, 1, 2, 1, 3, 2, 2, 3, 3, 3};
      conv_mfma(acc[yo[j]], sx, sy[j], yo[j]);
      if constexpr (i == 4 || i == 12 || i == 20) dma16(w1rs, w1o, (i / 8) * 64, L_W1E + w1d + (i / 8) * 6144);
      if constexpr (i == 37) put_row(acc[3], 3);
      SB();
    });
#pragma unroll
    for (int Yo = 4; Yo < 7; Yo++) put_row(acc[Yo], Yo);
  }
  bar_lds();
  STAMP(5);

  const bool bias_wave = w == 4 && bid < CH;
  u32 bw[6], bb[6];
  if (bias_wave) {
#pragma unroll
    for (int i = 0; i < 6; i++) {
      bw[i] = __builtin_amdgcn_raw_buffer_load_b16(make_rsrc(w2, HID * CH * 2), lane * (CH * 2), bid * 2 + i * (64 * CH * 2), 0);
      bb[i] = __builtin_amdgcn_raw_buffer_load_b16(make_rsrc(grn_b, HID * 2), lane * 2 + i * 128, 0, 0);
    }
  }
  SB();
  layer_norm_unit(L, adr[A_LN0], adr[A_LN1], adr[A_YD], adr[A_DIAG]);
  if (w == LN_LAST) layer_norm_last(L, lane);
  asm volatile("s_waitcnt vmcnt(0)" :: "v"(warm) : "memory");
  epoch = ((u64)(u32)__builtin_amdgcn_readfirstlane((int)(epoch >> 32)) << 32) | (u32)__builtin_amdgcn_readfirstlane((int)epoch);
  const u64 tag = (epoch + 1) & ((1ull << 40) - 1);
  float bias_val = 0.f;
  if (bias_wave) {
    float a = 0.f;
#pragma unroll
    for (int i = 0; i < 6; i++) { asm volatile("" : "+v"(bw[i]), "+v"(bb[i])); a = __builtin_fmaf(bf_lo(bw[i]), bf_lo(bb[i]), a); }
    bias_val = __int_as_float(__builtin_amdgcn_readfirstlane(__float_as_int(wave_sum(a))));
  }
  bar_lds();
  STAMP(6);

  s16x8 hk[6], hs[6];
  const int rq = T / 3, otj[3] = {T % 3, (T + 1) % 3, (T + 2) % 3};
  const u32 totw = WS_TOT + (bid & 7) * TOT_ROW;
  const u32 ooff = adr[A_OOFF];
  u32x4 xr[3];
  u64 bgr;
  const f32x16 zero16 = {0.f, 0.f, 0.f, 0.f, 0.f, 0.f, 0.f, 0.f, 0.f, 0.f, 0.f, 0.f, 0.f, 0.f, 0.f, 0.f};
  {
    LDS_AS char* wE = L + adr[A_WE];
    LDS_AS char* wL = L + adr[A_WB] + L_W1L;
    LDS_AS char* sw = L + adr[A_SW];
    LDS_AS char* sr = L + adr[A_SR];
    LDS_AS char* wr = L + adr[A_WR];
    LDS_AS char* yr = L + adr[A_YR];
    s16x8 yf[6], wf[6], ry[3], rw[3];
    f32x4 racc[2] = {{0.f, 0.f, 0.f, 0.f}, {0.f, 0.f, 0.f, 0.f}};
    float ssv[6];
    f32x16 acc = zero16;
    __builtin_amdgcn_s_setprio(3);
#pragma unroll
    for (int k = 0; k < 6; k++) { yf[k] = *(LDS_AS s16x8*)(L + adr[A_Y] + k * 1024); wf[k] = tr_frag(wE + k * 1024, 256); }
    SB();
#pragma unroll
    for (int u = 0; u < 3; u++) dma16(w1rs, w1o, 192 + u * 64, L_W1L + w1d + u * 6144);
    SB();
#pragma unroll
    for (int k = 0; k < 6; k++) {
      acc = mfma32(yf[k], wf[k], acc);
      wf[k] = tr_frag(wE + 6144 + k * 1024, 256);
      SB();
    }
    const u32 w2raw = wave_slot(w);
    auto fetch_w2 = [&](int c) {
      await(c);
      const u32x4 rs = {(u32)(u64)w2, (u32)((u64)w2 >> 32) & 0xffffu, HID * CH * 2, 0x00020000};
#pragma unroll
      for (int j = 0; j < 6; j++) dma16(rs, adr[A_W2V], (j & 1) * 384 + (j >> 1) * 64, w2raw + j * 1024);
    };
    asm volatile("s_waitcnt vmcnt(0)" ::: "memory");
    arrive(0);
    static_for<6>([&](auto sc_) {
      constexpr int s = sc_.value, NP = s == 3 ? 10 : 8;
      LDS_AS char* wp2 = (s + 2 < 3 ? wE : wL) + ((s + 2) % 3) * 6144;
      __builtin_amdgcn_s_setprio((G1_PRIO >> (2 * s)) & 3);
      if constexpr (s == 0) { if (bias_wave && lane < 8) ws_store(ws, WS_TOT + lane * TOT_ROW + TOT_BIAS + bid, granule(tag, bias_val)); }
      if constexpr (s == 3) arrive(1);
      if constexpr (s == 1) { if (w < 6) fetch_w2(0); else await(0); }
      if constexpr (s == 4) { if (w >= 6) fetch_w2(1); }
      if constexpr (s == 1 || s == 2) {
#pragma unroll
        for (int j = 0; j < 3; j++) {
          if (s == 1) ry[j] = *(LDS_AS s16x8*)(yr + j * 256);
          rw[j] = tr_frag(wr + j * 2048 + (s - 1) * 32, 256);
        }
      }
      f32x2 xv[NP], z[NP];
#pragma unroll
      for (int i = 0; i < NP; i++) {
        if (i < 8) xv[i] = f32x2{acc[2 * (i & 7)], acc[2 * (i & 7) + 1]};
        else xv[i] = __builtin_amdgcn_inverse_ballot_w64(0xffffull) ? f32x2{racc[0][2 * (i & 1)], racc[0][2 * (i & 1) + 1]} : f32x2{racc[1][2 * (i & 1)], racc[1][2 * (i & 1) + 1]};
        z[i] = __builtin_elementwise_fma(xv[i], xv[i], f32x2{GK, GK});
      }
#pragma unroll
      for (int i = 0; i < NP; i++) PIN(z[i]);
      SB();
#pragma unroll
      for (int i = 0; i < NP; i++) z[i] *= xv[i];
#pragma unroll
      for (int i = 0; i < NP; i++) PIN(z[i]);
      SB();
      f32x16 nxt = zero16;
      float e[2 * NP];
#pragma unroll
      for (int i = 0; i < 2 * NP; i++) {
        constexpr bool rem = s == 1 || s == 2;
        if (s < 5 && i % 3 == 0 && i < 9) { nxt = mfma32(yf[i / 3], wf[i / 3], nxt); PIN(nxt); SB(); }
        if (rem && (i == 12 || i == 13)) { racc[(s - 1) & 1] = mfma16(ry[i - 12], rw[i - 12], racc[(s - 1) & 1]); PIN(racc[(s - 1) & 1]); SB(); }
        if (s == 5 && i % 3 == 0 && i < 9) { xr[i / 3] = __builtin_amdgcn_raw_buffer_load_b128(xrs, ooff, otj[i / 3] * 64, LD_AUX); SB(); }
        if (s == 5 && i == 9) { bgr = ws_load(ws + (totw + TOT_BIAS + (w & 1) * 64), lane); SB(); }
        e[i] = __builtin_amdgcn_exp2f(-z[i >> 1][i & 1]);
        PIN(e[i]);
      }
      SB();
      if constexpr (s < 4) {
#pragma unroll
        for (int k = 0; k < 3; k++) wf[k] = tr_frag(wp2 + k * 1024, 256);
      }
#pragma unroll
      for (int i = 0; i < NP; i++) z[i] = f32x2{e[2 * i], e[2 * i + 1]} + 1.0f;
#pragma unroll
      for (int i = 0; i < NP; i++) PIN(z[i]);
      SB();
      float rc[2 * NP];
#pragma unroll
      for (int i = 0; i < 2 * NP; i++) {
        if (s < 5 && i % 3 == 0 && i < 9) { nxt = mfma32(yf[3 + i / 3], wf[3 + i / 3], nxt); PIN(nxt); SB(); }
        if ((s == 1 || s == 2) && i == 9) { racc[(s - 1) & 1] = mfma16(ry[2], rw[2], racc[(s - 1) & 1]); PIN(racc[(s - 1) & 1]); SB(); }
        rc[i] = __builtin_amdgcn_rcpf(z[i >> 1][i & 1]);
        PIN(rc[i]);
      }
      SB();
      if constexpr (s < 4) {
#pragma unroll
        for (int k = 3; k < 6; k++) wf[k] = tr_frag(wp2 + k * 1024, 256);
      }
#pragma unroll
      for (int i = 0; i < NP; i++) xv[i] *= f32x2{rc[2 * i], rc[2 * i + 1]};
#pragma unroll
      for (int i = 0; i < NP; i++) PIN(xv[i]);
      SB();
      u32 hp8[8];
#pragma unroll
      for (int i = 0; i < 8; i++) { hp8[i] = pack_bf16(xv[i].x, xv[i].y); PIN(hp8[i]); }
      SB();
#pragma unroll
      for (int Q = 0; Q < 4; Q++) *(volatile LDS_AS u32x2*)(sw + Q * 16) = u32x2{hp8[2 * Q], hp8[2 * Q + 1]};
      SB();
      ssv[s] = sum_sq8(hp8);
      hk[s] = tr_frag(sr, 4 * 72);
      hs[s] = tr_frag(sr + 32, 4 * 72);
      if constexpr (s == 3) {
        const f32x2 sq = __builtin_elementwise_fma(xv[8], xv[8], xv[9] * xv[9]);
        if (lane < 32) {
          const u32 p01 = pack_bf16(xv[8].x, xv[8].y), p23 = pack_bf16(xv[9].x, xv[9].y);
          LDS_AS unsigned short* hr = (LDS_AS unsigned short*)(L + adr[A_HR]);
          hr[0] = (unsigned short)p01; hr[8] = (unsigned short)(p01 >> 16); hr[16] = (unsigned short)p23; hr[24] = (unsigned short)(p23 >> 16);
          *(LDS_AS float*)(sw + 64) = sq.x + sq.y;
        }
      }
      acc = nxt;
      SB();
    });
#pragma unroll
    for (int i = 0; i < 3; i++) *(volatile LDS_AS f32x2*)(sw + i * 16) = f32x2{ssv[2 * i], ssv[2 * i + 1]};
  }
  asm volatile("" : "+v"(xr[0]), "+v"(xr[1]), "+v"(xr[2]), "+v"(bgr));
  bar_all();
  STAMP(7);

  if (t < HID) {
    float v[13];
    LDS_AS char* q = L + L_SCR + (w / 3) * SCR_W + (lane & 31) * 72;
#pragma unroll
    for (int i = 0; i < 12; i++) v[i] = *(LDS_AS float*)(q + (w % 3) * 16 + (lane >> 5) * 4 + (i >> 1) * 2 * SCR_W + (i & 1) * 8);
    v[12] = *(LDS_AS float*)(q + (2 * (w % 3) + (lane >> 5)) * 2 * SCR_W + 64);
    const float s = (((v[0] + v[1]) + (v[2] + v[3])) + ((v[4] + v[5]) + (v[6] + v[7]))) + (((v[8] + v[9]) + (v[10] + v[11])) + v[12]);
    ws_store(ws, WS_PART + (((u32)t >> 4) * 256 + bid) * 16 + (t & 15), granule(tag, s));
  }
  STAMP(11);
  u32x2 w2r[12];
  u32 ap[4], ab[2], hp, rw, w2d0, w2d1;
  f32x2 w2f[24];
  auto unpack_w2 = [&](int i) {
    const int ot = i >> 3, r = (i >> 1) & 3, kp = i & 1;
    const u32 a = w2r[4 * ot + 2 * kp][r >> 1], b = w2r[4 * ot + 2 * kp + 1][r >> 1];
    w2f[i] = (r & 1) ? f32x2{bf_hi(a), bf_hi(b)} : f32x2{bf_lo(a), bf_lo(b)};
    PIN(w2f[i]);
  };
  auto swap_out = [&]() {
    SB();
#pragma unroll
    for (int i = 0; i < 12; i++) w2r[i] = *(volatile LDS_AS u32x2*)(L + wave_slot(w) + lane * 8 + i * 512);
#pragma unroll
    for (int i = 0; i < 6; i++) *(volatile LDS_AS s16x8*)(L + wave_slot(w) + lane * 16 + i * 1024) = hs[i];
    if (t == 512) *(LDS_AS u32*)(L + L_FL) = 0;
    SB();
    const u32 w2d = L_W2 + ((t & 1) * 12 + w) * 1024 + (16 * g4 + ((t >> 1) & 3)) * 16 + (t & 8);
    const u32 aslot = L_W2 + (16 * g4 + q4) * 16;
    ab[0] = aslot + p4 * 64; ab[1] = aslot + (p4 ^ 1) * 64 + 12288;
#pragma unroll
    for (int i = 0; i < 4; i++) ap[i] = (((i >> 1) ^ rq) ? ab[1] : ab[0]) + (otj[0] * 24576 + ((i & 1) ^ Hh) * 6144);
    hp = L_HREM + (Hh * 24 + g4) * 64 + p4 * 16;
    rw = L_RP + (Hh * 4 * CH + 32 * otj[0] + 4 * rq) * 4 + p4 * (CH * 4) + g4 * 32;
    w2d0 = w2d + (t & 1) * 64; w2d1 = w2d + ((t & 1) ^ 1) * 64;
    asm volatile("" : "+v"(ap[0]), "+v"(ap[1]), "+v"(ap[2]), "+v"(ap[3]), "+v"(ab[0]), "+v"(ab[1]), "+v"(hp), "+v"(rw), "+v"(w2d0), "+v"(w2d1));
#pragma unroll
    for (int i = 0; i < W2_EARLY; i++) unpack_w2(i);
    SB();
  };
  {
    const bool mine = (u32)(t - HID) < CH;
    if (!__all(!mine || (bgr >> 24) == tag)) {
#pragma unroll 1
      for (int spin = 0; spin < SPIN_CAP; spin++) {
        bgr = ws_load(ws + (totw + TOT_BIAS + (w & 1) * 64), lane);
        if (__all(!mine || (bgr >> 24) == tag)) break;
      }
    }
    if (mine) *(LDS_AS float*)(L + L_BP + (t - HID) * 4) = granule_val(bgr);
  }
  if (bid < 24) {
    u64 gr[6];
    const u32 partw = WS_PART + bid * 4096 + t;
#pragma unroll
    for (int i = 0; i < 6; i++) gr[i] = ws_load(ws, partw + (i < 5 || t < 256 ? i * NT : 0));
    swap_out();
    STAMP(12);
#pragma unroll 1
    for (int spin = 0; spin < SPIN_CAP; spin++) {
      bool ok = true;
#pragma unroll
      for (int i = 0; i < 6; i++) ok &= (gr[i] >> 24) == tag;
      if (__all(ok)) break;
#pragma unroll
      for (int i = 0; i < 6; i++)
        if ((gr[i] >> 24) != tag) gr[i] = ws_load(ws, partw + (i < 5 || t < 256 ? i * NT : 0));
    }
    float s = ((granule_val(gr[0]) + granule_val(gr[1])) + (granule_val(gr[2]) + granule_val(gr[3]))) + (granule_val(gr[4]) + (t < 256 ? granule_val(gr[5]) : 0.f));
    s = sum_xor32(sum_xor16(s));
    if (lane < 16) *(LDS_AS float*)(L + L_RED + (lane * 12 + w) * 4) = s;
  } else {
    swap_out();
    STAMP(12);
  }
  STAMP(13);
  bar_lds();
  if (bid < 24 && w < 2) {
    LDS_AS char* q = L + L_RED + (t & 15) * 48;
    const f32x4 a = *(LDS_AS f32x4*)(q) + *(LDS_AS f32x4*)(q + 16) + *(LDS_AS f32x4*)(q + 32);
    const float gx = __builtin_amdgcn_sqrtf(__builtin_fmaf((a[0] + a[1]) + (a[2] + a[3]), GA_INV2, 1e-6f));
    ws_store(ws, WS_TOT + (t >> 4) * TOT_ROW + bid * 16 + (t & 15), granule(tag, gx));
  }
  SB();
#pragma unroll
  for (int i = 0; i < 6; i++) hs[i] = *(LDS_AS s16x8*)(L + wave_slot(w ^ 1) + lane * 16 + i * 1024);
  const u32 remo = lane < 48 ? (u32)(((y0 + 12) * IMG + x0 + 12) * (CH * 2)) + (lane >= 24 ? IMG * CH * 2 - 384 : 0) + lane * 16 : 0x80000000u;
  u32x4 xrem = ld128(xrs, remo);
  u32 gam = grn_g[t < HID ? t : 0];
  f32x4 acc2[3][2];
#pragma unroll
  for (int i = 0; i < 6; i++) acc2[i >> 1][i & 1] = *(LDS_AS f32x4*)(L + L_BP + g4 * 32 + otj[i >> 1] * 128 + ((i & 1) ^ (i < 2 ? rq : 0)) * 16);
  SB();
#pragma unroll
  for (int i = 0; i < 6; i++) {
    const u32x4 xv = i < 2 && rq ? u32x4{xr[0][2], xr[0][3], xr[0][0], xr[0][1]} : xr[i >> 1];
    acc2[i >> 1][i & 1] += f32x4{bf_lo(xv[2 * (i & 1)]), bf_hi(xv[2 * (i & 1)]), bf_lo(xv[2 * (i & 1) + 1]), bf_hi(xv[2 * (i & 1) + 1])};
    asm volatile("" : "+v"(acc2[i >> 1][i & 1]));
  }
  SB();
  const f32x4 zero4 = {0.f, 0.f, 0.f, 0.f};
#pragma unroll
  for (int i = W2_EARLY; i < 24; i++) unpack_w2(i);
#pragma unroll
  for (int i = 0; i < 6; i++) asm volatile("" : "+v"(hs[i]));
  STAMP(8);
  if (t >= HID) PIN(xrem);

  if (t < HID) {
    u64 g;
    if (POLL_DEPTH == 1) {
#pragma unroll 1
      for (int spin = 0; spin < SPIN_CAP; spin++) {
        g = ws_load(ws, totw + t);
        if (__all((g >> 24) == tag)) break;
      }
    } else {
      u64 ga = ws_load(ws, totw + t);
      __builtin_amdgcn_s_sleep(POLL_SLEEP);
      u64 gb = ws_load(ws, totw + t);
#pragma unroll 1
      for (int spin = 0; spin < SPIN_CAP; spin++) {
        g = ga;
        if (__all((g >> 24) == tag)) break;
        ga = ws_load(ws, totw + t);
        g = gb;
        if (__all((g >> 24) == tag)) break;
        gb = ws_load(ws, totw + t);
      }
    }
    PIN(xrem);
    const float gx = granule_val(g);
    *(LDS_AS float*)(L + L_GG + t * 4) = bf_lo(gam) * gx;
    const float s = wave_sum(gx);
    if (lane == 0) *(LDS_AS float*)(L + L_PS + w * 4) = s;
  }
  bar_lds();
  STAMP(9);
  {
    const f32x4 pa = *(LDS_AS f32x4*)(L + L_PS);
    const f32x2 pb = *(LDS_AS f32x2*)(L + L_PS + 16);
    const f32x4 gg = *(LDS_AS f32x4*)(L + L_GG + (t >> 3) * 16);
    if (bid == 0 && t == NT - 1) ws_store(ws, WS_EPOCH, epoch + 1);
    const float mean = (((pa[0] + pa[1]) + (pa[2] + pa[3])) + (pb[0] + pb[1])) * (1.0f / HID);
    const float inv = GA_INV * __builtin_amdgcn_rcpf(mean + 1e-6f);
    const f32x2 s01 = f32x2{gg[0], gg[1]} * inv + GA_INV, s23 = f32x2{gg[2], gg[3]} * inv + GA_INV;
#pragma unroll
    for (int i = 0; i < 12; i++) {
      const f32x2 a = w2f[2 * i] * s01, b = w2f[2 * i + 1] * s23;
      *(volatile LDS_AS u32x2*)(L + (i & 1 ? w2d1 : w2d0) + ((i >> 1) & 1) * 128 + (i >> 2) * 24576) = u32x2{pack_bf16(a.x, a.y), pack_bf16(b.x, b.y)};
    }
  }

  {
    const rsrc_t ors = make_rsrc(out, X_BYTES);
    constexpr int PF = G2_PF;
    s16x8 af[72], rb[6];
    f32x4 ra = zero4;
    u32 rem_cnt = 0;
    auto load_a = [&](int i) {
      if (i % 24 == 0 && i) {
#pragma unroll
        for (int j = 0; j < 4; j++) ap[j] = ab[j >> 1] + (otj[i / 24] * 24576 + ((j & 1) ^ Hh) * 6144);
      }
      af[i] = *(volatile LDS_AS s16x8*)(L + ap[(i / 6) & 3] + (i % 6) * 1024);
    };
    auto store_tile = [&](int j) {
      const u32x2 a = {pack_bf16(acc2[j][0][0], acc2[j][0][1]), pack_bf16(acc2[j][0][2], acc2[j][0][3])};
      const u32x2 b = {pack_bf16(acc2[j][1][0], acc2[j][1][1]), pack_bf16(acc2[j][1][2], acc2[j][1][3])};
      __builtin_amdgcn_raw_buffer_store_b128(j == 0 && rq ? u32x4{b.x, b.y, a.x, a.y} : u32x4{a.x, a.y, b.x, b.y}, ors, ooff, otj[j] * 64, ST_AUX);
    };
    bar_lds();
    STAMP(10);
    if (G2_PRIO) __builtin_amdgcn_s_setprio(3);
#pragma unroll
    for (int i = 0; i < PF; i++) load_a(i);
    SB();
    static_for<72>([&](auto ic) {
      constexpr int i = ic.value, ot = i / 24, rt = (i / 12) & 1, k = i % 12;
      acc2[ot][rt] = mfma16(af[i], k < 6 ? hk[k % 6] : hs[k % 6], acc2[ot][rt]);
      if constexpr (i + PF < 72) load_a(i + PF);
      if constexpr (G2_PRIO && i % 18 == 17 && i < 60) __builtin_amdgcn_s_setprio(2 - i / 18);
      if constexpr (i < 12 && (i & 1)) rb[i >> 1] = *(volatile LDS_AS s16x8*)(L + hp + (i >> 1) * 256);
      if constexpr (i >= 6 && i < 18 && (i & 1) == 0) ra = mfma16(af[(i - 6) >> 1], rb[(i - 6) >> 1], ra);
      if constexpr (i == 19) {
        if (q4 == 0) *(volatile LDS_AS f32x4*)(L + rw) = ra;
        u32 fl = L_FL;
        PIN(fl);
        if (lane == 0) rem_cnt = __hip_atomic_fetch_add((LDS_AS u32*)(L + fl), 1u, __ATOMIC_RELAXED, __HIP_MEMORY_SCOPE_WORKGROUP);
      }
      if constexpr (i == 25 || i == 49) store_tile(ot - 1);
      if constexpr (i == 40) {
        if (__builtin_amdgcn_readfirstlane(rem_cnt) == 11 && lane < 48) {
          f32x4 xq[2];
#pragma unroll
          for (int j = 0; j < 2; j++) {
            const f32x4 a = *(volatile LDS_AS f32x4*)(L + L_RP + (lane * 8 + j * 4) * 4), b = *(volatile LDS_AS f32x4*)(L + L_RP + (4 * CH + lane * 8 + j * 4) * 4);
            const f32x4 c = *(volatile LDS_AS f32x4*)(L + L_BP + (lane - 12 * ((lane * 43) >> 9)) * 32 + j * 16);
#pragma unroll
            for (int e = 0; e < 4; e++) xq[j][e] = add1(add1((e & 1) ? bf_hi(xrem[2 * j + (e >> 1)]) : bf_lo(xrem[2 * j + (e >> 1)]), c[e]), add1(a[e], b[e]));
          }
          __builtin_amdgcn_raw_buffer_store_b128(u32x4{pack_bf16(xq[0][0], xq[0][1]), pack_bf16(xq[0][2], xq[0][3]), pack_bf16(xq[1][0], xq[1][1]), pack_bf16(xq[1][2], xq[1][3])}, ors, remo, 0, ST_AUX);
        }
      }
      SB();
    });
    store_tile(2);
  }
  { const u64 d = (st_r[14] - st_r[0]) * 0 + 200; const u64 e = __builtin_amdgcn_s_memrealtime() + d;
    if (w == 5) while (__builtin_amdgcn_s_memrealtime() < e) __builtin_amdgcn_s_sleep(1); (void)stamps; (void)st_c; }
}

extern "C" void kernel_launch(void* const* d_in, const int* in_sizes, int n_in,
                              void* d_out, int out_size, void* d_ws, size_t ws_size,
                              hipStream_t stream) {
  (void)in_sizes; (void)n_in; (void)out_size; (void)ws_size;
  (void)hipFuncSetAttribute((const void*)slak_block_kernel, hipFuncAttributeMaxDynamicSharedMemorySize, LDS_BYTES);
  slak_block_kernel<<<256, NT, LDS_BYTES, stream>>>(
      (gcu16*)d_in[0], (gcu16*)d_in[1], (gcu16*)d_in[2], (gcu16*)d_in[3], (gcu16*)d_in[4], (gcu16*)d_in[5], (gu16*)d_out, (gu64*)d_ws);
}
```

```cpp
#include <hip/hip_runtime.h>
#include <stdint.h>

typedef unsigned long long u64;
typedef unsigned u32;
typedef float f32x2 __attribute__((ext_vector_type(2)));
typedef float f32x4 __attribute__((ext_vector_type(4)));
typedef float f32x16 __attribute__((ext_vector_type(16)));
typedef short s16x4 __attribute__((ext_vector_type(4)));
typedef short s16x8 __attribute__((ext_vector_type(8)));
typedef __bf16 bf16x2 __attribute__((ext_vector_type(2)));
typedef u32 u32x2 __attribute__((ext_vector_type(2)));
typedef u32 u32x4 __attribute__((ext_vector_type(4)));
typedef u64 u64x8 __attribute__((ext_vector_type(8)));
#define LDS_AS __attribute__((address_space(3)))
typedef __attribute__((address_space(1))) u64 gu64;
typedef __attribute__((address_space(1))) unsigned short gu16;
typedef __attribute__((address_space(1))) const unsigned short gcu16;
#define RLX_AGENT __ATOMIC_RELAXED, __HIP_MEMORY_SCOPE_AGENT
#define DI __device__ __forceinline__

constexpr int IMG = 224, CH = 96, HID = 384, NT = 768;
constexpr u32 X_BYTES = IMG * IMG * CH * 2;

constexpr float GA = 0.4686686930532063f, GK = 4.91222953926424f;
constexpr float GA_INV = 2.1337034344781243f, GA_INV2 = 4.552690346303743f;

constexpr u32 L_X = 0, X_REC = 1600;
constexpr u32 L_Y = 0, L_YR = 36864;
constexpr u32 L_W1L = 37632;
constexpr u32 L_SS = 74496, SS_W = 2304;
constexpr u32 L_SCR = 102144;
constexpr u32 L_V = 83200, V_ROW = 416;
constexpr u32 L_T = 83200, T_ROW = 144;
constexpr u32 L_W1E = 123136;
constexpr u32 L_HREM = 160000;
constexpr u32 L_CNT = 163072;
constexpr u32 L_W2 = 0;
constexpr u32 L_RP = 114432;
constexpr u32 L_BP = 117504;
constexpr u32 L_GG = 117888, L_PS = 119424, L_RED = 119488;
constexpr u32 L_FL = 120256;
constexpr u32 LDS_BYTES = 163840;

constexpr u32 TOT_ROW = 512, TOT_BIAS = 384;
constexpr u32 WS_EPOCH = 0, WS_TOT = 64, WS_PART = 64 + 8 * TOT_ROW;
constexpr int SPIN_CAP = 1 << 20;
constexpr int CODE_LINES = 172;
constexpr int ST_AUX = 16;
constexpr int LN_LAST = 2;
constexpr int G1_PRIO = 0x1bf;
constexpr int W2_EARLY = 8;

#define SB() __builtin_amdgcn_sched_barrier(0)
#define PIN(v) asm volatile("" : "+v"(v))
template <int V> struct int_c { static constexpr int value = V; };
template <int N, int I = 0, class F> DI void static_for(F&& f) { if constexpr (I < N) { f(int_c<I>{}); static_for<N, I + 1>(f); } }
DI void bar_lds() { asm volatile("s_waitcnt lgkmcnt(0)\n\ts_barrier" ::: "memory"); }
DI void bar_all() { asm volatile("s_waitcnt vmcnt(0) lgkmcnt(0)\n\ts_barrier" ::: "memory"); }
typedef __amdgpu_buffer_rsrc_t rsrc_t;
DI rsrc_t make_rsrc(gcu16* p, u32 bytes) { return __builtin_amdgcn_make_buffer_rsrc((void*)p, 0, bytes, 0x00020000); }
DI u32x4 ld128(rsrc_t r, u32 off) { return __builtin_amdgcn_raw_buffer_load_b128(r, off, 0, 0); }
DI void dma16(u32x4 rs, u32 voff, u32 soff, u32 lds) {
  u32 keep;
  asm volatile("s_mov_b32 %0, m0\n\ts_mov_b32 m0, %1\n\ts_nop 0\n\tbuffer_load_dwordx4 %2, %3, %4 offen lds\n\ts_mov_b32 m0, %0"
               : "=&s"(keep) : "s"(lds), "v"(voff), "s"(rs), "s"(soff) : "memory");
}
DI u32 pack_bf16(float a, float b) { bf16x2 v = __builtin_convertvector(f32x2{a, b}, bf16x2); return __builtin_bit_cast(u32, v); }
DI float bf_lo(u32 v) { return __uint_as_float(v << 16); }
DI float bf_hi(u32 v) { return __uint_as_float(v & 0xffff0000u); }
DI u32 perm_lo(u32 a, u32 b) { return __builtin_amdgcn_perm(b, a, 0x05040100u); }
DI u32 perm_hi(u32 a, u32 b) { return __builtin_amdgcn_perm(b, a, 0x07060302u); }
DI s16x4 tr_read(LDS_AS char* p) { return __builtin_amdgcn_ds_read_tr16_b64_v4i16((LDS_AS s16x4*)p); }
DI s16x8 tr_frag(LDS_AS char* p, int step) {
  s16x4 a = tr_read(p), b = tr_read(p + step);
  return __builtin_shufflevector(a, b, 0, 1, 2, 3, 4, 5, 6, 7);
}
DI f32x16 mfma32(s16x8 a, s16x8 b, f32x16 c) { return __builtin_amdgcn_mfma_f32_32x32x16_bf16(a, b, c, 0, 0, 0); }
DI f32x4 mfma16(s16x8 a, s16x8 b, f32x4 c) { return __builtin_amdgcn_mfma_f32_16x16x32_bf16(a, b, c, 0, 0, 0); }
DI float sum_xor32(float v) { auto r = __builtin_amdgcn_permlane32_swap(__float_as_uint(v), __float_as_uint(v), false, false); return __uint_as_float(r[0]) + __uint_as_float(r[1]); }
DI float sum_xor16(float v) { auto r = __builtin_amdgcn_permlane16_swap(__float_as_uint(v), __float_as_uint(v), false, false); return __uint_as_float(r[0]) + __uint_as_float(r[1]); }
typedef __attribute__((address_space(1))) char gchar;
DI u64 ws_load(gu64* ws, u32 word) { return __hip_atomic_load((gu64*)((gchar*)ws + word * 8u), RLX_AGENT); }
DI void ws_store(gu64* ws, u32 word, u64 v) { __hip_atomic_store((gu64*)((gchar*)ws + word * 8u), v, RLX_AGENT); }
DI u64 granule(u64 tag, float v) { return (tag << 24) | (u64)((__float_as_uint(v) + 0x80u) >> 8); }
DI float granule_val(u64 g) { return __uint_as_float((u32)g << 8); }
template <int CTRL> DI float dpp_add(float v) { return v + __builtin_bit_cast(float, __builtin_amdgcn_update_dpp(0, __builtin_bit_cast(int, v), CTRL, 0xf, 0xf, false)); }
DI float wave_sum(float v) { return dpp_add<0xb1>(dpp_add<0x4e>(dpp_add<0x124>(dpp_add<0x128>(sum_xor32(sum_xor16(v)))))); }
enum { C_YFREE, C_W1EFREE, C_STAGED };
DI void arrive(int c) {
  if (__builtin_amdgcn_inverse_ballot_w64(1ull)) asm volatile("ds_add_u32 %0, %1" :: "v"(L_CNT + 4 * c), "v"(1u) : "memory");
}
DI void await(int c) {
#pragma unroll 1
  for (int spin = 0; spin < SPIN_CAP; spin++) {
    if (__builtin_amdgcn_readfirstlane(*(volatile LDS_AS u32*)((LDS_AS char*)0u + L_CNT + 4 * c)) >= 12) break;
    __builtin_amdgcn_s_sleep(1);
  }
  asm volatile("" ::: "memory");
}
DI u32 half_tile_put(u32 lane) { return ((lane & 19) + ((lane & 4) << 1) + ((lane & 8) >> 1)) * 32 + (((lane >> 5) ^ (lane >> 2)) & 1) * 16; }
DI u32 half_tile_get(u32 lane) { return (((lane >> 2) & 7) + ((lane >> 5) << 4)) * 32 + (lane & 1) * 16 + (lane & 2) * 4; }
DI u32 wave_slot(int w) { return w < 6 ? L_Y + w * 6144 : L_W1E + (w - 6) * 6144; }
DI u32 pos_channel(u32 p) { const u32 e = (p * 43) >> 9; return 8 * (p - 12 * e) + e; }

DI void layer_norm_unit(LDS_AS char* L, u32 src0, u32 src1, u32 dst, u32 diag) {
  LDS_AS char* p0 = L + src0;
  LDS_AS char* p1 = L + src1;
  constexpr u32 dst_step = 2048;
  s16x8 vf[3];
#pragma unroll
  for (int c = 0; c < 3; c++)
    vf[c] = __builtin_shufflevector(tr_read(p0 + c * 32 * V_ROW), tr_read(p1 + c * 32 * V_ROW), 0, 1, 2, 3, 4, 5, 6, 7);
  const s16x8 ones = {0x3f80, 0x3f80, 0x3f80, 0x3f80, 0x3f80, 0x3f80, 0x3f80, 0x3f80};
  f32x4 ms = {0.f, 0.f, 0.f, 0.f}, mq = {0.f, 0.f, 0.f, 0.f};
#pragma unroll
  for (int c = 0; c < 3; c++) { mq = mfma16(vf[c], vf[c], mq); ms = mfma16(ones, vf[c], ms); }
  f32x2 f[12];
#pragma unroll
  for (int i = 0; i < 12; i++) { const u32 v = __builtin_bit_cast(u32x4, vf[i >> 2])[i & 3]; f[i] = f32x2{bf_lo(v), bf_hi(v)}; }
  const bool b0 = __builtin_amdgcn_inverse_ballot_w64(0xaaaaaaaaaaaaaaaaull), b1 = __builtin_amdgcn_inverse_ballot_w64(0xccccccccccccccccull);
  const float d01 = b0 ? mq[1] : mq[0], d23 = b0 ? mq[3] : mq[2], dg = b1 ? d23 : d01;
  const float sq = __int_as_float(__builtin_amdgcn_ds_bpermute(diag, __float_as_int(dg))), sum = ms[0];
  const float mu = sum * (1.0f / CH);
  const float var = fmaxf(__builtin_fmaf(-mu, mu, sq * (1.0f / CH)), 0.f);
  const float rs = GA * __builtin_amdgcn_rsqf(var + 1e-5f);
  const float off = -mu * rs;
#pragma unroll
  for (int c = 0; c < 3; c++) {
    u32x4 o;
#pragma unroll
    for (int i = 0; i < 4; i++) { f32x2 y = __builtin_elementwise_fma(f[4 * c + i], f32x2{rs, rs}, f32x2{off, off}); o[i] = pack_bf16(y.x, y.y); }
    *(LDS_AS u32x4*)(L + dst + c * dst_step) = o;
  }
}

DI void layer_norm_last(LDS_AS char* L, int lane) {
  const int cq = lane >> 2, q = lane & 3, oq = 4 * (lane >> 4) + (lane & 3);
  const u32x2 a = __builtin_bit_cast(u32x2, tr_read(L + L_V + (4 * oq + ((lane >> 2) & 3)) * V_ROW + 48 * 8));
  const u32x2 b = __builtin_bit_cast(u32x2, tr_read(L + L_V + (64 + 4 * (oq & 7) + ((lane >> 2) & 3)) * V_ROW + 48 * 8));
  f32x2 f[4] = {{bf_lo(a.x), bf_hi(a.x)}, {bf_lo(a.y), bf_hi(a.y)}, {bf_lo(b.x), bf_hi(b.x)}, {bf_lo(b.y), bf_hi(b.y)}};
  const float m = cq < 8 ? 1.f : 0.f;
  const f32x2 s = (f[0] + f[1]) + (f[2] + f[3]) * m;
  const f32x2 ss = __builtin_elementwise_fma(f[0], f[0], f[1] * f[1]) + __builtin_elementwise_fma(f[2], f[2], f[3] * f[3]) * m;
  const float sum = dpp_add<0x124>(dpp_add<0x128>(sum_xor32(sum_xor16(s.x + s.y))));
  const float sq = dpp_add<0x124>(dpp_add<0x128>(sum_xor32(sum_xor16(ss.x + ss.y))));
  const float mu = sum * (1.0f / CH);
  const float var = fmaxf(__builtin_fmaf(-mu, mu, sq * (1.0f / CH)), 0.f);
  const float rs = GA * __builtin_amdgcn_rsqf(var + 1e-5f);
  const float off = -mu * rs;
#pragma unroll
  for (int i = 0; i < 4; i++) f[i] = __builtin_elementwise_fma(f[i], f32x2{rs, rs}, f32x2{off, off});
  LDS_AS char* d = L + L_YR + ((cq >> 1) * 4 + q) * 16 + ((cq ^ (cq >> 1)) & 1) * 8;
  *(LDS_AS u32x2*)d = u32x2{pack_bf16(f[0].x, f[0].y), pack_bf16(f[1].x, f[1].y)};
  if (cq < 8) *(LDS_AS u32x2*)(d + 512) = u32x2{pack_bf16(f[2].x, f[2].y), pack_bf16(f[3].x, f[3].y)};
}

DI float sum_sq8(const u32* h) {
  float r;
  asm volatile("v_dot2_f32_bf16 %0, %1, %1, 0\n\tv_dot2_f32_bf16 %0, %2, %2, %0\n\tv_dot2_f32_bf16 %0, %3, %3, %0\n\tv_dot2_f32_bf16 %0, %4, %4, %0\n\t"
               "v_dot2_f32_bf16 %0, %5, %5, %0\n\tv_dot2_f32_bf16 %0, %6, %6, %0\n\tv_dot2_f32_bf16 %0, %7, %7, %0\n\tv_dot2_f32_bf16 %0, %8, %8, %0\n\ts_nop 2"
               : "=&v"(r) : "v"(h[0]), "v"(h[1]), "v"(h[2]), "v"(h[3]), "v"(h[4]), "v"(h[5]), "v"(h[6]), "v"(h[7]));
  return r;
}
DI float add1(float a, float b) { float r; asm("v_add_f32 %0, %1, %2" : "=v"(r) : "v"(a), "v"(b)); return r; }

__global__ void __launch_bounds__(NT, 3)
slak_block_kernel(gcu16*, gcu16*, gcu16*, gcu16*, gcu16*, gcu16*, gu16*, gu64*) {
  u64x8 karg;
  asm volatile("s_load_dwordx16 %0, %1, 0x0" : "=s"(karg) : "s"(__builtin_amdgcn_kernarg_segment_ptr()));
  LDS_AS char* const L = (LDS_AS char*)0u;
  const int t = threadIdx.x, lane = t & 63, bid = blockIdx.x;
  const int w = __builtin_amdgcn_readfirstlane(t >> 6);
  u32 warm = 0;
  if (t < CODE_LINES / 4)
    warm = *(const __attribute__((address_space(1))) u32*)((__builtin_amdgcn_s_getpc() & ~255ull) + (((bid >> 3) & 3) + 4 * t) * 128);
  SB();
  const int xcd = bid & 7, idx = bid >> 3;
  const int y0 = 14 * ((xcd >> 1) * 4 + (idx >> 3)), x0 = 14 * ((xcd & 1) * 8 + (idx & 7));

  const int cg = w >> 1, xh = w & 1;
  const u32 tq = ((u32)t * 2731) >> 16, c4 = t - tq * 24;
  u32 dwo[2], xo[4], xdst[2];
  {
    const u32 r0 = 2 * (tq & 3) - (tq >> 2);
#pragma unroll
    for (int h = 0; h < 2; h++) dwo[h] = r0 + h < 7u ? (r0 + h) * (7 * CH * 2) + c4 * 8 : 0x80000000u;
  }
  auto item_offsets = [&](int it) {
    const u32 item = t + it * NT, P = (item * 5462) >> 16, o = item - P * 12, Y = (P * 6554) >> 16, X = P - Y * 10;
    const u32 gy = y0 - 3 + 2 * Y, gx = (it == 0 || item < 1200) ? x0 - 3 + 2 * X : 1024u;
    u32 base = (u32)(__mul24((int)gy, IMG * CH * 2) + __mul24((int)gx, CH * 2)) + o * 16;
    PIN(base);
    const u32 b0 = gx < (u32)IMG ? base : 0x80000000u, b1 = gx + 1 < (u32)IMG ? base + CH * 2 : 0x80000000u;
    xdst[it] = L_X + ((Y >> 1) * 10 + X) * X_REC + o * 16 + (Y & 1) * 8;
#pragma unroll
    for (int k = 0; k < 4; k++) xo[k] = gy + (k >> 1) < (u32)IMG ? ((k & 1) ? b1 : b0) + (k >> 1) * (IMG * CH * 2) : 0x80000000u;
  };
  item_offsets(0);
  u32 pfo = (idx * 18 + lane) * 128;
  asm volatile("" : "+v"(dwo[0]), "+v"(dwo[1]), "+v"(xo[0]), "+v"(xo[1]), "+v"(xo[2]), "+v"(xo[3]), "+v"(pfo));
  SB();
  asm volatile("s_waitcnt lgkmcnt(0)" : "+s"(karg));
  gcu16* const x = (gcu16*)karg[0]; gcu16* const dw = (gcu16*)karg[1]; gcu16* const w1 = (gcu16*)karg[2]; gcu16* const w2 = (gcu16*)karg[3];
  gcu16* const grn_g = (gcu16*)karg[4]; gcu16* const grn_b = (gcu16*)karg[5]; gu16* const out = (gu16*)karg[6]; gu64* const ws = (gu64*)karg[7];
  const rsrc_t xrs = make_rsrc(x, X_BYTES);
  u32x2 dv[2][7];
  if (w < 3) {
    const rsrc_t drs = make_rsrc(dw, 49 * CH * 2);
#pragma unroll
    for (int i = 0; i < 14; i++) dv[i / 7][i % 7] = __builtin_amdgcn_raw_buffer_load_b64(drs, dwo[i / 7] + (i % 7) * (CH * 2), 0, 0);
  }
  u32x4 xin[2][4];
  auto load_item = [&](int it) {
#pragma unroll
    for (int k = 0; k < 4; k++) xin[it][k] = ld128(xrs, xo[k]);
  };
  auto store_item = [&](int it) {
#pragma unroll
    for (int e = 0; e < 8; e++) {
      const u32x4* v = xin[it];
      *(volatile LDS_AS u32x2*)(L + xdst[it] + e * 192) = (e & 1) ? u32x2{perm_hi(v[0][e >> 1], v[1][e >> 1]), perm_hi(v[2][e >> 1], v[3][e >> 1])}
                                                                  : u32x2{perm_lo(v[0][e >> 1], v[1][e >> 1]), perm_lo(v[2][e >> 1], v[3][e >> 1])};
    }
  };
  load_item(0);
  if (w == 1 && lane < 36) warm = *(const __attribute__((address_space(1))) u32*)((gchar*)(lane < 18 ? w1 : w2 - 18 * 64) + pfo);
  SB();
  if (w < 7) item_offsets(1);
  const u32x4 w1rs = {(u32)(u64)w1, (u32)((u64)w1 >> 32) & 0xffffu, CH * HID * 2, 0x00020000};
  const u32 w1d = (w / 6) * 3 * 6144 + (w % 6) * 1024;
  const u32 k1 = 16 * (w % 6) + (lane >> 2);
  u32 w1o = __umul24(pos_channel(k1 ^ (((k1 >> 3) & 1) << 2)), HID * 2) + (w / 6) * 384 + (lane & 3) * 16;
  u32 tp = L_T + (16 * cg + (cg >= 3 ? 4 : 0) + (lane >> 2) + 100 * (lane & 3)) * T_ROW;
  u32 xb = L_X + (4 * xh + (lane & 3)) * X_REC + (16 * cg + (lane >> 2)) * 16, xb2 = xb + 40 * X_REC;
  u32 vb = 4 * xh + (lane & 3) < 7 ? L_V + (16 * cg + (lane >> 2)) * V_ROW + (4 * xh + (lane & 3)) * 8 : L_HREM + lane * 8;
  asm volatile("" : "+v"(w1o), "+v"(tp), "+v"(xb), "+v"(xb2), "+v"(vb));
  const int T = w >> 1, Hh = w & 1, g4 = lane >> 4, q4 = (lane >> 2) & 3, p4 = lane & 3;
  enum { A_LN0, A_LN1, A_YD, A_DIAG, A_WB, A_WE, A_Y, A_SS, A_SW, A_SR0, A_SR1, A_WR, A_YR, A_W2V, A_OOFF, A_HR, A_N };
  u32 adr[A_N];
  auto make_adr = [&](int i) {
    u32 v;
    switch (i) {
      case A_LN0: v = L_V + (8 * g4 + 4 * (g4 & 1) + q4) * V_ROW + (4 * w + p4) * 8; break;
      case A_LN1: v = (g4 & 1) ? adr[A_LN0] - 4 * V_ROW : adr[A_LN0] + 4 * V_ROW; break;
      case A_YD: v = L_Y + T * 6144 + (g4 * 32 + 16 * Hh + (lane & 15)) * 16; break;
      case A_DIAG: v = ((lane & 15) + (lane & 12) * 4) * 4; break;
      case A_WB: v = (8 * (g4 >> 1) + q4) * 64 + (g4 & 1) * 32 + p4 * 8 + Hh * 3 * 6144; break;
      case A_WE: v = adr[A_WB] + L_W1E; break;
      case A_Y: v = L_Y + T * 6144 + (lane ^ (16 * Hh)) * 16; break;
      case A_SS: v = L_SS + w * SS_W + (lane & 31) * 72 + (lane >> 5) * 8; break;
      case A_SW: v = L_SCR + w * 1024 + half_tile_put(lane); break;
      case A_SR0: v = L_SCR + w * 1024 + half_tile_get(lane); break;
      case A_SR1: v = (adr[A_SR0] ^ 16) + 256; break;
      case A_WR: v = (T < 3 ? L_W1E : L_W1L) + (Hh * 3 + T % 3) * 6144 + (8 * g4 + q4) * 64 + p4 * 8; break;
      case A_YR: v = L_YR + (g4 * 4 + p4) * 16; break;
      case A_W2V: v = (32 * w + 4 * ((lane >> 2) & 7) + (lane >> 5)) * (CH * 2) + p4 * 16; break;
      case A_OOFF: {
        const u32 pi = 4 * w + q4, py = (pi * 37) >> 8;
        v = (u32)__mul24((y0 + 2 * py + (p4 >> 1)) * IMG + x0 + 2 * (pi - 7 * py) + (p4 & 1), CH * 2) + g4 * 16;
      } break;
      default: v = L_HREM + (Hh * 24 + T * 4 + (lane >> 3)) * 64 + (lane & 7) * 2; break;
    }
    PIN(v);
    adr[i] = v;
  };
#pragma unroll
  for (int i = 0; i < A_N; i++) make_adr(i);
  SB();
  if (w < 3) {
#pragma unroll
    for (int e = 0; e < 4; e++) {
      u32 q[2][2][4];
#pragma unroll
      for (int h = 0; h < 2; h++) {
        u32 v[7];
#pragma unroll
        for (int j = 0; j < 7; j++) v[j] = dv[h][j][e >> 1];
#pragma unroll
        for (int sx = 0; sx < 3; sx++) {
          q[0][h][sx] = (e & 1) ? perm_hi(v[2 * sx], v[2 * sx + 1]) : perm_lo(v[2 * sx], v[2 * sx + 1]);
          q[1][h][sx + 1] = (e & 1) ? perm_hi(v[2 * sx + 1], v[2 * sx + 2]) : perm_lo(v[2 * sx + 1], v[2 * sx + 2]);
        }
        q[0][h][3] = (e & 1) ? v[6] >> 16 : v[6] & 0xffffu;
        q[1][h][0] = (e & 1) ? v[0] & 0xffff0000u : v[0] << 16;
      }
      LDS_AS char* d = L + L_T + (52 * (c4 & 1) + 12 * e + (c4 >> 1) + 200 * (tq >> 2)) * T_ROW + (tq & 3) * 32;
#pragma unroll
      for (int ox = 0; ox < 2; ox++) {
        *(LDS_AS u32x4*)(d + ox * 100 * T_ROW) = u32x4{q[ox][0][0], q[ox][1][0], q[ox][0][1], q[ox][1][1]};
        *(LDS_AS u32x4*)(d + ox * 100 * T_ROW + 16) = u32x4{q[ox][0][2], q[ox][1][2], q[ox][0][3], q[ox][1][3]};
      }
    }
  }
  if (t == NT - 1) *(LDS_AS u32x4*)(L + L_CNT) = u32x4{0, 0, 0, 0};
  asm volatile("" : "+v"(xin[0][0]), "+v"(xin[0][1]), "+v"(xin[0][2]), "+v"(xin[0][3]));
  if (w < 7) load_item(1);
  SB();
  store_item(0);
  bar_lds();

  u64 epoch;
  {
    s16x8 wq[8], d[4][5];
    auto load_x = [&](int sx, int yp) {
      d[sx][yp] = yp < 4 ? *(volatile LDS_AS s16x8*)(L + xb + (yp * 10 + sx) * X_REC) : *(volatile LDS_AS s16x8*)(L + xb2 + sx * X_REC);
    };
    auto load_w = [&](int i) { wq[i] = *(volatile LDS_AS s16x8*)(L + tp + i * 16); };
    auto half = [](s16x8 v, int h) { return h ? __builtin_shufflevector(v, v, 4, 5, 6, 7) : __builtin_shufflevector(v, v, 0, 1, 2, 3); };
    auto conv_mfma = [&](f32x4& acc, int sx, int sy, int Yo) {
      acc = __builtin_amdgcn_mfma_f32_4x4x4bf16_1k(half(wq[2 * sy + (sx >> 1)], sx & 1), half(d[sx][(Yo + sy) >> 1], (Yo + sy) & 1), acc, 0, 0, 0);
    };
    auto put_row = [&](const f32x4& r, int Yo) { *(LDS_AS u32x2*)(L + vb + Yo * 56) = u32x2{pack_bf16(r[0], r[1]), pack_bf16(r[2], r[3])}; };
    load_w(0); load_x(0, 0); load_x(0, 1); load_w(2); load_w(4); load_x(0, 2); load_w(6);
    f32x4 acc[7];
#pragma unroll
    for (int i = 0; i < 7; i++) acc[i] = f32x4{0.f, 0.f, 0.f, 0.f};
    SB();
    static_for<16>([&](auto ic) {
      constexpr int i = ic.value, sx = i >> 2, sy = i & 3;
#pragma unroll
      for (int Yo = 0; Yo + sy < 6; Yo++) conv_mfma(acc[Yo], sx, sy, Yo);
      if constexpr (i < 9) load_x(1 + i / 3, i % 3);
      if constexpr (i < 4) load_w(2 * i + 1);
      if constexpr (i == 8) { if (w >= 7) arrive(C_STAGED); }
      SB();
    });
    if (w < 7) {
      if (t < 432) store_item(1);
      arrive(C_STAGED);
    }
    await(C_STAGED);
#pragma unroll
    for (int i = 0; i < 8; i++) load_x(i >> 1, 3 + (i & 1));
    epoch = ws_load(ws, WS_EPOCH);
#pragma unroll
    for (int Yo = 0; Yo < 3; Yo++) put_row(acc[Yo], Yo);
    SB();
    static_for<40>([&](auto ic) {
      constexpr int i = ic.value, sx = i / 10, j = i % 10;
      constexpr int yo[10] = {6, 5, 4, 6, 3, 5, 6, 4, 5, 6}, sy[10] = {0, 1, 2, 1, 3, 2, 2, 3, 3, 3};
      conv_mfma(acc[yo[j]], sx, sy[j], yo[j]);
      if constexpr (i == 4 || i == 12 || i == 20) dma16(w1rs, w1o, (i / 8) * 64, L_W1E + w1d + (i / 8) * 6144);
      if constexpr (i == 37) put_row(acc[3], 3);
      SB();
    });
#pragma unroll
    for (int Yo = 4; Yo < 7; Yo++) put_row(acc[Yo], Yo);
  }
  bar_lds();

  const bool bias_wave = w == 4 && bid < CH;
  u32 bw[6], bb[6];
  if (bias_wave) {
#pragma unroll
    for (int i = 0; i < 6; i++) {
      bw[i] = __builtin_amdgcn_raw_buffer_load_b16(make_rsrc(w2, HID * CH * 2), lane * (CH * 2), bid * 2 + i * (64 * CH * 2), 0);
      bb[i] = __builtin_amdgcn_raw_buffer_load_b16(make_rsrc(grn_b, HID * 2), lane * 2 + i * 128, 0, 0);
    }
  }
  SB();
  layer_norm_unit(L, adr[A_LN0], adr[A_LN1], adr[A_YD], adr[A_DIAG]);
  if (w == LN_LAST) layer_norm_last(L, lane);
  asm volatile("s_waitcnt vmcnt(0)" :: "v"(warm) : "memory");
  epoch = ((u64)(u32)__builtin_amdgcn_readfirstlane((int)(epoch >> 32)) << 32) | (u32)__builtin_amdgcn_readfirstlane((int)epoch);
  const u64 tag = (epoch + 1) & ((1ull << 40) - 1);
  float bias_val = 0.f;
  if (bias_wave) {
    float a = 0.f;
#pragma unroll
    for (int i = 0; i < 6; i++) { asm volatile("" : "+v"(bw[i]), "+v"(bb[i])); a = __builtin_fmaf(bf_lo(bw[i]), bf_lo(bb[i]), a); }
    bias_val = __int_as_float(__builtin_amdgcn_readfirstlane(__float_as_int(wave_sum(a))));
  }
  bar_lds();

  s16x8 hk[6], hs[6];
  const int rq = T / 3, otj[3] = {T % 3, (T + 1) % 3, (T + 2) % 3};
  const u32 totw = WS_TOT + (bid & 7) * TOT_ROW;
  const u32 ooff = adr[A_OOFF];
  u32x4 xr[3];
  u64 bgr;
  const f32x16 zero16 = {0.f, 0.f, 0.f, 0.f, 0.f, 0.f, 0.f, 0.f, 0.f, 0.f, 0.f, 0.f, 0.f, 0.f, 0.f, 0.f};
  {
    LDS_AS char* wE = L + adr[A_WE];
    LDS_AS char* wL = L + adr[A_WB] + L_W1L;
    LDS_AS char* ss = L + adr[A_SS];
    LDS_AS char* wr = L + adr[A_WR];
    LDS_AS char* yr = L + adr[A_YR];
    s16x8 yf[6], wf[6], ry[3], rw[3];
    f32x4 racc[2] = {{0.f, 0.f, 0.f, 0.f}, {0.f, 0.f, 0.f, 0.f}};
    float ssv[6];
    f32x16 acc = zero16;
    __builtin_amdgcn_s_setprio(3);
#pragma unroll
    for (int k = 0; k < 6; k++) { yf[k] = *(LDS_AS s16x8*)(L + adr[A_Y] + k * 1024); wf[k] = tr_frag(wE + k * 1024, 256); }
    SB();
#pragma unroll
    for (int u = 0; u < 3; u++) dma16(w1rs, w1o, 192 + u * 64, L_W1L + w1d + u * 6144);
    SB();
#pragma unroll
    for (int k = 0; k < 6; k++) {
      acc = mfma32(yf[k], wf[k], acc);
      wf[k] = tr_frag(wE + 6144 + k * 1024, 256);
      SB();
    }
    const u32 w2raw = wave_slot(w);
    auto fetch_w2 = [&](int c) {
      await(c);
      const u32x4 rs = {(u32)(u64)w2, (u32)((u64)w2 >> 32) & 0xffffu, HID * CH * 2, 0x00020000};
#pragma unroll
      for (int j = 0; j < 6; j++) dma16(rs, adr[A_W2V], (j & 1) * 384 + (j >> 1) * 64, w2raw + j * 1024);
    };
    asm volatile("s_waitcnt vmcnt(0)" ::: "memory");
    arrive(C_YFREE);
    static_for<6>([&](auto sc_) {
      constexpr int s = sc_.value, NP = s == 3 ? 10 : 8;
      LDS_AS char* wp2 = (s + 2 < 3 ? wE : wL) + ((s + 2) % 3) * 6144;
      __builtin_amdgcn_s_setprio((G1_PRIO >> (2 * s)) & 3);
      if constexpr (s == 0) { if (bias_wave && lane < 8) ws_store(ws, WS_TOT + lane * TOT_ROW + TOT_BIAS + bid, granule(tag, bias_val)); }
      if constexpr (s == 3) arrive(C_W1EFREE);
      if constexpr (s == 1) { if (w < 6) fetch_w2(C_YFREE); else await(C_YFREE); }
      if constexpr (s == 4) { if (w >= 6) fetch_w2(C_W1EFREE); }
      if constexpr (s == 1 || s == 2) {
#pragma unroll
        for (int j = 0; j < 3; j++) {
          if (s == 1) ry[j] = *(LDS_AS s16x8*)(yr + j * 256);
          rw[j] = tr_frag(wr + j * 2048 + (s - 1) * 32, 256);
        }
      }
      f32x2 xv[NP], z[NP];
#pragma unroll
      for (int i = 0; i < NP; i++) {
        if (i < 8) xv[i] = f32x2{acc[2 * (i & 7)], acc[2 * (i & 7) + 1]};
        else xv[i] = __builtin_amdgcn_inverse_ballot_w64(0xffffull) ? f32x2{racc[0][2 * (i & 1)], racc[0][2 * (i & 1) + 1]} : f32x2{racc[1][2 * (i & 1)], racc[1][2 * (i & 1) + 1]};
        z[i] = __builtin_elementwise_fma(xv[i], xv[i], f32x2{GK, GK});
      }
#pragma unroll
      for (int i = 0; i < NP; i++) PIN(z[i]);
      SB();
#pragma unroll
      for (int i = 0; i < NP; i++) z[i] *= xv[i];
#pragma unroll
      for (int i = 0; i < NP; i++) PIN(z[i]);
      SB();
      f32x16 nxt = zero16;
      float e[2 * NP];
#pragma unroll
      for (int i = 0; i < 2 * NP; i++) {
        constexpr bool rem = s == 1 || s == 2;
        if (s < 5 && i % 3 == 0 && i < 9) { nxt = mfma32(yf[i / 3], wf[i / 3], nxt); PIN(nxt); SB(); }
        if (rem && (i == 12 || i == 13)) { racc[(s - 1) & 1] = mfma16(ry[i - 12], rw[i - 12], racc[(s - 1) & 1]); PIN(racc[(s - 1) & 1]); SB(); }
        if (s == 5 && i % 3 == 0 && i < 9) { xr[i / 3] = __builtin_amdgcn_raw_buffer_load_b128(xrs, ooff, otj[i / 3] * 64, 0); SB(); }
        if (s == 5 && i == 9) { bgr = ws_load(ws + (totw + TOT_BIAS + (w & 1) * 64), lane); SB(); }
        e[i] = __builtin_amdgcn_exp2f(-z[i >> 1][i & 1]);
        PIN(e[i]);
      }
      SB();
      if constexpr (s < 4) {
#pragma unroll
        for (int k = 0; k < 3; k++) wf[k] = tr_frag(wp2 + k * 1024, 256);
      }
#pragma unroll
      for (int i = 0; i < NP; i++) z[i] = f32x2{e[2 * i], e[2 * i + 1]} + 1.0f;
#pragma unroll
      for (int i = 0; i < NP; i++) PIN(z[i]);
      SB();
      float rc[2 * NP];
#pragma unroll
      for (int i = 0; i < 2 * NP; i++) {
        if (s < 5 && i % 3 == 0 && i < 9) { nxt = mfma32(yf[3 + i / 3], wf[3 + i / 3], nxt); PIN(nxt); SB(); }
        if ((s == 1 || s == 2) && i == 9) { racc[(s - 1) & 1] = mfma16(ry[2], rw[2], racc[(s - 1) & 1]); PIN(racc[(s - 1) & 1]); SB(); }
        rc[i] = __builtin_amdgcn_rcpf(z[i >> 1][i & 1]);
        PIN(rc[i]);
      }
      SB();
      if constexpr (s < 4) {
#pragma unroll
        for (int k = 3; k < 6; k++) wf[k] = tr_frag(wp2 + k * 1024, 256);
      }
#pragma unroll
      for (int i = 0; i < NP; i++) xv[i] *= f32x2{rc[2 * i], rc[2 * i + 1]};
#pragma unroll
      for (int i = 0; i < NP; i++) PIN(xv[i]);
      SB();
      u32 hp8[8];
#pragma unroll
      for (int i = 0; i < 8; i++) { hp8[i] = pack_bf16(xv[i].x, xv[i].y); PIN(hp8[i]); }
      SB();
      *(volatile LDS_AS u32x4*)(L + adr[A_SW]) = u32x4{hp8[0], hp8[1], hp8[2], hp8[3]};
      SB();
      ssv[s] = sum_sq8(hp8);
      hk[s] = __builtin_shufflevector(tr_read(L + adr[A_SR0]), tr_read(L + adr[A_SR1]), 0, 1, 2, 3, 4, 5, 6, 7);
      hs[s] = __builtin_bit_cast(s16x8, u32x4{hp8[4], hp8[5], hp8[6], hp8[7]});
      if constexpr (s == 3) {
        const f32x2 sq = __builtin_elementwise_fma(xv[8], xv[8], xv[9] * xv[9]);
        if (lane < 32) {
          const u32 p01 = pack_bf16(xv[8].x, xv[8].y), p23 = pack_bf16(xv[9].x, xv[9].y);
          LDS_AS unsigned short* hr = (LDS_AS unsigned short*)(L + adr[A_HR]);
          hr[0] = (unsigned short)p01; hr[8] = (unsigned short)(p01 >> 16); hr[16] = (unsigned short)p23; hr[24] = (unsigned short)(p23 >> 16);
          *(LDS_AS float*)(ss + 64) = sq.x + sq.y;
        }
      }
      acc = nxt;
      SB();
    });
#pragma unroll
    for (int i = 0; i < 3; i++) *(volatile LDS_AS f32x2*)(ss + i * 16) = f32x2{ssv[2 * i], ssv[2 * i + 1]};
  }
  asm volatile("" : "+v"(xr[0]), "+v"(xr[1]), "+v"(xr[2]), "+v"(bgr));
  bar_all();

  if (t < HID) {
    float v[13];
    LDS_AS char* q = L + L_SS + (w / 3) * SS_W + (lane & 31) * 72;
#pragma unroll
    for (int i = 0; i < 12; i++) v[i] = *(LDS_AS float*)(q + (w % 3) * 16 + (lane >> 5) * 4 + (i >> 1) * 2 * SS_W + (i & 1) * 8);
    v[12] = *(LDS_AS float*)(q + (2 * (w % 3) + (lane >> 5)) * 2 * SS_W + 64);
    const float s = (((v[0] + v[1]) + (v[2] + v[3])) + ((v[4] + v[5]) + (v[6] + v[7]))) + (((v[8] + v[9]) + (v[10] + v[11])) + v[12]);
    ws_store(ws, WS_PART + (((u32)t >> 4) * 256 + bid) * 16 + (t & 15), granule(tag, s));
  }
  u32x2 w2r[12];
  u32 ap[4], ab[2], hp, rw, w2d0, w2d1;
  f32x2 w2f[24];
  auto unpack_w2 = [&](int i) {
    const int ot = i >> 3, r = (i >> 1) & 3, kp = i & 1;
    const u32 a = w2r[4 * ot + 2 * kp][r >> 1], b = w2r[4 * ot + 2 * kp + 1][r >> 1];
    w2f[i] = (r & 1) ? f32x2{bf_hi(a), bf_hi(b)} : f32x2{bf_lo(a), bf_lo(b)};
    PIN(w2f[i]);
  };
  auto swap_out = [&]() {
    SB();
#pragma unroll
    for (int i = 0; i < 12; i++) w2r[i] = *(volatile LDS_AS u32x2*)(L + wave_slot(w) + lane * 8 + i * 512);
#pragma unroll
    for (int i = 0; i < 6; i++) *(volatile LDS_AS s16x8*)(L + (adr[A_SW] - (L_SCR + w * 1024) + wave_slot(w)) + i * 1024) = hs[i];
    if (t == 512) *(LDS_AS u32*)(L + L_FL) = 0;
    SB();
    const u32 w2d = L_W2 + ((t & 1) * 12 + w) * 1024 + (16 * g4 + ((t >> 1) & 3)) * 16 + (t & 8);
    const u32 aslot = L_W2 + (16 * g4 + q4) * 16;
    ab[0] = aslot + p4 * 64; ab[1] = aslot + (p4 ^ 1) * 64 + 12288;
#pragma unroll
    for (int i = 0; i < 4; i++) ap[i] = (((i >> 1) ^ rq) ? ab[1] : ab[0]) + (otj[0] * 24576 + ((i & 1) ^ Hh) * 6144);
    hp = L_HREM + (Hh * 24 + g4) * 64 + p4 * 16;
    rw = L_RP + (Hh * 4 * CH + 32 * otj[0] + 4 * rq) * 4 + p4 * (CH * 4) + g4 * 32;
    w2d0 = w2d + (t & 1) * 64; w2d1 = w2d + ((t & 1) ^ 1) * 64;
    asm volatile("" : "+v"(ap[0]), "+v"(ap[1]), "+v"(ap[2]), "+v"(ap[3]), "+v"(ab[0]), "+v"(ab[1]), "+v"(hp), "+v"(rw), "+v"(w2d0), "+v"(w2d1));
#pragma unroll
    for (int i = 0; i < W2_EARLY; i++) unpack_w2(i);
    SB();
  };
  {
    const bool mine = (u32)(t - HID) < CH;
    if (!__all(!mine || (bgr >> 24) == tag)) {
#pragma unroll 1
      for (int spin = 0; spin < SPIN_CAP; spin++) {
        bgr = ws_load(ws + (totw + TOT_BIAS + (w & 1) * 64), lane);
        if (__all(!mine || (bgr >> 24) == tag)) break;
      }
    }
    if (mine) *(LDS_AS float*)(L + L_BP + (t - HID) * 4) = granule_val(bgr);
  }
  if (bid < 24) {
    u64 gr[6];
    const u32 partw = WS_PART + bid * 4096 + t;
#pragma unroll
    for (int i = 0; i < 6; i++) gr[i] = ws_load(ws, partw + (i < 5 || t < 256 ? i * NT : 0));
    swap_out();
#pragma unroll 1
    for (int spin = 0; spin < SPIN_CAP; spin++) {
      bool ok = true;
#pragma unroll
      for (int i = 0; i < 6; i++) ok &= (gr[i] >> 24) == tag;
      if (__all(ok)) break;
#pragma unroll
      for (int i = 0; i < 6; i++)
        if ((gr[i] >> 24) != tag) gr[i] = ws_load(ws, partw + (i < 5 || t < 256 ? i * NT : 0));
    }
    float s = ((granule_val(gr[0]) + granule_val(gr[1])) + (granule_val(gr[2]) + granule_val(gr[3]))) + (granule_val(gr[4]) + (t < 256 ? granule_val(gr[5]) : 0.f));
    s = sum_xor32(sum_xor16(s));
    if (lane < 16) *(LDS_AS float*)(L + L_RED + (lane * 12 + w) * 4) = s;
  } else {
    swap_out();
  }
  bar_lds();
  if (bid < 24 && w < 2) {
    LDS_AS char* q = L + L_RED + (t & 15) * 48;
    const f32x4 a = *(LDS_AS f32x4*)(q) + *(LDS_AS f32x4*)(q + 16) + *(LDS_AS f32x4*)(q + 32);
    const float gx = __builtin_amdgcn_sqrtf(__builtin_fmaf((a[0] + a[1]) + (a[2] + a[3]), GA_INV2, 1e-6f));
    ws_store(ws, WS_TOT + (t >> 4) * TOT_ROW + bid * 16 + (t & 15), granule(tag, gx));
  }
  SB();
#pragma unroll
  for (int i = 0; i < 6; i++)
    hs[i] = __builtin_shufflevector(tr_read(L + (adr[A_SR0] - (L_SCR + w * 1024) + wave_slot(w ^ 1)) + i * 1024),
                                    tr_read(L + (adr[A_SR1] - (L_SCR + w * 1024) + wave_slot(w ^ 1)) + i * 1024), 0, 1, 2, 3, 4, 5, 6, 7);
  const u32 remo = lane < 48 ? (u32)(((y0 + 12) * IMG + x0 + 12) * (CH * 2)) + (lane >= 24 ? IMG * CH * 2 - 384 : 0) + lane * 16 : 0x80000000u;
  u32x4 xrem = ld128(xrs, remo);
  u32 gam = grn_g[t < HID ? t : 0];
  f32x4 acc2[3][2];
#pragma unroll
  for (int i = 0; i < 6; i++) acc2[i >> 1][i & 1] = *(LDS_AS f32x4*)(L + L_BP + g4 * 32 + otj[i >> 1] * 128 + ((i & 1) ^ (i < 2 ? rq : 0)) * 16);
  SB();
#pragma unroll
  for (int i = 0; i < 6; i++) {
    const u32x4 xv = i < 2 && rq ? u32x4{xr[0][2], xr[0][3], xr[0][0], xr[0][1]} : xr[i >> 1];
    acc2[i >> 1][i & 1] += f32x4{bf_lo(xv[2 * (i & 1)]), bf_hi(xv[2 * (i & 1)]), bf_lo(xv[2 * (i & 1) + 1]), bf_hi(xv[2 * (i & 1) + 1])};
    asm volatile("" : "+v"(acc2[i >> 1][i & 1]));
  }
  SB();
  const f32x4 zero4 = {0.f, 0.f, 0.f, 0.f};
#pragma unroll
  for (int i = W2_EARLY; i < 24; i++) unpack_w2(i);
#pragma unroll
  for (int i = 0; i < 6; i++) asm volatile("" : "+v"(hs[i]));
  if (t >= HID) PIN(xrem);

  if (t < HID) {
    u64 g;
#pragma unroll 1
    for (int spin = 0; spin < SPIN_CAP; spin++) {
      g = ws_load(ws, totw + t);
      if (__all((g >> 24) == tag)) break;
    }
    PIN(xrem);
    const float gx = granule_val(g);
    *(LDS_AS float*)(L + L_GG + t * 4) = bf_lo(gam) * gx;
    const float s = wave_sum(gx);
    if (lane == 0) *(LDS_AS float*)(L + L_PS + w * 4) = s;
  }
  bar_lds();
  {
    const f32x4 pa = *(LDS_AS f32x4*)(L + L_PS);
    const f32x2 pb = *(LDS_AS f32x2*)(L + L_PS + 16);
    const f32x4 gg = *(LDS_AS f32x4*)(L + L_GG + (t >> 3) * 16);
    if (bid == 0 && t == NT - 1) ws_store(ws, WS_EPOCH, epoch + 1);
    const float mean = (((pa[0] + pa[1]) + (pa[2] + pa[3])) + (pb[0] + pb[1])) * (1.0f / HID);
    const float inv = GA_INV * __builtin_amdgcn_rcpf(mean + 1e-6f);
    const f32x2 s01 = f32x2{gg[0], gg[1]} * inv + GA_INV, s23 = f32x2{gg[2], gg[3]} * inv + GA_INV;
#pragma unroll
    for (int i = 0; i < 12; i++) {
      const f32x2 a = w2f[2 * i] * s01, b = w2f[2 * i + 1] * s23;
      *(volatile LDS_AS u32x2*)(L + (i & 1 ? w2d1 : w2d0) + ((i >> 1) & 1) * 128 + (i >> 2) * 24576) = u32x2{pack_bf16(a.x, a.y), pack_bf16(b.x, b.y)};
    }
  }

  {
    const rsrc_t ors = make_rsrc(out, X_BYTES);
    constexpr int PF = 3;
    s16x8 af[72], rb[6];
    f32x4 ra = zero4;
    u32 rem_cnt = 0;
    auto load_a = [&](int i) {
      if (i % 24 == 0 && i) {
#pragma unroll
        for (int j = 0; j < 4; j++) ap[j] = ab[j >> 1] + (otj[i / 24] * 24576 + ((j & 1) ^ Hh) * 6144);
      }
      af[i] = *(volatile LDS_AS s16x8*)(L + ap[(i / 6) & 3] + (i % 6) * 1024);
    };
    auto store_tile = [&](int j) {
      const u32x2 a = {pack_bf16(acc2[j][0][0], acc2[j][0][1]), pack_bf16(acc2[j][0][2], acc2[j][0][3])};
      const u32x2 b = {pack_bf16(acc2[j][1][0], acc2[j][1][1]), pack_bf16(acc2[j][1][2], acc2[j][1][3])};
      __builtin_amdgcn_raw_buffer_store_b128(j == 0 && rq ? u32x4{b.x, b.y, a.x, a.y} : u32x4{a.x, a.y, b.x, b.y}, ors, ooff, otj[j] * 64, ST_AUX);
    };
    bar_lds();
    __builtin_amdgcn_s_setprio(3);
#pragma unroll
    for (int i = 0; i < PF; i++) load_a(i);
    SB();
    static_for<72>([&](auto ic) {
      constexpr int i = ic.value, ot = i / 24, rt = (i / 12) & 1, k = i % 12;
      acc2[ot][rt] = mfma16(af[i], k < 6 ? hk[k % 6] : hs[k % 6], acc2[ot][rt]);
      if constexpr (i + PF < 72) load_a(i + PF);
      if constexpr (i % 18 == 17 && i < 60) __builtin_amdgcn_s_setprio(2 - i / 18);
      if constexpr (i < 12 && (i & 1)) rb[i >> 1] = *(volatile LDS_AS s16x8*)(L + hp + (i >> 1) * 256);
      if constexpr (i >= 6 && i < 18 && (i & 1) == 0) ra = mfma16(af[(i - 6) >> 1], rb[(i - 6) >> 1], ra);
      if constexpr (i == 19) {
        if (q4 == 0) *(volatile LDS_AS f32x4*)(L + rw) = ra;
        u32 fl = L_FL;
        PIN(fl);
        if (lane == 0) rem_cnt = __hip_atomic_fetch_add((LDS_AS u32*)(L + fl), 1u, __ATOMIC_RELAXED, __HIP_MEMORY_SCOPE_WORKGROUP);
      }
      if constexpr (i == 25 || i == 49) store_tile(ot - 1);
      if constexpr (i == 40) {
        if (__builtin_amdgcn_readfirstlane(rem_cnt) == 11 && lane < 48) {
          f32x4 xq[2];
#pragma unroll
          for (int j = 0; j < 2; j++) {
            const f32x4 a = *(volatile LDS_AS f32x4*)(L + L_RP + (lane * 8 + j * 4) * 4), b = *(volatile LDS_AS f32x4*)(L + L_RP + (4 * CH + lane * 8 + j * 4) * 4);
            const f32x4 c = *(volatile LDS_AS f32x4*)(L + L_BP + (lane - 12 * ((lane * 43) >> 9)) * 32 + j * 16);
#pragma unroll
            for (int e = 0; e < 4; e++) xq[j][e] = add1(add1((e & 1) ? bf_hi(xrem[2 * j + (e >> 1)]) : bf_lo(xrem[2 * j + (e >> 1)]), c[e]), add1(a[e], b[e]));
          }
          __builtin_amdgcn_raw_buffer_store_b128(u32x4{pack_bf16(xq[0][0], xq[0][1]), pack_bf16(xq[0][2], xq[0][3]), pack_bf16(xq[1][0], xq[1][1]), pack_bf16(xq[1][2], xq[1][3])}, ors, remo, 0, ST_AUX);
        }
      }
      SB();
    });
    store_tile(2);
  }
}

extern "C" void kernel_launch(void* const* d_in, const int* in_sizes, int n_in,
                              void* d_out, int out_size, void* d_ws, size_t ws_size,
                              hipStream_t stream) {
  (void)in_sizes; (void)n_in; (void)out_size; (void)ws_size;
  (void)hipFuncSetAttribute((const void*)slak_block_kernel, hipFuncAttributeMaxDynamicSharedMemorySize, LDS_BYTES);
  slak_block_kernel<<<256, NT, LDS_BYTES, stream>>>(
      (gcu16*)d_in[0], (gcu16*)d_in[1], (gcu16*)d_in[2], (gcu16*)d_in[3], (gcu16*)d_in[4], (gcu16*)d_in[5], (gu16*)d_out, (gu64*)d_ws);
}
```

```cpp
#include <hip/hip_runtime.h>
#include <stdint.h>

typedef unsigned long long u64;
typedef unsigned u32;
typedef float f32x2 __attribute__((ext_vector_type(2)));
typedef float f32x4 __attribute__((ext_vector_type(4)));
typedef float f32x16 __attribute__((ext_vector_type(16)));
typedef short s16x4 __attribute__((ext_vector_type(4)));
typedef short s16x8 __attribute__((ext_vector_type(8)));
typedef __bf16 bf16x2 __attribute__((ext_vector_type(2)));
typedef u32 u32x2 __attribute__((ext_vector_type(2)));
typedef u32 u32x4 __attribute__((ext_vector_type(4)));
typedef u64 u64x8 __attribute__((ext_vector_type(8)));
#define LDS_AS __attribute__((address_space(3)))
typedef __attribute__((address_space(1))) u64 gu64;
typedef __attribute__((address_space(1))) unsigned short gu16;
typedef __attribute__((address_space(1))) const unsigned short gcu16;
#define RLX_AGENT __ATOMIC_RELAXED, __HIP_MEMORY_SCOPE_AGENT
#define DI __device__ __forceinline__

constexpr int IMG = 224, CH = 96, HID = 384, NT = 768;
constexpr u32 X_BYTES = IMG * IMG * CH * 2;

constexpr float GA = 0.4686686930532063f, GK = 4.91222953926424f;
constexpr float GA_INV = 2.1337034344781243f, GA_INV2 = 4.552690346303743f;

constexpr u32 L_X = 0, X_REC = 1600;
constexpr u32 L_Y = 0, L_YR = 36864;
constexpr u32 L_W1L = 37632;
constexpr u32 L_SS = 74496, SS_W = 2304;
constexpr u32 L_SCR = 102144;
constexpr u32 L_V = 83200, V_ROW = 416;
constexpr u32 L_T = 83200, T_ROW = 144;
constexpr u32 L_W1E = 123136;
constexpr u32 L_HREM = 160000;
constexpr u32 L_CNT = 163072;
constexpr u32 L_W2 = 0;
constexpr u32 L_RP = 114432;
constexpr u32 L_BP = 117504;
constexpr u32 L_GG = 117888, L_PS = 119424, L_RED = 119488;
constexpr u32 L_ROLE = 120272;
constexpr u32 L_FL = 120256;
constexpr u32 LDS_BYTES = 163840;

constexpr u32 TOT_ROW = 512, TOT_BIAS = 384;
constexpr u32 WS_EPOCH = 0, WS_CLAIM = 64, WS_TOT = 512, WS_PART = 512 + 8 * TOT_ROW;
constexpr int SPIN_CAP = 1 << 20;
constexpr int CODE_LINES = 172;
constexpr int ST_AUX = 16;
constexpr int LN_LAST = 2;
constexpr int G1_PRIO = 0x1bf;
constexpr int K_INLOOP = 3;
constexpr int CLAIM_W = 6;

#define SB() __builtin_amdgcn_sched_barrier(0)
#define PIN(v) asm volatile("" : "+v"(v))
template <int V> struct int_c { static constexpr int value = V; };
template <int N, int I = 0, class F> DI void static_for(F&& f) { if constexpr (I < N) { f(int_c<I>{}); static_for<N, I + 1>(f); } }
DI void bar_lds() { asm volatile("s_waitcnt lgkmcnt(0)\n\ts_barrier" ::: "memory"); }
DI void bar_all() { asm volatile("s_waitcnt vmcnt(0) lgkmcnt(0)\n\ts_barrier" ::: "memory"); }
typedef __amdgpu_buffer_rsrc_t rsrc_t;
DI rsrc_t make_rsrc(gcu16* p, u32 bytes) { return __builtin_amdgcn_make_buffer_rsrc((void*)p, 0, bytes, 0x00020000); }
DI u32x4 ld128(rsrc_t r, u32 off) { return __builtin_amdgcn_raw_buffer_load_b128(r, off, 0, 0); }
DI void dma16(u32x4 rs, u32 voff, u32 soff, u32 lds) {
  u32 keep;
  asm volatile("s_mov_b32 %0, m0\n\ts_mov_b32 m0, %1\n\ts_nop 0\n\tbuffer_load_dwordx4 %2, %3, %4 offen lds\n\ts_mov_b32 m0, %0"
               : "=&s"(keep) : "s"(lds), "v"(voff), "s"(rs), "s"(soff) : "memory");
}
DI u32 pack_bf16(float a, float b) { bf16x2 v = __builtin_convertvector(f32x2{a, b}, bf16x2); return __builtin_bit_cast(u32, v); }
DI float bf_lo(u32 v) { return __uint_as_float(v << 16); }
DI float bf_hi(u32 v) { return __uint_as_float(v & 0xffff0000u); }
DI u32 perm_lo(u32 a, u32 b) { return __builtin_amdgcn_perm(b, a, 0x05040100u); }
DI u32 perm_hi(u32 a, u32 b) { return __builtin_amdgcn_perm(b, a, 0x07060302u); }
DI s16x4 tr_read(LDS_AS char* p) { return __builtin_amdgcn_ds_read_tr16_b64_v4i16((LDS_AS s16x4*)p); }
DI s16x8 tr_frag(LDS_AS char* p, int step) {
  s16x4 a = tr_read(p), b = tr_read(p + step);
  return __builtin_shufflevector(a, b, 0, 1, 2, 3, 4, 5, 6, 7);
}
DI f32x16 mfma32(s16x8 a, s16x8 b, f32x16 c) { return __builtin_amdgcn_mfma_f32_32x32x16_bf16(a, b, c, 0, 0, 0); }
DI f32x4 mfma16(s16x8 a, s16x8 b, f32x4 c) { return __builtin_amdgcn_mfma_f32_16x16x32_bf16(a, b, c, 0, 0, 0); }
DI float sum_xor32(float v) { auto r = __builtin_amdgcn_permlane32_swap(__float_as_uint(v), __float_as_uint(v), false, false); return __uint_as_float(r[0]) + __uint_as_float(r[1]); }
DI float sum_xor16(float v) { auto r = __builtin_amdgcn_permlane16_swap(__float_as_uint(v), __float_as_uint(v), false, false); return __uint_as_float(r[0]) + __uint_as_float(r[1]); }
typedef __attribute__((address_space(1))) char gchar;
DI u64 ws_load(gu64* ws, u32 word) { return __hip_atomic_load((gu64*)((gchar*)ws + word * 8u), RLX_AGENT); }
DI void ws_store(gu64* ws, u32 word, u64 v) { __hip_atomic_store((gu64*)((gchar*)ws + word * 8u), v, RLX_AGENT); }
DI u64 granule(u64 tag, float v) { return (tag << 24) | (u64)((__float_as_uint(v) + 0x80u) >> 8); }
DI float granule_val(u64 g) { return __uint_as_float((u32)g << 8); }
template <int CTRL> DI float dpp_add(float v) { return v + __builtin_bit_cast(float, __builtin_amdgcn_update_dpp(0, __builtin_bit_cast(int, v), CTRL, 0xf, 0xf, false)); }
DI float wave_sum(float v) { return dpp_add<0xb1>(dpp_add<0x4e>(dpp_add<0x124>(dpp_add<0x128>(sum_xor32(sum_xor16(v)))))); }
enum { C_YFREE, C_W1EFREE, C_STAGED };
DI void arrive(int c) {
  if (__builtin_amdgcn_inverse_ballot_w64(1ull)) asm volatile("ds_add_u32 %0, %1" :: "v"(L_CNT + 4 * c), "v"(1u) : "memory");
}
DI void await(int c) {
#pragma unroll 1
  for (int spin = 0; spin < SPIN_CAP; spin++) {
    if (__builtin_amdgcn_readfirstlane(*(volatile LDS_AS u32*)((LDS_AS char*)0u + L_CNT + 4 * c)) >= 12) break;
    __builtin_amdgcn_s_sleep(1);
  }
  asm volatile("" ::: "memory");
}
DI u32 half_tile_put(u32 lane) { return ((lane & 19) + ((lane & 4) << 1) + ((lane & 8) >> 1)) * 32 + (((lane >> 5) ^ (lane >> 2)) & 1) * 16; }
DI u32 half_tile_get(u32 lane) { return (((lane >> 2) & 7) + ((lane >> 5) << 4)) * 32 + (lane & 1) * 16 + (lane & 2) * 4; }
DI u32 wave_slot(int w) { return w < 6 ? L_Y + w * 6144 : L_W1E + (w - 6) * 6144; }
DI u32 pos_channel(u32 p) { const u32 e = (p * 43) >> 9; return 8 * (p - 12 * e) + e; }

DI void layer_norm_unit(LDS_AS char* L, u32 src0, u32 src1, u32 dst, u32 diag) {
  LDS_AS char* p0 = L + src0;
  LDS_AS char* p1 = L + src1;
  constexpr u32 dst_step = 2048;
  s16x8 vf[3];
#pragma unroll
  for (int c = 0; c < 3; c++)
    vf[c] = __builtin_shufflevector(tr_read(p0 + c * 32 * V_ROW), tr_read(p1 + c * 32 * V_ROW), 0, 1, 2, 3, 4, 5, 6, 7);
  const s16x8 ones = {0x3f80, 0x3f80, 0x3f80, 0x3f80, 0x3f80, 0x3f80, 0x3f80, 0x3f80};
  f32x4 ms = {0.f, 0.f, 0.f, 0.f}, mq = {0.f, 0.f, 0.f, 0.f};
#pragma unroll
  for (int c = 0; c < 3; c++) { mq = mfma16(vf[c], vf[c], mq); ms = mfma16(ones, vf[c], ms); }
  f32x2 f[12];
#pragma unroll
  for (int i = 0; i < 12; i++) { const u32 v = __builtin_bit_cast(u32x4, vf[i >> 2])[i & 3]; f[i] = f32x2{bf_lo(v), bf_hi(v)}; }
  const bool b0 = __builtin_amdgcn_inverse_ballot_w64(0xaaaaaaaaaaaaaaaaull), b1 = __builtin_amdgcn_inverse_ballot_w64(0xccccccccccccccccull);
  const float d01 = b0 ? mq[1] : mq[0], d23 = b0 ? mq[3] : mq[2], dg = b1 ? d23 : d01;
  const float sq = __int_as_float(__builtin_amdgcn_ds_bpermute(diag, __float_as_int(dg))), sum = ms[0];
  const float mu = sum * (1.0f / CH);
  const float var = fmaxf(__builtin_fmaf(-mu, mu, sq * (1.0f / CH)), 0.f);
  const float rs = GA * __builtin_amdgcn_rsqf(var + 1e-5f);
  const float off = -mu * rs;
#pragma unroll
  for (int c = 0; c < 3; c++) {
    u32x4 o;
#pragma unroll
    for (int i = 0; i < 4; i++) { f32x2 y = __builtin_elementwise_fma(f[4 * c + i], f32x2{rs, rs}, f32x2{off, off}); o[i] = pack_bf16(y.x, y.y); }
    *(LDS_AS u32x4*)(L + dst + c * dst_step) = o;
  }
}

DI void layer_norm_last(LDS_AS char* L, int lane) {
  const int cq = lane >> 2, q = lane & 3, oq = 4 * (lane >> 4) + (lane & 3);
  const u32x2 a = __builtin_bit_cast(u32x2, tr_read(L + L_V + (4 * oq + ((lane >> 2) & 3)) * V_ROW + 48 * 8));
  const u32x2 b = __builtin_bit_cast(u32x2, tr_read(L + L_V + (64 + 4 * (oq & 7) + ((lane >> 2) & 3)) * V_ROW + 48 * 8));
  f32x2 f[4] = {{bf_lo(a.x), bf_hi(a.x)}, {bf_lo(a.y), bf_hi(a.y)}, {bf_lo(b.x), bf_hi(b.x)}, {bf_lo(b.y), bf_hi(b.y)}};
  const float m = cq < 8 ? 1.f : 0.f;
  const f32x2 s = (f[0] + f[1]) + (f[2] + f[3]) * m;
  const f32x2 ss = __builtin_elementwise_fma(f[0], f[0], f[1] * f[1]) + __builtin_elementwise_fma(f[2], f[2], f[3] * f[3]) * m;
  const float sum = dpp_add<0x124>(dpp_add<0x128>(sum_xor32(sum_xor16(s.x + s.y))));
  const float sq = dpp_add<0x124>(dpp_add<0x128>(sum_xor32(sum_xor16(ss.x + ss.y))));
  const float mu = sum * (1.0f / CH);
  const float var = fmaxf(__builtin_fmaf(-mu, mu, sq * (1.0f / CH)), 0.f);
  const float rs = GA * __builtin_amdgcn_rsqf(var + 1e-5f);
  const float off = -mu * rs;
#pragma unroll
  for (int i = 0; i < 4; i++) f[i] = __builtin_elementwise_fma(f[i], f32x2{rs, rs}, f32x2{off, off});
  LDS_AS char* d = L + L_YR + ((cq >> 1) * 4 + q) * 16 + ((cq ^ (cq >> 1)) & 1) * 8;
  *(LDS_AS u32x2*)d = u32x2{pack_bf16(f[0].x, f[0].y), pack_bf16(f[1].x, f[1].y)};
  if (cq < 8) *(LDS_AS u32x2*)(d + 512) = u32x2{pack_bf16(f[2].x, f[2].y), pack_bf16(f[3].x, f[3].y)};
}

DI float sum_sq8(const u32* h) {
  float r;
  asm volatile("v_dot2_f32_bf16 %0, %1, %1, 0\n\tv_dot2_f32_bf16 %0, %2, %2, %0\n\tv_dot2_f32_bf16 %0, %3, %3, %0\n\tv_dot2_f32_bf16 %0, %4, %4, %0\n\t"
               "v_dot2_f32_bf16 %0, %5, %5, %0\n\tv_dot2_f32_bf16 %0, %6, %6, %0\n\tv_dot2_f32_bf16 %0, %7, %7, %0\n\tv_dot2_f32_bf16 %0, %8, %8, %0\n\ts_nop 2"
               : "=&v"(r) : "v"(h[0]), "v"(h[1]), "v"(h[2]), "v"(h[3]), "v"(h[4]), "v"(h[5]), "v"(h[6]), "v"(h[7]));
  return r;
}
DI float add1(float a, float b) { float r; asm("v_add_f32 %0, %1, %2" : "=v"(r) : "v"(a), "v"(b)); return r; }

__global__ void __launch_bounds__(NT, 3)
slak_block_kernel(gcu16*, gcu16*, gcu16*, gcu16*, gcu16*, gcu16*, gu16*, gu64*) {
  u64x8 karg;
  asm volatile("s_load_dwordx16 %0, %1, 0x0" : "=s"(karg) : "s"(__builtin_amdgcn_kernarg_segment_ptr()));
  LDS_AS char* const L = (LDS_AS char*)0u;
  const int t = threadIdx.x, lane = t & 63, bid = blockIdx.x;
  const int w = __builtin_amdgcn_readfirstlane(t >> 6);
  u32 warm = 0;
  if (t < CODE_LINES / 4)
    warm = *(const __attribute__((address_space(1))) u32*)((__builtin_amdgcn_s_getpc() & ~255ull) + (((bid >> 3) & 3) + 4 * t) * 128);
  SB();
  const int xcd = bid & 7, idx = bid >> 3;
  const int y0 = 14 * ((xcd >> 1) * 4 + (idx >> 3)), x0 = 14 * ((xcd & 1) * 8 + (idx & 7));

  const int cg = w >> 1, xh = w & 1;
  const u32 tq = ((u32)t * 2731) >> 16, c4 = t - tq * 24;
  u32 dwo[2], xo[4], xdst[2];
  {
    const u32 r0 = 2 * (tq & 3) - (tq >> 2);
#pragma unroll
    for (int h = 0; h < 2; h++) dwo[h] = r0 + h < 7u ? (r0 + h) * (7 * CH * 2) + c4 * 8 : 0x80000000u;
  }
  auto item_offsets = [&](int it) {
    const u32 item = t + it * NT, P = (item * 5462) >> 16, o = item - P * 12, Y = (P * 6554) >> 16, X = P - Y * 10;
    const u32 gy = y0 - 3 + 2 * Y, gx = (it == 0 || item < 1200) ? x0 - 3 + 2 * X : 1024u;
    u32 base = (u32)(__mul24((int)gy, IMG * CH * 2) + __mul24((int)gx, CH * 2)) + o * 16;
    PIN(base);
    const u32 b0 = gx < (u32)IMG ? base : 0x80000000u, b1 = gx + 1 < (u32)IMG ? base + CH * 2 : 0x80000000u;
    xdst[it] = L_X + ((Y >> 1) * 10 + X) * X_REC + o * 16 + (Y & 1) * 8;
#pragma unroll
    for (int k = 0; k < 4; k++) xo[k] = gy + (k >> 1) < (u32)IMG ? ((k & 1) ? b1 : b0) + (k >> 1) * (IMG * CH * 2) : 0x80000000u;
  };
  item_offsets(0);
  u32 pfo = (idx * 18 + lane) * 128;
  asm volatile("" : "+v"(dwo[0]), "+v"(dwo[1]), "+v"(xo[0]), "+v"(xo[1]), "+v"(xo[2]), "+v"(xo[3]), "+v"(pfo));
  SB();
  asm volatile("s_waitcnt lgkmcnt(0)" : "+s"(karg));
  gcu16* const x = (gcu16*)karg[0]; gcu16* const dw = (gcu16*)karg[1]; gcu16* const w1 = (gcu16*)karg[2]; gcu16* const w2 = (gcu16*)karg[3];
  gcu16* const grn_g = (gcu16*)karg[4]; gcu16* const grn_b = (gcu16*)karg[5]; gu16* const out = (gu16*)karg[6]; gu64* const ws = (gu64*)karg[7];
  const rsrc_t xrs = make_rsrc(x, X_BYTES);
  u32x2 dv[2][7];
  if (w < 3) {
    const rsrc_t drs = make_rsrc(dw, 49 * CH * 2);
#pragma unroll
    for (int i = 0; i < 14; i++) dv[i / 7][i % 7] = __builtin_amdgcn_raw_buffer_load_b64(drs, dwo[i / 7] + (i % 7) * (CH * 2), 0, 0);
  }
  u32x4 xin[2][4];
  auto load_item = [&](int it) {
#pragma unroll
    for (int k = 0; k < 4; k++) xin[it][k] = ld128(xrs, xo[k]);
  };
  auto store_item = [&](int it) {
#pragma unroll
    for (int e = 0; e < 8; e++) {
      const u32x4* v = xin[it];
      *(volatile LDS_AS u32x2*)(L + xdst[it] + e * 192) = (e & 1) ? u32x2{perm_hi(v[0][e >> 1], v[1][e >> 1]), perm_hi(v[2][e >> 1], v[3][e >> 1])}
                                                                  : u32x2{perm_lo(v[0][e >> 1], v[1][e >> 1]), perm_lo(v[2][e >> 1], v[3][e >> 1])};
    }
  };
  load_item(0);
  if (w == 1 && lane < 36) warm = *(const __attribute__((address_space(1))) u32*)((gchar*)(lane < 18 ? w1 : w2 - 18 * 64) + pfo);
  SB();
  if (w < 7) item_offsets(1);
  const u32x4 w1rs = {(u32)(u64)w1, (u32)((u64)w1 >> 32) & 0xffffu, CH * HID * 2, 0x00020000};
  const u32 w1d = (w / 6) * 3 * 6144 + (w % 6) * 1024;
  const u32 k1 = 16 * (w % 6) + (lane >> 2);
  u32 w1o = __umul24(pos_channel(k1 ^ (((k1 >> 3) & 1) << 2)), HID * 2) + (w / 6) * 384 + (lane & 3) * 16;
  u32 tp = L_T + (16 * cg + (cg >= 3 ? 4 : 0) + (lane >> 2) + 100 * (lane & 3)) * T_ROW;
  u32 xb = L_X + (4 * xh + (lane & 3)) * X_REC + (16 * cg + (lane >> 2)) * 16, xb2 = xb + 40 * X_REC;
  u32 vb = 4 * xh + (lane & 3) < 7 ? L_V + (16 * cg + (lane >> 2)) * V_ROW + (4 * xh + (lane & 3)) * 8 : L_HREM + lane * 8;
  asm volatile("" : "+v"(w1o), "+v"(tp), "+v"(xb), "+v"(xb2), "+v"(vb));
  const int T = w >> 1, Hh = w & 1, g4 = lane >> 4, q4 = (lane >> 2) & 3, p4 = lane & 3;
  enum { A_LN0, A_LN1, A_YD, A_DIAG, A_WB, A_WE, A_Y, A_SS, A_SW, A_SR0, A_SR1, A_WR, A_YR, A_W2V, A_OOFF, A_HR, A_N };
  u32 adr[A_N];
  auto make_adr = [&](int i) {
    u32 v;
    switch (i) {
      case A_LN0: v = L_V + (8 * g4 + 4 * (g4 & 1) + q4) * V_ROW + (4 * w + p4) * 8; break;
      case A_LN1: v = (g4 & 1) ? adr[A_LN0] - 4 * V_ROW : adr[A_LN0] + 4 * V_ROW; break;
      case A_YD: v = L_Y + T * 6144 + (g4 * 32 + 16 * Hh + (lane & 15)) * 16; break;
      case A_DIAG: v = ((lane & 15) + (lane & 12) * 4) * 4; break;
      case A_WB: v = (8 * (g4 >> 1) + q4) * 64 + (g4 & 1) * 32 + p4 * 8 + Hh * 3 * 6144; break;
      case A_WE: v = adr[A_WB] + L_W1E; break;
      case A_Y: v = L_Y + T * 6144 + (lane ^ (16 * Hh)) * 16; break;
      case A_SS: v = L_SS + w * SS_W + (lane & 31) * 72 + (lane >> 5) * 8; break;
      case A_SW: v = L_SCR + w * 1024 + half_tile_put(lane); break;
      case A_SR0: v = L_SCR + w * 1024 + half_tile_get(lane); break;
      case A_SR1: v = (adr[A_SR0] ^ 16) + 256; break;
      case A_WR: v = (T < 3 ? L_W1E : L_W1L) + (Hh * 3 + T % 3) * 6144 + (8 * g4 + q4) * 64 + p4 * 8; break;
      case A_YR: v = L_YR + (g4 * 4 + p4) * 16; break;
      case A_W2V: v = (32 * w + 4 * ((lane >> 2) & 7) + (lane >> 5)) * (CH * 2) + p4 * 16; break;
      case A_OOFF: {
        const u32 pi = 4 * w + q4, py = (pi * 37) >> 8;
        v = (u32)__mul24((y0 + 2 * py + (p4 >> 1)) * IMG + x0 + 2 * (pi - 7 * py) + (p4 & 1), CH * 2) + g4 * 16;
      } break;
      default: v = L_HREM + (Hh * 24 + T * 4 + (lane >> 3)) * 64 + (lane & 7) * 2; break;
    }
    PIN(v);
    adr[i] = v;
  };
#pragma unroll
  for (int i = 0; i < A_N; i++) make_adr(i);
  SB();
  if (w < 3) {
#pragma unroll
    for (int e = 0; e < 4; e++) {
      u32 q[2][2][4];
#pragma unroll
      for (int h = 0; h < 2; h++) {
        u32 v[7];
#pragma unroll
        for (int j = 0; j < 7; j++) v[j] = dv[h][j][e >> 1];
#pragma unroll
        for (int sx = 0; sx < 3; sx++) {
          q[0][h][sx] = (e & 1) ? perm_hi(v[2 * sx], v[2 * sx + 1]) : perm_lo(v[2 * sx], v[2 * sx + 1]);
          q[1][h][sx + 1] = (e & 1) ? perm_hi(v[2 * sx + 1], v[2 * sx + 2]) : perm_lo(v[2 * sx + 1], v[2 * sx + 2]);
        }
        q[0][h][3] = (e & 1) ? v[6] >> 16 : v[6] & 0xffffu;
        q[1][h][0] = (e & 1) ? v[0] & 0xffff0000u : v[0] << 16;
      }
      LDS_AS char* d = L + L_T + (52 * (c4 & 1) + 12 * e + (c4 >> 1) + 200 * (tq >> 2)) * T_ROW + (tq & 3) * 32;
#pragma unroll
      for (int ox = 0; ox < 2; ox++) {
        *(LDS_AS u32x4*)(d + ox * 100 * T_ROW) = u32x4{q[ox][0][0], q[ox][1][0], q[ox][0][1], q[ox][1][1]};
        *(LDS_AS u32x4*)(d + ox * 100 * T_ROW + 16) = u32x4{q[ox][0][2], q[ox][1][2], q[ox][0][3], q[ox][1][3]};
      }
    }
  }
  if (t == NT - 1) *(LDS_AS u32x4*)(L + L_CNT) = u32x4{0, 0, 0, 0};
  asm volatile("" : "+v"(xin[0][0]), "+v"(xin[0][1]), "+v"(xin[0][2]), "+v"(xin[0][3]));
  if (w < 7) load_item(1);
  SB();
  store_item(0);
  bar_lds();

  u64 epoch;
  {
    s16x8 wq[8], d[4][5];
    auto load_x = [&](int sx, int yp) {
      d[sx][yp] = yp < 4 ? *(volatile LDS_AS s16x8*)(L + xb + (yp * 10 + sx) * X_REC) : *(volatile LDS_AS s16x8*)(L + xb2 + sx * X_REC);
    };
    auto load_w = [&](int i) { wq[i] = *(volatile LDS_AS s16x8*)(L + tp + i * 16); };
    auto half = [](s16x8 v, int h) { return h ? __builtin_shufflevector(v, v, 4, 5, 6, 7) : __builtin_shufflevector(v, v, 0, 1, 2, 3); };
    auto conv_mfma = [&](f32x4& acc, int sx, int sy, int Yo) {
      acc = __builtin_amdgcn_mfma_f32_4x4x4bf16_1k(half(wq[2 * sy + (sx >> 1)], sx & 1), half(d[sx][(Yo + sy) >> 1], (Yo + sy) & 1), acc, 0, 0, 0);
    };
    auto put_row = [&](const f32x4& r, int Yo) { *(LDS_AS u32x2*)(L + vb + Yo * 56) = u32x2{pack_bf16(r[0], r[1]), pack_bf16(r[2], r[3])}; };
    load_w(0); load_x(0, 0); load_x(0, 1); load_w(2); load_w(4); load_x(0, 2); load_w(6);
    f32x4 acc[7];
#pragma unroll
    for (int i = 0; i < 7; i++) acc[i] = f32x4{0.f, 0.f, 0.f, 0.f};
    SB();
    static_for<16>([&](auto ic) {
      constexpr int i = ic.value, sx = i >> 2, sy = i & 3;
#pragma unroll
      for (int Yo = 0; Yo + sy < 6; Yo++) conv_mfma(acc[Yo], sx, sy, Yo);
      if constexpr (i < 9) load_x(1 + i / 3, i % 3);
      if constexpr (i < 4) load_w(2 * i + 1);
      if constexpr (i == 8) { if (w >= 7) arrive(C_STAGED); }
      SB();
    });
    if (w < 7) {
      if (t < 432) store_item(1);
      arrive(C_STAGED);
    }
    await(C_STAGED);
#pragma unroll
    for (int i = 0; i < 8; i++) load_x(i >> 1, 3 + (i & 1));
    epoch = ws_load(ws, WS_EPOCH);
#pragma unroll
    for (int Yo = 0; Yo < 3; Yo++) put_row(acc[Yo], Yo);
    SB();
    static_for<40>([&](auto ic) {
      constexpr int i = ic.value, sx = i / 10, j = i % 10;
      constexpr int yo[10] = {6, 5, 4, 6, 3, 5, 6, 4, 5, 6}, sy[10] = {0, 1, 2, 1, 3, 2, 2, 3, 3, 3};
      conv_mfma(acc[yo[j]], sx, sy[j], yo[j]);
      if constexpr (i == 4 || i == 12 || i == 20) dma16(w1rs, w1o, (i / 8) * 64, L_W1E + w1d + (i / 8) * 6144);
      if constexpr (i == 37) put_row(acc[3], 3);
      SB();
    });
#pragma unroll
    for (int Yo = 4; Yo < 7; Yo++) put_row(acc[Yo], Yo);
  }
  bar_lds();

  const bool bias_wave = w == 4 && bid < CH;
  u32 bw[6], bb[6];
  if (bias_wave) {
#pragma unroll
    for (int i = 0; i < 6; i++) {
      bw[i] = __builtin_amdgcn_raw_buffer_load_b16(make_rsrc(w2, HID * CH * 2), lane * (CH * 2), bid * 2 + i * (64 * CH * 2), 0);
      bb[i] = __builtin_amdgcn_raw_buffer_load_b16(make_rsrc(grn_b, HID * 2), lane * 2 + i * 128, 0, 0);
    }
  }
  SB();
  layer_norm_unit(L, adr[A_LN0], adr[A_LN1], adr[A_YD], adr[A_DIAG]);
  if (w == LN_LAST) layer_norm_last(L, lane);
  asm volatile("s_waitcnt vmcnt(0)" :: "v"(warm) : "memory");
  epoch = ((u64)(u32)__builtin_amdgcn_readfirstlane((int)(epoch >> 32)) << 32) | (u32)__builtin_amdgcn_readfirstlane((int)epoch);
  const u64 tag = (epoch + 1) & ((1ull << 40) - 1);
  float bias_val = 0.f;
  if (bias_wave) {
    float a = 0.f;
#pragma unroll
    for (int i = 0; i < 6; i++) { asm volatile("" : "+v"(bw[i]), "+v"(bb[i])); a = __builtin_fmaf(bf_lo(bw[i]), bf_lo(bb[i]), a); }
    bias_val = __int_as_float(__builtin_amdgcn_readfirstlane(__float_as_int(wave_sum(a))));
  }
  bar_lds();

  s16x8 hk[6], hs[6];
  const int rq = T / 3, otj[3] = {T % 3, (T + 1) % 3, (T + 2) % 3};
  const u32 totw = WS_TOT + (bid & 7) * TOT_ROW;
  const u32 ooff = adr[A_OOFF];
  u32x4 xr[3];
  u64 bgr;
  auto flip_own = [&](int s) {
    *(volatile LDS_AS s16x8*)(L + adr[A_SW]) = hk[s];
    hk[s] = __builtin_shufflevector(tr_read(L + adr[A_SR0]), tr_read(L + adr[A_SR1]), 0, 1, 2, 3, 4, 5, 6, 7);
  };
  const f32x16 zero16 = {0.f, 0.f, 0.f, 0.f, 0.f, 0.f, 0.f, 0.f, 0.f, 0.f, 0.f, 0.f, 0.f, 0.f, 0.f, 0.f};
  {
    LDS_AS char* wE = L + adr[A_WE];
    LDS_AS char* wL = L + adr[A_WB] + L_W1L;
    LDS_AS char* ss = L + adr[A_SS];
    LDS_AS char* wr = L + adr[A_WR];
    LDS_AS char* yr = L + adr[A_YR];
    s16x8 yf[6], wf[6], ry[3], rw[3];
    f32x4 racc[2] = {{0.f, 0.f, 0.f, 0.f}, {0.f, 0.f, 0.f, 0.f}};
    float ssv[6];
    f32x16 acc = zero16;
    __builtin_amdgcn_s_setprio(3);
#pragma unroll
    for (int k = 0; k < 6; k++) { yf[k] = *(LDS_AS s16x8*)(L + adr[A_Y] + k * 1024); wf[k] = tr_frag(wE + k * 1024, 256); }
    SB();
#pragma unroll
    for (int u = 0; u < 3; u++) dma16(w1rs, w1o, 192 + u * 64, L_W1L + w1d + u * 6144);
    SB();
#pragma unroll
    for (int k = 0; k < 6; k++) {
      acc = mfma32(yf[k], wf[k], acc);
      wf[k] = tr_frag(wE + 6144 + k * 1024, 256);
      SB();
    }
    const u32 w2raw = wave_slot(w);
    auto fetch_w2 = [&](int c) {
      await(c);
      const u32x4 rs = {(u32)(u64)w2, (u32)((u64)w2 >> 32) & 0xffffu, HID * CH * 2, 0x00020000};
#pragma unroll
      for (int j = 0; j < 6; j++) dma16(rs, adr[A_W2V], (j & 1) * 384 + (j >> 1) * 64, w2raw + j * 1024);
    };
    asm volatile("s_waitcnt vmcnt(0)" ::: "memory");
    arrive(C_YFREE);
    static_for<6>([&](auto sc_) {
      constexpr int s = sc_.value, NP = s == 3 ? 10 : 8;
      LDS_AS char* wp2 = (s + 2 < 3 ? wE : wL) + ((s + 2) % 3) * 6144;
      __builtin_amdgcn_s_setprio((G1_PRIO >> (2 * s)) & 3);
      if constexpr (s == 0) { if (bias_wave && lane < 8) ws_store(ws, WS_TOT + lane * TOT_ROW + TOT_BIAS + bid, granule(tag, bias_val)); }
      if constexpr (s == 3) arrive(C_W1EFREE);
      if constexpr (s == 1) { if (w < 6) fetch_w2(C_YFREE); else await(C_YFREE); }
      if constexpr (s == 4) { if (w >= 6) fetch_w2(C_W1EFREE); }
      if constexpr (s == 1 || s == 2) {
#pragma unroll
        for (int j = 0; j < 3; j++) {
          if (s == 1) ry[j] = *(LDS_AS s16x8*)(yr + j * 256);
          rw[j] = tr_frag(wr + j * 2048 + (s - 1) * 32, 256);
        }
      }
      f32x2 xv[NP], z[NP];
#pragma unroll
      for (int i = 0; i < NP; i++) {
        if (i < 8) xv[i] = f32x2{acc[2 * (i & 7)], acc[2 * (i & 7) + 1]};
        else xv[i] = __builtin_amdgcn_inverse_ballot_w64(0xffffull) ? f32x2{racc[0][2 * (i & 1)], racc[0][2 * (i & 1) + 1]} : f32x2{racc[1][2 * (i & 1)], racc[1][2 * (i & 1) + 1]};
        z[i] = __builtin_elementwise_fma(xv[i], xv[i], f32x2{GK, GK});
      }
#pragma unroll
      for (int i = 0; i < NP; i++) PIN(z[i]);
      SB();
#pragma unroll
      for (int i = 0; i < NP; i++) z[i] *= xv[i];
#pragma unroll
      for (int i = 0; i < NP; i++) PIN(z[i]);
      SB();
      f32x16 nxt = zero16;
      float e[2 * NP];
#pragma unroll
      for (int i = 0; i < 2 * NP; i++) {
        constexpr bool rem = s == 1 || s == 2;
        if (s < 5 && i % 3 == 0 && i < 9) { nxt = mfma32(yf[i / 3], wf[i / 3], nxt); PIN(nxt); SB(); }
        if (rem && (i == 12 || i == 13)) { racc[(s - 1) & 1] = mfma16(ry[i - 12], rw[i - 12], racc[(s - 1) & 1]); PIN(racc[(s - 1) & 1]); SB(); }
        if (s == 5 && i % 3 == 0 && i < 9) { xr[i / 3] = __builtin_amdgcn_raw_buffer_load_b128(xrs, ooff, otj[i / 3] * 64, 0); SB(); }
        if (s == 5 && i == 9) { bgr = ws_load(ws + (totw + TOT_BIAS + (w & 1) * 64), lane); SB(); }
        e[i] = __builtin_amdgcn_exp2f(-z[i >> 1][i & 1]);
        PIN(e[i]);
      }
      SB();
      if constexpr (s < 4) {
#pragma unroll
        for (int k = 0; k < 3; k++) wf[k] = tr_frag(wp2 + k * 1024, 256);
      }
#pragma unroll
      for (int i = 0; i < NP; i++) z[i] = f32x2{e[2 * i], e[2 * i + 1]} + 1.0f;
#pragma unroll
      for (int i = 0; i < NP; i++) PIN(z[i]);
      SB();
      float rc[2 * NP];
#pragma unroll
      for (int i = 0; i < 2 * NP; i++) {
        if (s < 5 && i % 3 == 0 && i < 9) { nxt = mfma32(yf[3 + i / 3], wf[3 + i / 3], nxt); PIN(nxt); SB(); }
        if ((s == 1 || s == 2) && i == 9) { racc[(s - 1) & 1] = mfma16(ry[2], rw[2], racc[(s - 1) & 1]); PIN(racc[(s - 1) & 1]); SB(); }
        rc[i] = __builtin_amdgcn_rcpf(z[i >> 1][i & 1]);
        PIN(rc[i]);
      }
      SB();
      if constexpr (s < 4) {
#pragma unroll
        for (int k = 3; k < 6; k++) wf[k] = tr_frag(wp2 + k * 1024, 256);
      }
#pragma unroll
      for (int i = 0; i < NP; i++) xv[i] *= f32x2{rc[2 * i], rc[2 * i + 1]};
#pragma unroll
      for (int i = 0; i < NP; i++) PIN(xv[i]);
      SB();
      u32 hp8[8];
#pragma unroll
      for (int i = 0; i < 8; i++) { hp8[i] = pack_bf16(xv[i].x, xv[i].y); PIN(hp8[i]); }
      SB();
      hk[s] = __builtin_bit_cast(s16x8, u32x4{hp8[0], hp8[1], hp8[2], hp8[3]});
      if constexpr (s < K_INLOOP) flip_own(s);
      SB();
      ssv[s] = sum_sq8(hp8);
      hs[s] = __builtin_bit_cast(s16x8, u32x4{hp8[4], hp8[5], hp8[6], hp8[7]});
      if constexpr (s == 3) {
        const f32x2 sq = __builtin_elementwise_fma(xv[8], xv[8], xv[9] * xv[9]);
        if (lane < 32) {
          const u32 p01 = pack_bf16(xv[8].x, xv[8].y), p23 = pack_bf16(xv[9].x, xv[9].y);
          LDS_AS unsigned short* hr = (LDS_AS unsigned short*)(L + adr[A_HR]);
          hr[0] = (unsigned short)p01; hr[8] = (unsigned short)(p01 >> 16); hr[16] = (unsigned short)p23; hr[24] = (unsigned short)(p23 >> 16);
          *(LDS_AS float*)(ss + 64) = sq.x + sq.y;
        }
      }
      acc = nxt;
      SB();
    });
#pragma unroll
    for (int i = 0; i < 3; i++) *(volatile LDS_AS f32x2*)(ss + i * 16) = f32x2{ssv[2 * i], ssv[2 * i + 1]};
  }
  asm volatile("" : "+v"(xr[0]), "+v"(xr[1]), "+v"(xr[2]), "+v"(bgr));
  bar_all();

  if (t < HID) {
    float v[13];
    LDS_AS char* q = L + L_SS + (w / 3) * SS_W + (lane & 31) * 72;
#pragma unroll
    for (int i = 0; i < 12; i++) v[i] = *(LDS_AS float*)(q + (w % 3) * 16 + (lane >> 5) * 4 + (i >> 1) * 2 * SS_W + (i & 1) * 8);
    v[12] = *(LDS_AS float*)(q + (2 * (w % 3) + (lane >> 5)) * 2 * SS_W + 64);
    const float s = (((v[0] + v[1]) + (v[2] + v[3])) + ((v[4] + v[5]) + (v[6] + v[7]))) + (((v[8] + v[9]) + (v[10] + v[11])) + v[12]);
    ws_store(ws, WS_PART + (((u32)t >> 4) * 256 + bid) * 16 + (t & 15), granule(tag, s));
  }
  u32x2 w2r[12];
  u32 ap[4], ab[2], hp, rw, w2d0, w2d1;
  f32x2 w2f[24];
  auto unpack_w2 = [&](int i) {
    const int ot = i >> 3, r = (i >> 1) & 3, kp = i & 1;
    const u32 a = w2r[4 * ot + 2 * kp][r >> 1], b = w2r[4 * ot + 2 * kp + 1][r >> 1];
    w2f[i] = (r & 1) ? f32x2{bf_hi(a), bf_hi(b)} : f32x2{bf_lo(a), bf_lo(b)};
    PIN(w2f[i]);
  };
  auto swap_out = [&]() {
    SB();
#pragma unroll
    for (int i = 0; i < 12; i++) w2r[i] = *(volatile LDS_AS u32x2*)(L + wave_slot(w) + lane * 8 + i * 512);
#pragma unroll
    for (int i = 0; i < 6; i++) *(volatile LDS_AS s16x8*)(L + (adr[A_SW] - (L_SCR + w * 1024) + wave_slot(w)) + i * 1024) = hs[i];
    if (t == 512) *(LDS_AS u32*)(L + L_FL) = 0;
    SB();
    const u32 w2d = L_W2 + ((t & 1) * 12 + w) * 1024 + (16 * g4 + ((t >> 1) & 3)) * 16 + (t & 8);
    const u32 aslot = L_W2 + (16 * g4 + q4) * 16;
    ab[0] = aslot + p4 * 64; ab[1] = aslot + (p4 ^ 1) * 64 + 12288;
#pragma unroll
    for (int i = 0; i < 4; i++) ap[i] = (((i >> 1) ^ rq) ? ab[1] : ab[0]) + (otj[0] * 24576 + ((i & 1) ^ Hh) * 6144);
    hp = L_HREM + (Hh * 24 + g4) * 64 + p4 * 16;
    rw = L_RP + (Hh * 4 * CH + 32 * otj[0] + 4 * rq) * 4 + p4 * (CH * 4) + g4 * 32;
    w2d0 = w2d + (t & 1) * 64; w2d1 = w2d + ((t & 1) ^ 1) * 64;
    asm volatile("" : "+v"(ap[0]), "+v"(ap[1]), "+v"(ap[2]), "+v"(ap[3]), "+v"(ab[0]), "+v"(ab[1]), "+v"(hp), "+v"(rw), "+v"(w2d0), "+v"(w2d1));
    SB();
  };
  {
    const bool mine = (u32)(t - HID) < CH;
    if (!__all(!mine || (bgr >> 24) == tag)) {
#pragma unroll 1
      for (int spin = 0; spin < SPIN_CAP; spin++) {
        bgr = ws_load(ws + (totw + TOT_BIAS + (w & 1) * 64), lane);
        if (__all(!mine || (bgr >> 24) == tag)) break;
      }
    }
    if (mine) *(LDS_AS float*)(L + L_BP + (t - HID) * 4) = granule_val(bgr);
  }
  if (w == CLAIM_W) {
    u32 co = WS_CLAIM + idx * 16;
    PIN(co);
    u64 old = epoch + 1;
    if (idx < 24 && lane == 0) old = __hip_atomic_exchange(ws + co, epoch + 1, RLX_AGENT);
    swap_out();
    if (lane == 0) *(LDS_AS u32*)(L + L_ROLE) = old != epoch + 1 ? idx : 255;
  } else {
    swap_out();
  }
  bar_lds();
  const u32 role = __builtin_amdgcn_readfirstlane(*(volatile LDS_AS u32*)(L + L_ROLE));
  if (role < 24) {
    u64 gr[6];
    const u32 partw = WS_PART + role * 4096 + t;
#pragma unroll
    for (int i = 0; i < 6; i++) gr[i] = ws_load(ws, partw + (i < 5 || t < 256 ? i * NT : 0));
#pragma unroll 1
    for (int spin = 0; spin < SPIN_CAP; spin++) {
      bool ok = true;
#pragma unroll
      for (int i = 0; i < 6; i++) ok &= (gr[i] >> 24) == tag;
      if (__all(ok)) break;
#pragma unroll
      for (int i = 0; i < 6; i++)
        if ((gr[i] >> 24) != tag) gr[i] = ws_load(ws, partw + (i < 5 || t < 256 ? i * NT : 0));
    }
    float s = ((granule_val(gr[0]) + granule_val(gr[1])) + (granule_val(gr[2]) + granule_val(gr[3]))) + (granule_val(gr[4]) + (t < 256 ? granule_val(gr[5]) : 0.f));
    s = sum_xor32(sum_xor16(s));
    if (lane < 16) *(LDS_AS float*)(L + L_RED + (lane * 12 + w) * 4) = s;
    bar_lds();
    if (w < 2) {
      LDS_AS char* q = L + L_RED + (t & 15) * 48;
      const f32x4 a = *(LDS_AS f32x4*)(q) + *(LDS_AS f32x4*)(q + 16) + *(LDS_AS f32x4*)(q + 32);
      const float gx = __builtin_amdgcn_sqrtf(__builtin_fmaf((a[0] + a[1]) + (a[2] + a[3]), GA_INV2, 1e-6f));
      ws_store(ws, WS_TOT + (t >> 4) * TOT_ROW + role * 16 + (t & 15), granule(tag, gx));
    }
  }
  SB();
#pragma unroll
  for (int i = 0; i < 6; i++)
    hs[i] = __builtin_shufflevector(tr_read(L + (adr[A_SR0] - (L_SCR + w * 1024) + wave_slot(w ^ 1)) + i * 1024),
                                    tr_read(L + (adr[A_SR1] - (L_SCR + w * 1024) + wave_slot(w ^ 1)) + i * 1024), 0, 1, 2, 3, 4, 5, 6, 7);
#pragma unroll
  for (int i = K_INLOOP; i < 6; i++) flip_own(i);
  const u32 remo = lane < 48 ? (u32)(((y0 + 12) * IMG + x0 + 12) * (CH * 2)) + (lane >= 24 ? IMG * CH * 2 - 384 : 0) + lane * 16 : 0x80000000u;
  u32x4 xrem = ld128(xrs, remo);
  u32 gam = grn_g[t < HID ? t : 0];
  f32x4 acc2[3][2];
#pragma unroll
  for (int i = 0; i < 6; i++) acc2[i >> 1][i & 1] = *(LDS_AS f32x4*)(L + L_BP + g4 * 32 + otj[i >> 1] * 128 + ((i & 1) ^ (i < 2 ? rq : 0)) * 16);
  SB();
#pragma unroll
  for (int i = 0; i < 6; i++) {
    const u32x4 xv = i < 2 && rq ? u32x4{xr[0][2], xr[0][3], xr[0][0], xr[0][1]} : xr[i >> 1];
    acc2[i >> 1][i & 1] += f32x4{bf_lo(xv[2 * (i & 1)]), bf_hi(xv[2 * (i & 1)]), bf_lo(xv[2 * (i & 1) + 1]), bf_hi(xv[2 * (i & 1) + 1])};
    asm volatile("" : "+v"(acc2[i >> 1][i & 1]));
  }
  SB();
  const f32x4 zero4 = {0.f, 0.f, 0.f, 0.f};
#pragma unroll
  for (int i = 0; i < 24; i++) unpack_w2(i);
#pragma unroll
  for (int i = 0; i < 6; i++) asm volatile("" : "+v"(hk[i]), "+v"(hs[i]));
  if (t >= HID) PIN(xrem);

  if (t < HID) {
    u64 g;
#pragma unroll 1
    for (int spin = 0; spin < SPIN_CAP; spin++) {
      g = ws_load(ws, totw + t);
      if (__all((g >> 24) == tag)) break;
    }
    PIN(xrem);
    const float gx = granule_val(g);
    *(LDS_AS float*)(L + L_GG + t * 4) = bf_lo(gam) * gx;
    const float s = wave_sum(gx);
    if (lane == 0) *(LDS_AS float*)(L + L_PS + w * 4) = s;
  }
  bar_lds();
  {
    const f32x4 pa = *(LDS_AS f32x4*)(L + L_PS);
    const f32x2 pb = *(LDS_AS f32x2*)(L + L_PS + 16);
    const f32x4 gg = *(LDS_AS f32x4*)(L + L_GG + (t >> 3) * 16);
    if (bid == 0 && t == NT - 1) ws_store(ws, WS_EPOCH, epoch + 1);
    const float mean = (((pa[0] + pa[1]) + (pa[2] + pa[3])) + (pb[0] + pb[1])) * (1.0f / HID);
    const float inv = GA_INV * __builtin_amdgcn_rcpf(mean + 1e-6f);
    const f32x2 s01 = f32x2{gg[0], gg[1]} * inv + GA_INV, s23 = f32x2{gg[2], gg[3]} * inv + GA_INV;
#pragma unroll
    for (int i = 0; i < 12; i++) {
      const f32x2 a = w2f[2 * i] * s01, b = w2f[2 * i + 1] * s23;
      *(volatile LDS_AS u32x2*)(L + (i & 1 ? w2d1 : w2d0) + ((i >> 1) & 1) * 128 + (i >> 2) * 24576) = u32x2{pack_bf16(a.x, a.y), pack_bf16(b.x, b.y)};
    }
  }

  {
    const rsrc_t ors = make_rsrc(out, X_BYTES);
    constexpr int PF = 3;
    s16x8 af[72], rb[6];
    f32x4 ra = zero4;
    u32 rem_cnt = 0;
    auto load_a = [&](int i) {
      if (i % 24 == 0 && i) {
#pragma unroll
        for (int j = 0; j < 4; j++) ap[j] = ab[j >> 1] + (otj[i / 24] * 24576 + ((j & 1) ^ Hh) * 6144);
      }
      af[i] = *(volatile LDS_AS s16x8*)(L + ap[(i / 6) & 3] + (i % 6) * 1024);
    };
    auto store_tile = [&](int j) {
      const u32x2 a = {pack_bf16(acc2[j][0][0], acc2[j][0][1]), pack_bf16(acc2[j][0][2], acc2[j][0][3])};
      const u32x2 b = {pack_bf16(acc2[j][1][0], acc2[j][1][1]), pack_bf16(acc2[j][1][2], acc2[j][1][3])};
      __builtin_amdgcn_raw_buffer_store_b128(j == 0 && rq ? u32x4{b.x, b.y, a.x, a.y} : u32x4{a.x, a.y, b.x, b.y}, ors, ooff, otj[j] * 64, ST_AUX);
    };
    bar_lds();
    __builtin_amdgcn_s_setprio(3);
#pragma unroll
    for (int i = 0; i < PF; i++) load_a(i);
    SB();
    static_for<72>([&](auto ic) {
      constexpr int i = ic.value, ot = i / 24, rt = (i / 12) & 1, k = i % 12;
      acc2[ot][rt] = mfma16(af[i], k < 6 ? hk[k % 6] : hs[k % 6], acc2[ot][rt]);
      if constexpr (i + PF < 72) load_a(i + PF);
      if constexpr (i % 18 == 17 && i < 60) __builtin_amdgcn_s_setprio(2 - i / 18);
      if constexpr (i < 12 && (i & 1)) rb[i >> 1] = *(volatile LDS_AS s16x8*)(L + hp + (i >> 1) * 256);
      if constexpr (i >= 6 && i < 18 && (i & 1) == 0) ra = mfma16(af[(i - 6) >> 1], rb[(i - 6) >> 1], ra);
      if constexpr (i == 19) {
        if (q4 == 0) *(volatile LDS_AS f32x4*)(L + rw) = ra;
        u32 fl = L_FL;
        PIN(fl);
        if (lane == 0) rem_cnt = __hip_atomic_fetch_add((LDS_AS u32*)(L + fl), 1u, __ATOMIC_RELAXED, __HIP_MEMORY_SCOPE_WORKGROUP);
      }
      if constexpr (i == 25 || i == 49) store_tile(ot - 1);
      if constexpr (i == 40) {
        if (__builtin_amdgcn_readfirstlane(rem_cnt) == 11 && lane < 48) {
          f32x4 xq[2];
#pragma unroll
          for (int j = 0; j < 2; j++) {
            const f32x4 a = *(volatile LDS_AS f32x4*)(L + L_RP + (lane * 8 + j * 4) * 4), b = *(volatile LDS_AS f32x4*)(L + L_RP + (4 * CH + lane * 8 + j * 4) * 4);
            const f32x4 c = *(volatile LDS_AS f32x4*)(L + L_BP + (lane - 12 * ((lane * 43) >> 9)) * 32 + j * 16);
#pragma unroll
            for (int e = 0; e < 4; e++) xq[j][e] = add1(add1((e & 1) ? bf_hi(xrem[2 * j + (e >> 1)]) : bf_lo(xrem[2 * j + (e >> 1)]), c[e]), add1(a[e], b[e]));
          }
          __builtin_amdgcn_raw_buffer_store_b128(u32x4{pack_bf16(xq[0][0], xq[0][1]), pack_bf16(xq[0][2], xq[0][3]), pack_bf16(xq[1][0], xq[1][1]), pack_bf16(xq[1][2], xq[1][3])}, ors, remo, 0, ST_AUX);
        }
      }
      SB();
    });
    store_tile(2);
  }
}

extern "C" void kernel_launch(void* const* d_in, const int* in_sizes, int n_in,
                              void* d_out, int out_size, void* d_ws, size_t ws_size,
                              hipStream_t stream) {
  (void)in_sizes; (void)n_in; (void)out_size; (void)ws_size;
  (void)hipFuncSetAttribute((const void*)slak_block_kernel, hipFuncAttributeMaxDynamicSharedMemorySize, LDS_BYTES);
  slak_block_kernel<<<256, NT, LDS_BYTES, stream>>>(
      (gcu16*)d_in[0], (gcu16*)d_in[1], (gcu16*)d_in[2], (gcu16*)d_in[3], (gcu16*)d_in[4], (gcu16*)d_in[5], (gu16*)d_out, (gu64*)d_ws);
}
```
